```python
import torch

def _softmax(x, axis=-1):
    return torch.softmax(x, dim=axis)

def _gelu(x):
    return torch.nn.functional.gelu(x, approximate='tanh')

def _silu(x):
    return torch.nn.functional.silu(x)

def _sigmoid(x):
    return torch.sigmoid(x)

def _layernorm(x, axis=-1, eps=1e-05):
    m = x.mean(dim=axis, keepdim=True)
    v = x.var(dim=axis, keepdim=True, unbiased=False)
    return (x - m) / torch.sqrt(v + eps)

def _scatter_add(tgt, idx, src, axis=0):
    return tgt.clone().index_add_(axis, idx, src)

def _segment_sum(src, seg, n):
    out = torch.zeros((n,) + src.shape[1:], dtype=src.dtype)
    return out.index_add_(0, seg, src)

def _pad(x, pw):
    flat = tuple((v for pair in reversed(pw) for v in pair))
    return torch.nn.functional.pad(x, flat)
kernel_name = 'parallel_sum__linear_kernel_hedgehog__mixer_gmlp__prompt'

def setup_inputs():

    _w = lambda a: (lambda t: t.to(torch.bfloat16) if t.is_floating_point() else t)(torch.as_tensor(a))

    def _A_setup():
        B, S, D, H = (1, 2048, 448, 16)
        Dh = D // H
        return {'x': _w((lambda *s: torch.randn(*s, dtype=torch.bfloat16) * 0.05)(B, S, D)), 'wqkv': _w((lambda *s: torch.randn(*s, dtype=torch.bfloat16) * 0.8)(D, 3 * D)), 'w_phi': _w((lambda *s: torch.randn(*s, dtype=torch.bfloat16) * 0.8)(H, Dh, Dh) * Dh ** (-0.5)), 'wo': _w((lambda *s: torch.randn(*s, dtype=torch.bfloat16) * 0.1)(D, D))}
    _A_in = _A_setup()

    def _B_setup():
        B, S, D, F = (1, 2048, 448, 1792)
        Fh = F // 2
        return {'x': _w((lambda *s: torch.randn(*s, dtype=torch.bfloat16) * 0.05)(B, S, D)), 'w_in': _w((lambda *s: torch.randn(*s, dtype=torch.bfloat16) * 0.4)(D, 2 * Fh)), 'w_sp': _w(0.01 * (lambda *s: torch.randn(*s, dtype=torch.bfloat16) * 0.4)(S, S)), 'b_sp': _w(1.0 + 0.01 * (lambda *s: torch.randn(*s, dtype=torch.bfloat16) * 0.05)(S)), 'w_out': _w((lambda *s: torch.randn(*s, dtype=torch.bfloat16) * 0.05)(Fh, D))}
    _B_in = _B_setup()
    return {'A_x': _A_in['x'], 'A_wqkv': _A_in['wqkv'], 'A_w_phi': _A_in['w_phi'], 'A_wo': _A_in['wo'], 'B_x': _B_in['x'], 'B_w_in': _B_in['w_in'], 'B_w_sp': _B_in['w_sp'], 'B_b_sp': _B_in['b_sp'], 'B_w_out': _B_in['w_out']}

def reference(A_x, A_wqkv, A_w_phi, A_wo, B_x, B_w_in, B_w_sp, B_b_sp, B_w_out):

    def _A_ref(x, wqkv, w_phi, wo):
        B, S, D = x.shape
        H = 16
        Dh = D // H
        qkv = (lambda x, *d: x.permute(*d))((x @ wqkv).reshape(B, S, 3, H, Dh), 2, 0, 3, 1, 4)
        q, k, v = (qkv[0], qkv[1], qkv[2])
        pq = torch.einsum('bhsd,hdf->bhsf', q, w_phi)
        pk = torch.einsum('bhsd,hdf->bhsf', k, w_phi)
        phi_q = torch.concatenate([_softmax(pq, axis=-1), _softmax(-pq, axis=-1)], axis=-1)
        phi_k = torch.concatenate([_softmax(pk, axis=-1), _softmax(-pk, axis=-1)], axis=-1)
        kv = torch.einsum('bhsf,bhsd->bhfd', phi_k, v)
        z = torch.einsum('bhsf->bhf', phi_k)
        num = torch.einsum('bhqf,bhfd->bhqd', phi_q, kv)
        den = torch.einsum('bhqf,bhf->bhq', phi_q, z)[..., None] + 1e-06
        o = num / den
        return (lambda x, *d: x.permute(*d))(o, 0, 2, 1, 3).reshape(B, S, D) @ wo

    def _B_ref(x, w_in, w_sp, b_sp, w_out):
        B, S, D = x.shape
        F = w_in.shape[-1]
        Fh = F // 2
        z = _gelu(torch.einsum('bsd,df->bsf', x, w_in))
        z1 = z[..., :Fh].clone()
        z2 = _layernorm(z[..., Fh:], axis=-1)
        g = torch.einsum('ts,bsf->btf', w_sp, z2) + b_sp[None, :, None]
        return torch.einsum('bsf,fd->bsd', z1 * g, w_out)
    x = A_x
    xn = _layernorm(x, axis=-1)
    return x + _A_ref(xn, A_wqkv, A_w_phi, A_wo) + _B_ref(xn, B_w_in, B_w_sp, B_b_sp, B_w_out)
```

```cpp
#include <hip/hip_runtime.h>
#include <hip/hip_bf16.h>
#include <stdint.h>

typedef __attribute__((ext_vector_type(8))) short bf16x8;
typedef __attribute__((ext_vector_type(4))) short s16x4;
typedef __attribute__((ext_vector_type(4))) float f32x4;
typedef __attribute__((ext_vector_type(2))) float f32x2;
typedef __attribute__((ext_vector_type(4))) unsigned u32x4;
typedef __attribute__((ext_vector_type(2))) unsigned u32x2;
typedef __attribute__((ext_vector_type(2))) __bf16 bf2_t;
typedef unsigned short u16;
#define LDS_AS __attribute__((address_space(3)))

constexpr int S_ = 2048, D_ = 448, NQKV = 1344, NIN = 1792, FH = 896, KO = 1344;

__device__ __forceinline__ unsigned pk2(float a, float b) {
  f32x2 v = {a, b};
  return __builtin_bit_cast(unsigned, __builtin_convertvector(v, bf2_t));
}
__device__ __forceinline__ float lo_f(unsigned u) { return __uint_as_float(u << 16); }
__device__ __forceinline__ float hi_f(unsigned u) { return __uint_as_float(u & 0xffff0000u); }
__device__ __forceinline__ float rbf(float a) { return lo_f(pk2(a, 0.f)); }
__device__ __forceinline__ float bf2f(u16 v) { return __uint_as_float(((unsigned)v) << 16); }

__device__ __forceinline__ float x16_max(float v) {
  auto r = __builtin_amdgcn_permlane16_swap(__float_as_uint(v), __float_as_uint(v), false, false);
  return fmaxf(__uint_as_float(r[0]), __uint_as_float(r[1]));
}
__device__ __forceinline__ float x32_max(float v) {
  auto r = __builtin_amdgcn_permlane32_swap(__float_as_uint(v), __float_as_uint(v), false, false);
  return fmaxf(__uint_as_float(r[0]), __uint_as_float(r[1]));
}
__device__ __forceinline__ float x16_min(float v) {
  auto r = __builtin_amdgcn_permlane16_swap(__float_as_uint(v), __float_as_uint(v), false, false);
  return fminf(__uint_as_float(r[0]), __uint_as_float(r[1]));
}
__device__ __forceinline__ float x32_min(float v) {
  auto r = __builtin_amdgcn_permlane32_swap(__float_as_uint(v), __float_as_uint(v), false, false);
  return fminf(__uint_as_float(r[0]), __uint_as_float(r[1]));
}
__device__ __forceinline__ float x16_add(float v) {
  auto r = __builtin_amdgcn_permlane16_swap(__float_as_uint(v), __float_as_uint(v), false, false);
  return __uint_as_float(r[0]) + __uint_as_float(r[1]);
}
__device__ __forceinline__ float x32_add(float v) {
  auto r = __builtin_amdgcn_permlane32_swap(__float_as_uint(v), __float_as_uint(v), false, false);
  return __uint_as_float(r[0]) + __uint_as_float(r[1]);
}
__device__ __forceinline__ float red8_add(float v) {
  v += __uint_as_float(__builtin_amdgcn_mov_dpp(__float_as_uint(v), 0xB1, 0xf, 0xf, true));
  v += __uint_as_float(__builtin_amdgcn_mov_dpp(__float_as_uint(v), 0x4E, 0xf, 0xf, true));
  v += __uint_as_float(__builtin_amdgcn_mov_dpp(__float_as_uint(v), 0x141, 0xf, 0xf, true));
  return v;
}
__device__ __forceinline__ f32x4 mfma16(bf16x8 a, bf16x8 b, f32x4 c) {
  return __builtin_amdgcn_mfma_f32_16x16x32_bf16(a, b, c, 0, 0, 0);
}
__device__ __forceinline__ float gelu_t(float u) {
  float y = 0.7978845608028654f * (u + 0.044715f * u * u * u);
  float e = __expf(-2.f * y);
  return u * __builtin_amdgcn_rcpf(1.f + e);
}

constexpr size_t WS_PHIQ = 0;
constexpr size_t WS_Z1 = WS_PHIQ + (size_t)S_ * 896 * 2;
constexpr size_t WS_Z2T = WS_Z1 + (size_t)S_ * 896 * 2;
constexpr size_t WS_Z2N = WS_Z2T + (size_t)S_ * 896 * 2;
constexpr size_t WS_OH = WS_Z2N + (size_t)S_ * 896 * 2;
constexpr size_t WS_WT = WS_OH + (size_t)S_ * KO * 2;
constexpr size_t WS_KVP = WS_WT + (size_t)D_ * KO * 2;
constexpr size_t WS_STAT = WS_KVP + (size_t)16 * 16 * 56 * 32 * 4;
constexpr size_t WS_END = WS_STAT + (size_t)8 * S_ * 8;

constexpr int K1_XB = 128 * 896;
constexpr int K1_WSUB = 1056;
constexpr int K1_WB = 14 * K1_WSUB;
constexpr int K1_LDS = K1_XB + 2 * K1_WB;

__device__ __forceinline__ void phi_map(bf16x8 qf, const bf16x8* wp, int g, unsigned (&pp)[2][2], unsigned (&pm)[2][2]) {
  f32x4 z = {0.f, 0.f, 0.f, 0.f};
  f32x4 p0 = mfma16(wp[0], qf, z);
  f32x4 p1 = mfma16(wp[1], qf, z);
  float v[8];
#pragma unroll
  for (int j = 0; j < 4; ++j) { v[j] = rbf(p0[j]); v[4 + j] = rbf(p1[j]); }
  const bool valid1 = (g != 3);
  float mx = fmaxf(fmaxf(v[0], v[1]), fmaxf(v[2], v[3]));
  float mn = fminf(fminf(v[0], v[1]), fminf(v[2], v[3]));
  float mx1 = fmaxf(fmaxf(v[4], v[5]), fmaxf(v[6], v[7]));
  float mn1 = fminf(fminf(v[4], v[5]), fminf(v[6], v[7]));
  mx = valid1 ? fmaxf(mx, mx1) : mx;
  mn = valid1 ? fminf(mn, mn1) : mn;
  mx = x32_max(x16_max(mx));
  mn = x32_min(x16_min(mn));
  float ep[8], em[8];
  float sp = 0.f, sm = 0.f;
#pragma unroll
  for (int j = 0; j < 8; ++j) {
    ep[j] = __expf(v[j] - mx);
    em[j] = __expf(mn - v[j]);
    if (j >= 4) { ep[j] = valid1 ? ep[j] : 0.f; em[j] = valid1 ? em[j] : 0.f; }
    sp += ep[j];
    sm += em[j];
  }
  sp = x32_add(x16_add(sp));
  sm = x32_add(x16_add(sm));
  float rp = 1.f / sp, rm = 1.f / sm;
  pp[0][0] = pk2(ep[0] * rp, ep[1] * rp); pp[0][1] = pk2(ep[2] * rp, ep[3] * rp);
  pp[1][0] = pk2(ep[4] * rp, ep[5] * rp); pp[1][1] = pk2(ep[6] * rp, ep[7] * rp);
  pm[0][0] = pk2(em[0] * rm, em[1] * rm); pm[0][1] = pk2(em[2] * rm, em[3] * rm);
  pm[1][0] = pk2(em[4] * rm, em[5] * rm); pm[1][1] = pk2(em[6] * rm, em[7] * rm);
}

__global__ __launch_bounds__(512) void k1_kernel(const u16* __restrict__ x, const u16* __restrict__ wqkv,
                                                  const u16* __restrict__ wphi, const u16* __restrict__ wo,
                                                  const u16* __restrict__ win, const u16* __restrict__ wout,
                                                  char* __restrict__ ws) {
  __shared__ __attribute__((aligned(16))) char sm[K1_LDS];
  const int tid = threadIdx.x, lane = tid & 63, wave = __builtin_amdgcn_readfirstlane(tid >> 6);
  const int b = blockIdx.x;

  if (b >= 224) {
    const int kb = b - 224;
    if (kb >= 21) return;
    const int k0 = kb * 64;
    u16* T = (u16*)sm;
    constexpr int TS = 456;
    for (int i = tid; i < 64 * 56; i += 512) {
      int r = i / 56, c = i % 56;
      int k = k0 + r;
      const u16* src = (k < 448) ? (wo + (size_t)k * 448) : (wout + (size_t)(k - 448) * 448);
      u32x4 v = *(const u32x4*)(src + c * 8);
      *(u32x4*)(T + r * TS + c * 8) = v;
    }
    __syncthreads();
    u16* wT = (u16*)(ws + WS_WT);
    for (int i = tid; i < 448 * 8; i += 512) {
      int n = i >> 3, c = i & 7;
      u16 t[8];
#pragma unroll
      for (int j = 0; j < 8; ++j) t[j] = T[(c * 8 + j) * TS + n];
      u32x4 v;
#pragma unroll
      for (int j = 0; j < 4; ++j) v[j] = (unsigned)t[2 * j] | ((unsigned)t[2 * j + 1] << 16);
      *(u32x4*)(wT + (size_t)n * KO + k0 + c * 8) = v;
    }
    return;
  }

  const int xcd = b & 7, idx = b >> 3;
  const int cb = (xcd & 1) * 7 + idx % 7;
  const int rt = (xcd >> 1) * 4 + idx / 7;
  const int row0 = rt * 128;
  const u16* W;
  int ldw, c0, c1;
  if (cb < 2) { W = wqkv; ldw = NQKV; c0 = 224 * cb; c1 = c0 + 112; }
  else if (cb < 6) { W = wqkv; ldw = NQKV; c0 = 448 + 112 * (cb - 2); c1 = c0 + 448; }
  else { W = win; ldw = NIN; c0 = 224 * (cb - 6); c1 = c0 + 112; }

  const int g = lane >> 4, r16 = lane & 15;
  const int wm = wave >> 1, wn = wave & 1;

  const u16* wsrc0; const u16* wsrc1; int wdst0, wdst1;
  {
    int i0 = tid, k = i0 / 28, c = i0 % 28;
    int col = (c < 14) ? (c0 + 8 * c) : (c1 + 8 * (c - 14));
    wsrc0 = W + (size_t)k * ldw + col;
    wdst0 = (c >> 1) * K1_WSUB + ((((k >> 2) & 1) << 4) + ((k >> 3) << 2) + (k & 3)) * 32 + (c & 1) * 16;
    int i1 = (tid < 384) ? tid + 512 : tid;
    k = i1 / 28; c = i1 % 28;
    col = (c < 14) ? (c0 + 8 * c) : (c1 + 8 * (c - 14));
    wsrc1 = W + (size_t)k * ldw + col;
    wdst1 = (c >> 1) * K1_WSUB + ((((k >> 2) & 1) << 4) + ((k >> 3) << 2) + (k & 3)) * 32 + (c & 1) * 16;
  }
  const bool has1 = tid < 384;
  const size_t wstep = (size_t)32 * ldw;
  u32x4 wr0 = *(const u32x4*)wsrc0;
  u32x4 wr1 = *(const u32x4*)wsrc1;

  u32x4 xv[2][7];
  {
    const int rr = lane >> 3, cc = lane & 7;
#pragma unroll
    for (int p = 0; p < 2; ++p) {
      const u16* xp = x + (size_t)(row0 + 16 * wave + 8 * p + rr) * D_ + cc * 8;
#pragma unroll
      for (int i = 0; i < 7; ++i) xv[p][i] = *(const u32x4*)(xp + 64 * i);
    }
  }
  bf16x8 wp[4][2];
  const bool do_phi = (cb < 2) || (cb < 6 && wn == 0);
  const int head0 = (cb < 2) ? (8 * cb + 4 * wn) : (4 * (cb - 2));
  if (do_phi) {
#pragma unroll
    for (int hh = 0; hh < 4; ++hh) {
      const int u1 = (g + hh) & 3;
      const u16* wph = wphi + (size_t)(head0 + hh) * 784;
#pragma unroll
      for (int ft = 0; ft < 2; ++ft) {
        const int f = 16 * ft + r16;
#pragma unroll
        for (int j = 0; j < 8; ++j) {
          const int d = 4 * u1 + (j & 3) + ((j >> 2) << 4);
          short v = 0;
          if (f < 28 && d < 28) v = (short)wph[d * 28 + f];
          wp[hh][ft][j] = v;
        }
      }
    }
  }
  {
    const int rr = lane >> 3, cc = lane & 7;
#pragma unroll
    for (int p = 0; p < 2; ++p) {
      float s = 0.f;
#pragma unroll
      for (int i = 0; i < 7; ++i)
#pragma unroll
        for (int j = 0; j < 4; ++j) s += lo_f(xv[p][i][j]) + hi_f(xv[p][i][j]);
      s = red8_add(s);
      const float mean = s * (1.f / 448.f);
      float q = 0.f;
#pragma unroll
      for (int i = 0; i < 7; ++i)
#pragma unroll
        for (int j = 0; j < 4; ++j) {
          float a = lo_f(xv[p][i][j]) - mean, c = hi_f(xv[p][i][j]) - mean;
          q += a * a + c * c;
        }
      q = red8_add(q);
      const float mb = rbf(mean);
      const float vb = rbf(q * (1.f / 448.f));
      const float sd = rbf(sqrtf(rbf(vb + 1e-5f)));
      const float inv = 1.f / sd;
      const int row = 16 * wave + 8 * p + rr;
      char* dst = sm + row * 896 + ((cc ^ ((row >> 1) & 7)) << 4);
#pragma unroll
      for (int i = 0; i < 7; ++i) {
        u32x4 o;
#pragma unroll
        for (int j = 0; j < 4; ++j) {
          unsigned d = pk2(lo_f(xv[p][i][j]) - mb, hi_f(xv[p][i][j]) - mb);
          o[j] = pk2(lo_f(d) * inv, hi_f(d) * inv);
        }
        *(u32x4*)(dst + 128 * i) = o;
      }
    }
  }
  char* wbuf = sm + K1_XB;
  *(u32x4*)(wbuf + wdst0) = wr0;
  if (has1) *(u32x4*)(wbuf + wdst1) = wr1;
  wsrc0 += wstep; wsrc1 += wstep;
  wr0 = *(const u32x4*)wsrc0;
  wr1 = *(const u32x4*)wsrc1;
  __syncthreads();

  f32x4 acc[7][2];
#pragma unroll
  for (int nt = 0; nt < 7; ++nt) { acc[nt][0] = f32x4{0.f, 0.f, 0.f, 0.f}; acc[nt][1] = f32x4{0.f, 0.f, 0.f, 0.f}; }
  const int sw = (r16 >> 1) & 7;
  const char* xbase = sm + (32 * wm + r16) * 896;
  const int xoE = ((g ^ sw) << 4), xoO = (((4 | g) ^ sw) << 4);
  const int wro = 7 * wn * K1_WSUB + lane * 8;
#pragma unroll 2
  for (int ks = 0; ks < 14; ++ks) {
    const char* wb = wbuf + (ks & 1) * K1_WB + wro;
    const char* xp = xbase + (ks >> 1) * 128 + ((ks & 1) ? xoO : xoE);
    bf16x8 xf0 = *(const bf16x8*)(xp);
    bf16x8 xf1 = *(const bf16x8*)(xp + 16 * 896);
    bf16x8 wf[7];
#pragma unroll
    for (int nt = 0; nt < 7; ++nt) {
      s16x4 a = __builtin_amdgcn_ds_read_tr16_b64_v4i16((LDS_AS s16x4*)(wb + nt * K1_WSUB));
      s16x4 c = __builtin_amdgcn_ds_read_tr16_b64_v4i16((LDS_AS s16x4*)(wb + nt * K1_WSUB + 512));
      wf[nt] = __builtin_shufflevector(a, c, 0, 1, 2, 3, 4, 5, 6, 7);
    }
#pragma unroll
    for (int nt = 0; nt < 7; ++nt) {
      acc[nt][0] = mfma16(wf[nt], xf0, acc[nt][0]);
      acc[nt][1] = mfma16(wf[nt], xf1, acc[nt][1]);
    }
    if (ks + 1 < 14) {
      char* wn_ = wbuf + ((ks + 1) & 1) * K1_WB;
      *(u32x4*)(wn_ + wdst0) = wr0;
      if (has1) *(u32x4*)(wn_ + wdst1) = wr1;
      if (ks + 2 < 14) {
        wsrc0 += wstep; wsrc1 += wstep;
        wr0 = *(const u32x4*)wsrc0;
        wr1 = *(const u32x4*)wsrc1;
      }
    }
    __syncthreads();
  }

  if (cb < 6) {
    unsigned qp[7][2][2];
#pragma unroll
    for (int nt = 0; nt < 7; ++nt)
#pragma unroll
      for (int mt = 0; mt < 2; ++mt) {
        qp[nt][mt][0] = pk2(acc[nt][mt][0], acc[nt][mt][1]);
        qp[nt][mt][1] = pk2(acc[nt][mt][2], acc[nt][mt][3]);
      }
    constexpr int TST = 272;
    char* PK = sm;
    char* VT = sm + 4 * 64 * TST;
    if (do_phi) {
      u16* phiq = (u16*)(ws + WS_PHIQ);
#pragma unroll
      for (int hh = 0; hh < 4; ++hh) {
#pragma unroll
        for (int mt = 0; mt < 2; ++mt) {
          unsigned l0, l1, h0, h1;
          if (hh == 0) { l0 = qp[0][mt][0]; l1 = qp[0][mt][1]; h0 = qp[1][mt][0]; h1 = qp[1][mt][1]; }
          else if (hh == 1) {
            bool c = (g == 3);
            l0 = c ? qp[1][mt][0] : qp[2][mt][0]; l1 = c ? qp[1][mt][1] : qp[2][mt][1];
            h0 = c ? qp[2][mt][0] : qp[3][mt][0]; h1 = c ? qp[2][mt][1] : qp[3][mt][1];
          } else if (hh == 2) {
            bool c = (g >= 2);
            l0 = c ? qp[3][mt][0] : qp[4][mt][0]; l1 = c ? qp[3][mt][1] : qp[4][mt][1];
            h0 = c ? qp[4][mt][0] : qp[5][mt][0]; h1 = c ? qp[4][mt][1] : qp[5][mt][1];
          } else {
            bool c = (g >= 1);
            l0 = c ? qp[5][mt][0] : qp[6][mt][0]; l1 = c ? qp[5][mt][1] : qp[6][mt][1];
            h0 = qp[6][mt][0]; h1 = qp[6][mt][1];
          }
          u32x4 qv = {l0, l1, h0, h1};
          unsigned pp[2][2], pm[2][2];
          phi_map(__builtin_bit_cast(bf16x8, qv), wp[hh], g, pp, pm);
          const int sl = 32 * wm + 16 * mt + r16;
          if (cb < 2) {
            u16* dst = phiq + (size_t)(row0 + sl) * 896 + (head0 + hh) * 56 + 4 * g;
            *(u32x2*)(dst) = u32x2{pp[0][0], pp[0][1]};
            *(u32x2*)(dst + 28) = u32x2{pm[0][0], pm[0][1]};
            if (g != 3) {
              *(u32x2*)(dst + 16) = u32x2{pp[1][0], pp[1][1]};
              *(u32x2*)(dst + 44) = u32x2{pm[1][0], pm[1][1]};
            }
          } else {
            char* dst = PK + (hh * 64 + 4 * g) * TST + sl * 2;
#pragma unroll
            for (int ft = 0; ft < 2; ++ft) {
              if (ft == 0 || g != 3) {
                char* d0 = dst + 16 * ft * TST;
                *(u16*)(d0) = (u16)pp[ft][0]; *(u16*)(d0 + TST) = (u16)(pp[ft][0] >> 16);
                *(u16*)(d0 + 2 * TST) = (u16)pp[ft][1]; *(u16*)(d0 + 3 * TST) = (u16)(pp[ft][1] >> 16);
                char* d1 = d0 + 28 * TST;
                *(u16*)(d1) = (u16)pm[ft][0]; *(u16*)(d1 + TST) = (u16)(pm[ft][0] >> 16);
                *(u16*)(d1 + 2 * TST) = (u16)pm[ft][1]; *(u16*)(d1 + 3 * TST) = (u16)(pm[ft][1] >> 16);
              }
            }
          }
        }
      }
    } else {
#pragma unroll
      for (int nt = 0; nt < 7; ++nt) {
        const int Q = 4 * nt + g;
        const int hh = Q / 7, d = 4 * (Q - 7 * hh);
#pragma unroll
        for (int mt = 0; mt < 2; ++mt) {
          char* d0 = VT + (hh * 32 + d) * TST + (32 * wm + 16 * mt + r16) * 2;
          *(u16*)(d0) = (u16)qp[nt][mt][0]; *(u16*)(d0 + TST) = (u16)(qp[nt][mt][0] >> 16);
          *(u16*)(d0 + 2 * TST) = (u16)qp[nt][mt][1]; *(u16*)(d0 + 3 * TST) = (u16)(qp[nt][mt][1] >> 16);
        }
      }
      {
        char* d0 = VT + (g * 32 + 28) * TST + (32 * wm + r16) * 2;
        *(u16*)(d0) = (u16)0x3F80;
        *(u16*)(d0 + 32) = (u16)0x3F80;
      }
    }
    if (cb >= 2) {
      __syncthreads();
      const int hh = wave >> 1, ftb = (wave & 1) * 2;
      f32x4 kacc[2][2];
#pragma unroll
      for (int a = 0; a < 2; ++a) { kacc[a][0] = f32x4{0.f, 0.f, 0.f, 0.f}; kacc[a][1] = f32x4{0.f, 0.f, 0.f, 0.f}; }
#pragma unroll
      for (int kk = 0; kk < 4; ++kk) {
        bf16x8 af[2], bfr[2];
#pragma unroll
        for (int a = 0; a < 2; ++a) {
          af[a] = *(const bf16x8*)(PK + (hh * 64 + 16 * (ftb + a) + r16) * TST + (32 * kk + 8 * g) * 2);
          bfr[a] = *(const bf16x8*)(VT + (hh * 32 + 16 * a + r16) * TST + (32 * kk + 8 * g) * 2);
        }
#pragma unroll
        for (int a = 0; a < 2; ++a)
#pragma unroll
          for (int c = 0; c < 2; ++c) kacc[a][c] = mfma16(af[a], bfr[c], kacc[a][c]);
      }
      float* kvp = (float*)(ws + WS_KVP) + (size_t)((rt * 16 + 4 * (cb - 2) + hh) * 56) * 32;
#pragma unroll
      for (int a = 0; a < 2; ++a)
#pragma unroll
        for (int c = 0; c < 2; ++c)
#pragma unroll
          for (int j = 0; j < 4; ++j) {
            const int f = 16 * (ftb + a) + 4 * g + j;
            if (f < 56) kvp[f * 32 + 16 * c + r16] = kacc[a][c][j];
          }
    }
  } else {
    unsigned zp[7][2][2];
#pragma unroll
    for (int nt = 0; nt < 7; ++nt)
#pragma unroll
      for (int mt = 0; mt < 2; ++mt) {
        unsigned a = pk2(acc[nt][mt][0], acc[nt][mt][1]), c = pk2(acc[nt][mt][2], acc[nt][mt][3]);
        zp[nt][mt][0] = pk2(gelu_t(lo_f(a)), gelu_t(hi_f(a)));
        zp[nt][mt][1] = pk2(gelu_t(lo_f(c)), gelu_t(hi_f(c)));
      }
    if (cb < 10) {
      u16* z1 = (u16*)(ws + WS_Z1);
#pragma unroll
      for (int mt = 0; mt < 2; ++mt) {
        u16* dst = z1 + (size_t)(row0 + 32 * wm + 16 * mt + r16) * FH + 224 * (cb - 6) + 112 * wn + 4 * g;
#pragma unroll
        for (int nt = 0; nt < 7; ++nt) *(u32x2*)(dst + 16 * nt) = u32x2{zp[nt][mt][0], zp[nt][mt][1]};
      }
    } else {
      constexpr int TST = 272;
#pragma unroll
      for (int mt = 0; mt < 2; ++mt) {
        float s = 0.f;
#pragma unroll
        for (int nt = 0; nt < 7; ++nt)
          s += (lo_f(zp[nt][mt][0]) + hi_f(zp[nt][mt][0])) + (lo_f(zp[nt][mt][1]) + hi_f(zp[nt][mt][1]));
        s = x32_add(x16_add(s));
        const float mean = s * (1.f / 112.f);
        float q = 0.f;
#pragma unroll
        for (int nt = 0; nt < 7; ++nt) {
          float a = lo_f(zp[nt][mt][0]) - mean, c = hi_f(zp[nt][mt][0]) - mean;
          float d = lo_f(zp[nt][mt][1]) - mean, e = hi_f(zp[nt][mt][1]) - mean;
          q += (a * a + c * c) + (d * d + e * e);
        }
        q = x32_add(x16_add(q));
        if (g == 0) {
          f32x2* st = (f32x2*)(ws + WS_STAT) + (size_t)(2 * (cb - 10) + wn) * S_ + row0 + 32 * wm + 16 * mt + r16;
          *st = f32x2{mean, q};
        }
#pragma unroll
        for (int nt = 0; nt < 7; ++nt) {
          char* d0 = sm + (112 * wn + 16 * nt + 4 * g) * TST + (32 * wm + 16 * mt + r16) * 2;
          *(u16*)(d0) = (u16)zp[nt][mt][0]; *(u16*)(d0 + TST) = (u16)(zp[nt][mt][0] >> 16);
          *(u16*)(d0 + 2 * TST) = (u16)zp[nt][mt][1]; *(u16*)(d0 + 3 * TST) = (u16)(zp[nt][mt][1] >> 16);
        }
      }
      __syncthreads();
      u16* z2t = (u16*)(ws + WS_Z2T);
#pragma unroll
      for (int i = 0; i < 7; ++i) {
        const int id = tid + 512 * i, f = id >> 4, c = id & 15;
        u32x4 v = *(const u32x4*)(sm + f * TST + c * 16);
        *(u32x4*)(z2t + (size_t)(224 * (cb - 10) + f) * S_ + row0 + c * 8) = v;
      }
    }
  }
}

__global__ __launch_bounds__(256) void k2_kernel(char* __restrict__ ws) {
  __shared__ __attribute__((aligned(16))) char sm[32 * 144];
  const int tid = threadIdx.x, lane = tid & 63, wave = tid >> 6;
  const int b = blockIdx.x;
  if (b < 128) {
    const int h = b & 15, rt = b >> 4;
    const float* kvp = (const float*)(ws + WS_KVP) + (size_t)h * 56 * 32;
    for (int i = tid; i < 64 * 32; i += 256) {
      const int f = i >> 5, d = i & 31;
      float s = 0.f;
      if (f < 56) {
#pragma unroll
        for (int p = 0; p < 16; ++p) s += kvp[(size_t)p * 16 * 56 * 32 + i];
      }
      *(u16*)(sm + d * 144 + f * 2) = (u16)pk2(s, 0.f);
    }
    __syncthreads();
    const int g = lane >> 4, r16 = lane & 15;
    bf16x8 af[2][2];
#pragma unroll
    for (int dt = 0; dt < 2; ++dt)
#pragma unroll
      for (int kk = 0; kk < 2; ++kk) af[dt][kk] = *(const bf16x8*)(sm + (16 * dt + r16) * 144 + (32 * kk + 8 * g) * 2);
    const u16* phiq = (const u16*)(ws + WS_PHIQ);
    u16* oh = (u16*)(ws + WS_OH);
#pragma unroll
    for (int st = 0; st < 4; ++st) {
      const int s = rt * 256 + wave * 64 + st * 16 + r16;
      const u16* pq = phiq + (size_t)s * 896 + h * 56 + 8 * g;
      bf16x8 b0 = *(const bf16x8*)(pq);
      bf16x8 b1 = {0, 0, 0, 0, 0, 0, 0, 0};
      if (g != 3) b1 = *(const bf16x8*)(pq + 32);
      f32x4 a0 = {0.f, 0.f, 0.f, 0.f}, a1 = {0.f, 0.f, 0.f, 0.f};
      a0 = mfma16(af[0][0], b0, a0); a0 = mfma16(af[0][1], b1, a0);
      a1 = mfma16(af[1][0], b0, a1); a1 = mfma16(af[1][1], b1, a1);
      float den = __shfl(a1[0], 48 + r16, 64);
      den = rbf(rbf(den) + 1e-6f);
      const float inv = 1.f / den;
      u16* dst = oh + (size_t)s * KO + h * 28 + 4 * g;
      *(u32x2*)(dst) = u32x2{pk2(rbf(a0[0]) * inv, rbf(a0[1]) * inv), pk2(rbf(a0[2]) * inv, rbf(a0[3]) * inv)};
      if (g != 3)
        *(u32x2*)(dst + 16) = u32x2{pk2(rbf(a1[0]) * inv, rbf(a1[1]) * inv), pk2(rbf(a1[2]) * inv, rbf(a1[3]) * inv)};
    }
  } else {
    const int bb = b - 128;
    const int ft = bb >> 4, stile = bb & 15;
    f32x2* ms = (f32x2*)sm;
    if (tid < 128) {
      const f32x2* st = (const f32x2*)(ws + WS_STAT) + stile * 128 + tid;
      f32x2 p[8];
#pragma unroll
      for (int j = 0; j < 8; ++j) p[j] = st[(size_t)j * S_];
      float m = 0.f;
#pragma unroll
      for (int j = 0; j < 8; ++j) m += p[j][0];
      m *= 0.125f;
      float q = 0.f;
#pragma unroll
      for (int j = 0; j < 8; ++j) { float d = p[j][0] - m; q += p[j][1] + 112.f * d * d; }
      const float mb = rbf(m);
      const float vb = rbf(q * (1.f / 896.f));
      const float sd = rbf(sqrtf(rbf(vb + 1e-5f)));
      ms[tid] = f32x2{mb, 1.f / sd};
    }
    __syncthreads();
    const u16* z2t = (const u16*)(ws + WS_Z2T);
    u16* z2n = (u16*)(ws + WS_Z2N);
    const int c = tid & 15;
    f32x2 st[8];
#pragma unroll
    for (int j = 0; j < 8; ++j) st[j] = ms[c * 8 + j];
#pragma unroll
    for (int i = 0; i < 4; ++i) {
      const int f = (tid >> 4) + 16 * i;
      if (f < 56) {
        const size_t off = (size_t)(ft * 56 + f) * S_ + stile * 128 + c * 8;
        u32x4 v = *(const u32x4*)(z2t + off);
        u32x4 o;
#pragma unroll
        for (int j = 0; j < 4; ++j) {
          unsigned d = pk2(lo_f(v[j]) - st[2 * j][0], hi_f(v[j]) - st[2 * j + 1][0]);
          o[j] = pk2(lo_f(d) * st[2 * j][1], hi_f(d) * st[2 * j + 1][1]);
        }
        *(u32x4*)(z2n + off) = o;
      }
    }
  }
}

constexpr int K3_STAGE = 176 * 256;
__global__ __launch_bounds__(512) void k3_kernel(const u16* __restrict__ wsp, const u16* __restrict__ bsp,
                                                  char* __restrict__ ws) {
  __shared__ __attribute__((aligned(16))) char sm[3 * K3_STAGE];
  const int tid = threadIdx.x, lane = tid & 63, wave = __builtin_amdgcn_readfirstlane(tid >> 6);
  const int b = blockIdx.x;
  const int xcd = b & 7, idx = b >> 3;
  const int mt_ = (xcd >> 1) * 8 + (idx & 7);
  const int nt_ = (xcd & 1) * 4 + (idx >> 3);
  const int t0 = mt_ * 64, f0 = nt_ * 112;
  const u16* z2n = (const u16*)(ws + WS_Z2N);

  const u16* src[6];
#pragma unroll
  for (int j = 0; j < 6; ++j) {
    int i = wave + 8 * j;
    if (i >= 44) i = wave;
    const int row = 4 * i + (lane >> 4);
    const int c = (lane & 15) ^ (row & 15);
    const u16* base = (row < 64) ? (wsp + (size_t)(t0 + row) * S_) : (z2n + (size_t)(f0 + row - 64) * S_);
    src[j] = base + c * 8;
  }
  const bool six = wave < 4;
  auto issue = [&](int t) {
    char* dst = sm + (t % 3) * K3_STAGE + wave * 1024;
#pragma unroll
    for (int j = 0; j < 5; ++j)
      __builtin_amdgcn_global_load_lds((const unsigned*)(src[j] + t * 128), (LDS_AS unsigned*)(dst + j * 8192), 16, 0, 0);
    if (six) __builtin_amdgcn_global_load_lds((const unsigned*)(src[5] + t * 128), (LDS_AS unsigned*)(dst + 5 * 8192), 16, 0, 0);
  };

  const int g = lane >> 4, r16 = lane & 15;
  const int kg = wave >> 2, wm = (wave >> 1) & 1, wn = (wave & 1) ^ kg;
  f32x4 acc[4][2];
#pragma unroll
  for (int n = 0; n < 4; ++n) { acc[n][0] = f32x4{0.f, 0.f, 0.f, 0.f}; acc[n][1] = f32x4{0.f, 0.f, 0.f, 0.f}; }
  const int co0 = ((8 * kg + g) ^ r16) << 4, co1 = ((8 * kg + 4 + g) ^ r16) << 4;
  const int arow = (32 * wm + r16) * 256;
  const int brow = (64 + 64 * wn + r16) * 256;

  issue(0);
  issue(1);
  for (int t = 0; t < 16; ++t) {
    if (t < 15) {
      if (six) asm volatile("s_waitcnt vmcnt(6)" ::: "memory");
      else asm volatile("s_waitcnt vmcnt(5)" ::: "memory");
    } else {
      asm volatile("s_waitcnt vmcnt(0)" ::: "memory");
    }
    asm volatile("s_waitcnt lgkmcnt(0)" ::: "memory");
    __builtin_amdgcn_s_barrier();
    if (t + 2 < 16) issue(t + 2);
    const char* sb = sm + (t % 3) * K3_STAGE;
#pragma unroll
    for (int kk = 0; kk < 2; ++kk) {
      const int co = kk ? co1 : co0;
      bf16x8 a0 = *(const bf16x8*)(sb + arow + co);
      bf16x8 a1 = *(const bf16x8*)(sb + arow + 16 * 256 + co);
      bf16x8 bf[4];
#pragma unroll
      for (int n = 0; n < 3; ++n) bf[n] = *(const bf16x8*)(sb + brow + n * 16 * 256 + co);
      if (wn == 0) bf[3] = *(const bf16x8*)(sb + brow + 3 * 16 * 256 + co);
#pragma unroll
      for (int n = 0; n < 3; ++n) {
        acc[n][0] = mfma16(bf[n], a0, acc[n][0]);
        acc[n][1] = mfma16(bf[n], a1, acc[n][1]);
      }
      if (wn == 0) {
        acc[3][0] = mfma16(bf[3], a0, acc[3][0]);
        acc[3][1] = mfma16(bf[3], a1, acc[3][1]);
      }
    }
  }
  __syncthreads();
  f32x4* red = (f32x4*)sm + (size_t)((wm * 2 + wn) * 8) * 64 + lane;
  if (kg == 1) {
#pragma unroll
    for (int n = 0; n < 4; ++n) { red[(2 * n) * 64] = acc[n][0]; red[(2 * n + 1) * 64] = acc[n][1]; }
  }
  __syncthreads();
  if (kg == 0) {
    const u16* z1 = (const u16*)(ws + WS_Z1);
    u16* oh = (u16*)(ws + WS_OH);
#pragma unroll
    for (int m = 0; m < 2; ++m) {
      const int t = t0 + 32 * wm + 16 * m + r16;
      const float bb = bf2f(bsp[t]);
#pragma unroll
      for (int n = 0; n < 4; ++n) {
        if (n < 3 || wn == 0) {
          f32x4 v = acc[n][m] + red[(2 * n + m) * 64];
          const int f = f0 + 64 * wn + 16 * n + 4 * g;
          u32x2 z = *(const u32x2*)(z1 + (size_t)t * FH + f);
          float g0 = rbf(rbf(v[0]) + bb), g1 = rbf(rbf(v[1]) + bb), g2 = rbf(rbf(v[2]) + bb), g3 = rbf(rbf(v[3]) + bb);
          u32x2 o = {pk2(lo_f(z[0]) * g0, hi_f(z[0]) * g1), pk2(lo_f(z[1]) * g2, hi_f(z[1]) * g3)};
          *(u32x2*)(oh + (size_t)t * KO + 448 + f) = o;
        }
      }
    }
  }
}

__global__ __launch_bounds__(384) void k4_kernel(const u16* __restrict__ x, const char* __restrict__ ws,
                                                  u16* __restrict__ out) {
  __shared__ __attribute__((aligned(16))) char sm[6 * 14 * 1024];
  const int tid = threadIdx.x, lane = tid & 63, wave = __builtin_amdgcn_readfirstlane(tid >> 6);
  const int b = blockIdx.x;
  const int xcd = b & 7, idx = b >> 3;
  const int mt_ = xcd * 8 + (idx & 7), nt_ = idx >> 3;
  const int t0 = mt_ * 32, n0 = nt_ * 112;
  const int g = lane >> 4, r16 = lane & 15;
  const u16* oh = (const u16*)(ws + WS_OH) + (size_t)(t0 + r16) * KO + wave * 224 + 8 * g;
  const u16* wt = (const u16*)(ws + WS_WT) + (size_t)(n0 + r16) * KO + wave * 224 + 8 * g;
  f32x4 acc[7][2];
#pragma unroll
  for (int n = 0; n < 7; ++n) { acc[n][0] = f32x4{0.f, 0.f, 0.f, 0.f}; acc[n][1] = f32x4{0.f, 0.f, 0.f, 0.f}; }
#pragma unroll
  for (int ks = 0; ks < 7; ++ks) {
    bf16x8 a0 = *(const bf16x8*)(oh + ks * 32);
    bf16x8 a1 = *(const bf16x8*)(oh + 16 * KO + ks * 32);
    bf16x8 wf[7];
#pragma unroll
    for (int n = 0; n < 7; ++n) wf[n] = *(const bf16x8*)(wt + (size_t)n * 16 * KO + ks * 32);
#pragma unroll
    for (int n = 0; n < 7; ++n) {
      acc[n][0] = mfma16(wf[n], a0, acc[n][0]);
      acc[n][1] = mfma16(wf[n], a1, acc[n][1]);
    }
  }
  f32x4* red = (f32x4*)sm;
#pragma unroll
  for (int n = 0; n < 7; ++n) {
    red[(wave * 14 + 2 * n) * 64 + lane] = acc[n][0];
    red[(wave * 14 + 2 * n + 1) * 64 + lane] = acc[n][1];
  }
  __syncthreads();
  for (int i = tid; i < 14 * 64; i += 384) {
    const int tile = i >> 6, l = i & 63;
    f32x4 va = red[i] + red[14 * 64 + i];
    f32x4 vb = (red[2 * 14 * 64 + i] + red[3 * 14 * 64 + i]) + (red[4 * 14 * 64 + i] + red[5 * 14 * 64 + i]);
    const int n = n0 + 16 * (tile >> 1) + 4 * (l >> 4);
    const int t = t0 + 16 * (tile & 1) + (l & 15);
    u32x2 xv = *(const u32x2*)(x + (size_t)t * D_ + n);
    float r0 = rbf(lo_f(xv[0]) + rbf(va[0])) + rbf(vb[0]);
    float r1 = rbf(hi_f(xv[0]) + rbf(va[1])) + rbf(vb[1]);
    float r2 = rbf(lo_f(xv[1]) + rbf(va[2])) + rbf(vb[2]);
    float r3 = rbf(hi_f(xv[1]) + rbf(va[3])) + rbf(vb[3]);
    *(u32x2*)(out + (size_t)t * D_ + n) = u32x2{pk2(r0, r1), pk2(r2, r3)};
  }
}

extern "C" void kernel_launch(void* const* d_in, const int* in_sizes, int n_in, void* d_out, int out_size,
                              void* d_ws, size_t ws_size, hipStream_t stream) {
  const u16* x = (const u16*)d_in[0];
  const u16* wqkv = (const u16*)d_in[1];
  const u16* wphi = (const u16*)d_in[2];
  const u16* wo = (const u16*)d_in[3];
  const u16* win = (const u16*)d_in[5];
  const u16* wsp = (const u16*)d_in[6];
  const u16* bsp = (const u16*)d_in[7];
  const u16* wout = (const u16*)d_in[8];
  char* ws = (char*)d_ws;
  k1_kernel<<<245, 512, 0, stream>>>(x, wqkv, wphi, wo, win, wout, ws);
  k2_kernel<<<384, 256, 0, stream>>>(ws);
  k3_kernel<<<256, 512, 0, stream>>>(wsp, bsp, ws);
  k4_kernel<<<256, 384, 0, stream>>>(x, ws, (u16*)d_out);
}
```

```cpp
#include <hip/hip_runtime.h>
#include <hip/hip_bf16.h>
#include <stdint.h>

typedef __attribute__((ext_vector_type(8))) short bf16x8;
typedef __attribute__((ext_vector_type(4))) short s16x4;
typedef __attribute__((ext_vector_type(4))) float f32x4;
typedef __attribute__((ext_vector_type(2))) float f32x2;
typedef __attribute__((ext_vector_type(4))) unsigned u32x4;
typedef __attribute__((ext_vector_type(2))) unsigned u32x2;
typedef __attribute__((ext_vector_type(2))) __bf16 bf2_t;
typedef unsigned short u16;
#define LDS_AS __attribute__((address_space(3)))

constexpr int S_ = 2048, D_ = 448, NQKV = 1344, NIN = 1792, FH = 896, KO = 1344;

__device__ __forceinline__ unsigned pk2(float a, float b) {
  f32x2 v = {a, b};
  return __builtin_bit_cast(unsigned, __builtin_convertvector(v, bf2_t));
}
__device__ __forceinline__ float lo_f(unsigned u) { return __uint_as_float(u << 16); }
__device__ __forceinline__ float hi_f(unsigned u) { return __uint_as_float(u & 0xffff0000u); }
__device__ __forceinline__ float rbf(float a) { return lo_f(pk2(a, 0.f)); }
__device__ __forceinline__ float bf2f(u16 v) { return __uint_as_float(((unsigned)v) << 16); }
__device__ __forceinline__ float dot2(unsigned a, unsigned b, float c) {
  return __builtin_amdgcn_fdot2_f32_bf16(__builtin_bit_cast(bf2_t, a), __builtin_bit_cast(bf2_t, b), c, false);
}

__device__ __forceinline__ float x16_max(float v) {
  auto r = __builtin_amdgcn_permlane16_swap(__float_as_uint(v), __float_as_uint(v), false, false);
  return fmaxf(__uint_as_float(r[0]), __uint_as_float(r[1]));
}
__device__ __forceinline__ float x32_max(float v) {
  auto r = __builtin_amdgcn_permlane32_swap(__float_as_uint(v), __float_as_uint(v), false, false);
  return fmaxf(__uint_as_float(r[0]), __uint_as_float(r[1]));
}
__device__ __forceinline__ float x16_min(float v) {
  auto r = __builtin_amdgcn_permlane16_swap(__float_as_uint(v), __float_as_uint(v), false, false);
  return fminf(__uint_as_float(r[0]), __uint_as_float(r[1]));
}
__device__ __forceinline__ float x32_min(float v) {
  auto r = __builtin_amdgcn_permlane32_swap(__float_as_uint(v), __float_as_uint(v), false, false);
  return fminf(__uint_as_float(r[0]), __uint_as_float(r[1]));
}
__device__ __forceinline__ float x16_add(float v) {
  auto r = __builtin_amdgcn_permlane16_swap(__float_as_uint(v), __float_as_uint(v), false, false);
  return __uint_as_float(r[0]) + __uint_as_float(r[1]);
}
__device__ __forceinline__ float x32_add(float v) {
  auto r = __builtin_amdgcn_permlane32_swap(__float_as_uint(v), __float_as_uint(v), false, false);
  return __uint_as_float(r[0]) + __uint_as_float(r[1]);
}
__device__ __forceinline__ float red8_add(float v) {
  v += __uint_as_float(__builtin_amdgcn_mov_dpp(__float_as_uint(v), 0xB1, 0xf, 0xf, true));
  v += __uint_as_float(__builtin_amdgcn_mov_dpp(__float_as_uint(v), 0x4E, 0xf, 0xf, true));
  v += __uint_as_float(__builtin_amdgcn_mov_dpp(__float_as_uint(v), 0x141, 0xf, 0xf, true));
  return v;
}
__device__ __forceinline__ f32x4 mfma16(bf16x8 a, bf16x8 b, f32x4 c) {
  return __builtin_amdgcn_mfma_f32_16x16x32_bf16(a, b, c, 0, 0, 0);
}
__device__ __forceinline__ unsigned gelu_pk(unsigned p) {
  const f32x2 u = {lo_f(p), hi_f(p)};
  const f32x2 c1 = {-2.3022081986f, -2.3022081986f};
  const f32x2 c2 = {-0.1029432397f, -0.1029432397f};
  f32x2 t = u * u;
  f32x2 a = u * (t * c2 + c1);
  f32x2 d = {1.f + __builtin_amdgcn_exp2f(a[0]), 1.f + __builtin_amdgcn_exp2f(a[1])};
  f32x2 r = {__builtin_amdgcn_rcpf(d[0]), __builtin_amdgcn_rcpf(d[1])};
  f32x2 o = u * r;
  return pk2(o[0], o[1]);
}
#define BAR() do { asm volatile("s_waitcnt lgkmcnt(0)" ::: "memory"); __builtin_amdgcn_s_barrier(); asm volatile("" ::: "memory"); } while (0)
__device__ __forceinline__ void glds16(const void* g, char* l) {
  __builtin_amdgcn_global_load_lds((const unsigned*)g, (LDS_AS unsigned*)l, 16, 0, 0);
}
__device__ __forceinline__ bf16x8 tr_frag(const char* p0, const char* p1) {
  s16x4 a = __builtin_amdgcn_ds_read_tr16_b64_v4i16((LDS_AS s16x4*)p0);
  s16x4 c = __builtin_amdgcn_ds_read_tr16_b64_v4i16((LDS_AS s16x4*)p1);
  return __builtin_shufflevector(a, c, 0, 1, 2, 3, 4, 5, 6, 7);
}

constexpr size_t WS_Q = 0;
constexpr size_t WS_Z1 = WS_Q + (size_t)S_ * 448 * 2 + 256;
constexpr size_t WS_Z2T = WS_Z1 + (size_t)S_ * 896 * 2;
constexpr size_t WS_Z2N = WS_Z2T + (size_t)S_ * 896 * 2;
constexpr size_t WS_OH = WS_Z2N + (size_t)S_ * 896 * 2;
constexpr size_t WS_WT = WS_OH + (size_t)S_ * KO * 2;
constexpr size_t WS_KVP = WS_WT + (size_t)D_ * KO * 2;
constexpr size_t WS_STAT = WS_KVP + (size_t)16 * 16 * 56 * 32 * 4;
constexpr size_t WS_END = WS_STAT + (size_t)8 * S_ * 8;

__device__ __forceinline__ void phi_map(bf16x8 qf, bf16x8 wp0, bf16x8 wp1, int g, unsigned (&pp)[2][2],
                                        unsigned (&pm)[2][2]) {
  f32x4 z = {0.f, 0.f, 0.f, 0.f};
  f32x4 p0 = mfma16(wp0, qf, z);
  f32x4 p1 = mfma16(wp1, qf, z);
  float v[8];
  {
    unsigned a = pk2(p0[0], p0[1]), b = pk2(p0[2], p0[3]), c = pk2(p1[0], p1[1]), d = pk2(p1[2], p1[3]);
    v[0] = lo_f(a); v[1] = hi_f(a); v[2] = lo_f(b); v[3] = hi_f(b);
    v[4] = lo_f(c); v[5] = hi_f(c); v[6] = lo_f(d); v[7] = hi_f(d);
  }
  const bool valid1 = (g != 3);
  if (!valid1) { v[4] = v[0]; v[5] = v[0]; v[6] = v[0]; v[7] = v[0]; }
  float mx = fmaxf(fmaxf(fmaxf(v[0], v[1]), fmaxf(v[2], v[3])), fmaxf(fmaxf(v[4], v[5]), fmaxf(v[6], v[7])));
  float mn = fminf(fminf(fminf(v[0], v[1]), fminf(v[2], v[3])), fminf(fminf(v[4], v[5]), fminf(v[6], v[7])));
  mx = x32_max(x16_max(mx));
  mn = x32_min(x16_min(mn));
  const float L2E = 1.4426950408889634f;
  const float mxl = mx * L2E, mnl = mn * L2E;
  float ep[8], em[8];
  float sp = 0.f, sm = 0.f;
#pragma unroll
  for (int j = 0; j < 8; ++j) {
    ep[j] = __builtin_amdgcn_exp2f(__builtin_fmaf(v[j], L2E, -mxl));
    em[j] = __builtin_amdgcn_exp2f(__builtin_fmaf(v[j], -L2E, mnl));
    if (j >= 4) { ep[j] = valid1 ? ep[j] : 0.f; em[j] = valid1 ? em[j] : 0.f; }
    sp += ep[j];
    sm += em[j];
  }
  sp = x32_add(x16_add(sp));
  sm = x32_add(x16_add(sm));
  const float rp = __builtin_amdgcn_rcpf(sp), rm = __builtin_amdgcn_rcpf(sm);
  pp[0][0] = pk2(ep[0] * rp, ep[1] * rp); pp[0][1] = pk2(ep[2] * rp, ep[3] * rp);
  pp[1][0] = pk2(ep[4] * rp, ep[5] * rp); pp[1][1] = pk2(ep[6] * rp, ep[7] * rp);
  pm[0][0] = pk2(em[0] * rm, em[1] * rm); pm[0][1] = pk2(em[2] * rm, em[3] * rm);
  pm[1][0] = pk2(em[4] * rm, em[5] * rm); pm[1][1] = pk2(em[6] * rm, em[7] * rm);
}

constexpr int K1_XB = 128 * 896;
constexpr int K1_WST = 32 * 448;
constexpr int K1_LDS = K1_XB + 3 * K1_WST;

__global__ __launch_bounds__(640) void k1_kernel(const u16* __restrict__ x, const u16* __restrict__ wqkv,
                                                  const u16* __restrict__ wphi, const u16* __restrict__ wo,
                                                  const u16* __restrict__ win, const u16* __restrict__ wout,
                                                  char* __restrict__ ws) {
  __shared__ __attribute__((aligned(16))) char sm[K1_LDS];
  const int tid = threadIdx.x, lane = tid & 63, wave = __builtin_amdgcn_readfirstlane(tid >> 6);
  const int b = blockIdx.x;

  if (b >= 224) {
    const int kb = b - 224;
    const int k0 = kb * 64;
    u16* T = (u16*)sm;
    constexpr int TS = 456;
    u32x4 v[6];
#pragma unroll
    for (int j = 0; j < 6; ++j) {
      int i = tid + 640 * j;
      if (i >= 64 * 56) i = tid;
      const int r = i / 56, c = i % 56;
      const int k = k0 + r;
      const u16* src = (k < 448) ? (wo + (size_t)k * 448) : (wout + (size_t)(k - 448) * 448);
      v[j] = *(const u32x4*)(src + c * 8);
    }
#pragma unroll
    for (int j = 0; j < 6; ++j) {
      const int i = tid + 640 * j;
      if (i < 64 * 56) *(u32x4*)(T + (i / 56) * TS + (i % 56) * 8) = v[j];
    }
    __syncthreads();
    u16* wT = (u16*)(ws + WS_WT);
    for (int i = tid; i < 448 * 8; i += 640) {
      const int n = i >> 3, c = i & 7;
      u16 t[8];
#pragma unroll
      for (int j = 0; j < 8; ++j) t[j] = T[(c * 8 + j) * TS + n];
      u32x4 o;
#pragma unroll
      for (int j = 0; j < 4; ++j) o[j] = (unsigned)t[2 * j] | ((unsigned)t[2 * j + 1] << 16);
      *(u32x4*)(wT + (size_t)n * KO + k0 + c * 8) = o;
    }
    return;
  }

  const int xcd = b & 7, idx = b >> 3;
  const int cb = (xcd & 1) * 7 + idx % 7;
  const int rt = (xcd >> 1) * 4 + idx / 7;
  const int row0 = rt * 128;
  const bool is_kv = (cb >= 2 && cb < 6);
  char* wring = sm + K1_XB;
  const int g = lane >> 4, r16 = lane & 15;
  const int wm = (wave >> 1) & 3, wn = wave & 1;
  f32x4 acc[7][2];
  u32x4 wphi_raw = {0u, 0u, 0u, 0u};

  if (wave >= 8) {
    const int lw = wave - 8;
    const u16* W = (cb < 6) ? wqkv : win;
    const int ldw = (cb < 6) ? NQKV : NIN;
    const u16* src[7];
#pragma unroll
    for (int j = 0; j < 7; ++j) {
      const int o = (lw + 2 * j) * 1024 + lane * 16;
      const int k = o / 448, cp = (o % 448) >> 4;
      const int G = (cp >> 1) ^ ((k >> 3) & 1);
      const int c = 2 * G + (cp & 1);
      int col;
      if (cb < 2) col = 224 * cb + 8 * c;
      else if (cb < 6) {
        const int w_ = (c >= 14) ? 1 : 0, cc = c - 14 * w_;
        const int hA = 4 * (cb - 2) + 2 * w_;
        col = (cc < 7) ? (448 + 28 * hA + 8 * cc) : (896 + 28 * hA + 8 * (cc - 7));
      } else col = 224 * (cb - 6) + 8 * c;
      src[j] = W + (size_t)k * ldw + col;
    }
    const size_t wstep = (size_t)32 * ldw;
    char* dst0 = wring + lw * 1024;
#pragma unroll
    for (int s = 0; s < 3; ++s)
#pragma unroll
      for (int j = 0; j < 7; ++j) glds16(src[j] + s * wstep, dst0 + s * K1_WST + j * 2048);
    asm volatile("s_waitcnt vmcnt(14)" ::: "memory");
    __builtin_amdgcn_s_barrier();
    int nb = 0;
    for (int t = 0; t < 14; ++t) {
      if (t + 2 < 14) asm volatile("s_waitcnt vmcnt(7)" ::: "memory");
      else asm volatile("s_waitcnt vmcnt(0)" ::: "memory");
      __builtin_amdgcn_s_barrier();
      if (t + 3 < 14) {
        char* dst = dst0 + nb * K1_WST;
#pragma unroll
        for (int j = 0; j < 7; ++j) glds16(src[j] + (t + 3) * wstep, dst + j * 2048);
        nb = (nb == 2) ? 0 : nb + 1;
      }
    }
  } else {
    u32x4 xv[2][7];
    const int rr = lane >> 3, cc = lane & 7;
#pragma unroll
    for (int p = 0; p < 2; ++p) {
      const u16* xp = x + (size_t)(row0 + 16 * wave + 8 * p + rr) * D_ + cc * 8;
#pragma unroll
      for (int i = 0; i < 7; ++i) xv[p][i] = *(const u32x4*)(xp + 64 * i);
    }
    if (is_kv && tid < 392) wphi_raw = *(const u32x4*)(wphi + (size_t)(4 * (cb - 2)) * 784 + tid * 8);
#pragma unroll
    for (int p = 0; p < 2; ++p) {
      float s = 0.f;
#pragma unroll
      for (int i = 0; i < 7; ++i)
#pragma unroll
        for (int j = 0; j < 4; ++j) s = dot2(xv[p][i][j], 0x3F803F80u, s);
      s = red8_add(s);
      const float mean = s * (1.f / 448.f);
      const float mb = rbf(mean);
      float q = 0.f;
#pragma unroll
      for (int i = 0; i < 7; ++i)
#pragma unroll
        for (int j = 0; j < 4; ++j) {
          const unsigned d = pk2(lo_f(xv[p][i][j]) - mb, hi_f(xv[p][i][j]) - mb);
          xv[p][i][j] = d;
          q = dot2(d, d, q);
        }
      q = red8_add(q);
      const float dm = mean - mb;
      const float var = fmaxf(q * (1.f / 448.f) - dm * dm, 0.f);
      const float sd = rbf(sqrtf(rbf(rbf(var) + 1e-5f)));
      const float inv = 1.f / sd;
      const int row = 16 * wave + 8 * p + rr;
      char* dst = sm + row * 896 + ((cc ^ ((row >> 1) & 7)) << 4);
#pragma unroll
      for (int i = 0; i < 7; ++i) {
        u32x4 o;
#pragma unroll
        for (int j = 0; j < 4; ++j) o[j] = pk2(lo_f(xv[p][i][j]) * inv, hi_f(xv[p][i][j]) * inv);
        *(u32x4*)(dst + 128 * i) = o;
      }
    }
#pragma unroll
    for (int nt = 0; nt < 7; ++nt) { acc[nt][0] = f32x4{0.f, 0.f, 0.f, 0.f}; acc[nt][1] = f32x4{0.f, 0.f, 0.f, 0.f}; }
    const int sw = (r16 >> 1) & 7;
    const char* xbase = sm + (32 * wm + r16) * 896;
    const int xoE = ((g ^ sw) << 4), xoO = (((4 | g) ^ sw) << 4);
    const int wrow = (8 * g + ((lane >> 2) & 3)) * 448 + (lane & 3) * 8 + 7 * wn * 32;
    const int wE = wrow + ((wn & 1) ? -32 * (g & 1) : 32 * (g & 1));
    const int wO = wrow + ((wn & 1) ? 32 * (g & 1) : -32 * (g & 1));
    bf16x8 xa[2], wa[7], xb[2], wb[7];
#define K1_READ(XF, WF, T, BUF)                                                              \
  do {                                                                                       \
    const char* xp_ = xbase + ((T) >> 1) * 128 + (((T)&1) ? xoO : xoE);                      \
    const char* wb_ = wring + (BUF)*K1_WST;                                                  \
    XF[0] = *(const bf16x8*)(xp_);                                                           \
    _Pragma("unroll") for (int nt = 0; nt < 7; ++nt) {                                       \
      const char* p_ = wb_ + ((nt & 1) ? wO : wE) + nt * 32;                                 \
      WF[nt] = tr_frag(p_, p_ + 4 * 448);                                                    \
    }                                                                                        \
    XF[1] = *(const bf16x8*)(xp_ + 16 * 896);                                                \
  } while (0)
#define K1_MMA(XF, WF)                                                                       \
  do {                                                                                       \
    _Pragma("unroll") for (int nt = 0; nt < 7; ++nt) acc[nt][0] = mfma16(WF[nt], XF[0], acc[nt][0]); \
    _Pragma("unroll") for (int nt = 0; nt < 7; ++nt) acc[nt][1] = mfma16(WF[nt], XF[1], acc[nt][1]); \
  } while (0)
    BAR();
    K1_READ(xa, wa, 0, 0);
    int rb = 1;
#pragma unroll 1
    for (int t = 0; t < 14; t += 2) {
      BAR();
      K1_READ(xb, wb, t + 1, rb);
      rb = (rb == 2) ? 0 : rb + 1;
      K1_MMA(xa, wa);
      BAR();
      if (t + 2 < 14) {
        K1_READ(xa, wa, t + 2, rb);
        rb = (rb == 2) ? 0 : rb + 1;
      }
      K1_MMA(xb, wb);
    }
  }
  __syncthreads();

  constexpr int TST = 272;
  constexpr int RST = 464;
  if (is_kv) {
    char* PK = sm;
    char* VT = sm + 4 * 64 * TST;
    char* WP = VT + 4 * 32 * TST;
    if (tid < 392) *(u32x4*)(WP + tid * 16) = wphi_raw;
    __syncthreads();
    if (wave < 8) {
      unsigned qp[7][2][2];
#pragma unroll
      for (int nt = 0; nt < 7; ++nt)
#pragma unroll
        for (int mt = 0; mt < 2; ++mt) {
          qp[nt][mt][0] = pk2(acc[nt][mt][0], acc[nt][mt][1]);
          qp[nt][mt][1] = pk2(acc[nt][mt][2], acc[nt][mt][3]);
        }
#pragma unroll
      for (int nt = 3; nt < 7; ++nt) {
        const int Q = 4 * nt + g - 14;
        if (Q >= 0) {
          const int hh = (Q >= 7) ? 1 : 0, d = 4 * (Q - 7 * hh);
#pragma unroll
          for (int mt = 0; mt < 2; ++mt) {
            char* d0 = VT + ((2 * wn + hh) * 32 + d) * TST + (32 * wm + 16 * mt + r16) * 2;
            *(u16*)(d0) = (u16)qp[nt][mt][0]; *(u16*)(d0 + TST) = (u16)(qp[nt][mt][0] >> 16);
            *(u16*)(d0 + 2 * TST) = (u16)qp[nt][mt][1]; *(u16*)(d0 + 3 * TST) = (u16)(qp[nt][mt][1] >> 16);
          }
        }
      }
      if (g < 2) {
        char* d0 = VT + ((2 * wn + g) * 32 + 28) * TST + (32 * wm + r16) * 2;
        *(u16*)(d0) = (u16)0x3F80;
        *(u16*)(d0 + 32) = (u16)0x3F80;
      }
#pragma unroll
      for (int hh = 0; hh < 2; ++hh) {
        const int u1 = (g + hh) & 3;
        const char* wp = WP + (2 * wn + hh) * 1568 + (4 * u1 + ((lane >> 2) & 3)) * 56 + (lane & 3) * 8;
        bf16x8 wp0 = tr_frag(wp, wp + 16 * 56);
        bf16x8 wp1 = tr_frag(wp + 32, wp + 16 * 56 + 32);
        if (u1 == 3) {
#pragma unroll
          for (int j = 4; j < 8; ++j) { wp0[j] = 0; wp1[j] = 0; }
        }
#pragma unroll
        for (int mt = 0; mt < 2; ++mt) {
          unsigned l0, l1, h0, h1;
          if (hh == 0) { l0 = qp[0][mt][0]; l1 = qp[0][mt][1]; h0 = qp[1][mt][0]; h1 = qp[1][mt][1]; }
          else {
            const bool c = (g == 3);
            l0 = c ? qp[1][mt][0] : qp[2][mt][0]; l1 = c ? qp[1][mt][1] : qp[2][mt][1];
            h0 = c ? qp[2][mt][0] : qp[3][mt][0]; h1 = c ? qp[2][mt][1] : qp[3][mt][1];
          }
          u32x4 qv = {l0, l1, h0, h1};
          unsigned pp[2][2], pm[2][2];
          phi_map(__builtin_bit_cast(bf16x8, qv), wp0, wp1, g, pp, pm);
          char* dst = PK + ((2 * wn + hh) * 64 + 4 * g) * TST + (32 * wm + 16 * mt + r16) * 2;
#pragma unroll
          for (int ft = 0; ft < 2; ++ft) {
            if (ft == 0 || g != 3) {
              char* d0 = dst + 16 * ft * TST;
              *(u16*)(d0) = (u16)pp[ft][0]; *(u16*)(d0 + TST) = (u16)(pp[ft][0] >> 16);
              *(u16*)(d0 + 2 * TST) = (u16)pp[ft][1]; *(u16*)(d0 + 3 * TST) = (u16)(pp[ft][1] >> 16);
              char* d1 = d0 + 28 * TST;
              *(u16*)(d1) = (u16)pm[ft][0]; *(u16*)(d1 + TST) = (u16)(pm[ft][0] >> 16);
              *(u16*)(d1 + 2 * TST) = (u16)pm[ft][1]; *(u16*)(d1 + 3 * TST) = (u16)(pm[ft][1] >> 16);
            }
          }
        }
      }
    }
    __syncthreads();
    if (wave < 8) {
      const int hh = wave >> 1, ftb = (wave & 1) * 2;
      f32x4 kacc[2][2];
#pragma unroll
      for (int a = 0; a < 2; ++a) { kacc[a][0] = f32x4{0.f, 0.f, 0.f, 0.f}; kacc[a][1] = f32x4{0.f, 0.f, 0.f, 0.f}; }
#pragma unroll
      for (int kk = 0; kk < 4; ++kk) {
        bf16x8 af[2], bfr[2];
#pragma unroll
        for (int a = 0; a < 2; ++a) {
          af[a] = *(const bf16x8*)(PK + (hh * 64 + 16 * (ftb + a) + r16) * TST + (32 * kk + 8 * g) * 2);
          bfr[a] = *(const bf16x8*)(VT + (hh * 32 + 16 * a + r16) * TST + (32 * kk + 8 * g) * 2);
        }
#pragma unroll
        for (int a = 0; a < 2; ++a)
#pragma unroll
          for (int c = 0; c < 2; ++c) kacc[a][c] = mfma16(af[a], bfr[c], kacc[a][c]);
      }
      float* kvp = (float*)(ws + WS_KVP) + (size_t)((rt * 16 + 4 * (cb - 2) + hh) * 56) * 32;
#pragma unroll
      for (int a = 0; a < 2; ++a)
#pragma unroll
        for (int c = 0; c < 2; ++c)
#pragma unroll
          for (int j = 0; j < 4; ++j) {
            const int f = 16 * (ftb + a) + 4 * g + j;
            if (f < 56) kvp[f * 32 + 16 * c + r16] = kacc[a][c][j];
          }
    }
  } else if (cb < 10) {
    if (wave < 8) {
#pragma unroll
      for (int mt = 0; mt < 2; ++mt) {
        char* dst = sm + (32 * wm + 16 * mt + r16) * RST + (112 * wn + 4 * g) * 2;
#pragma unroll
        for (int nt = 0; nt < 7; ++nt) {
          unsigned a = pk2(acc[nt][mt][0], acc[nt][mt][1]), c = pk2(acc[nt][mt][2], acc[nt][mt][3]);
          if (cb >= 6) { a = gelu_pk(a); c = gelu_pk(c); }
          *(u32x2*)(dst + 32 * nt) = u32x2{a, c};
        }
      }
    }
    __syncthreads();
    u16* dstb = (cb < 2) ? ((u16*)(ws + WS_Q) + (size_t)row0 * D_ + 224 * cb)
                         : ((u16*)(ws + WS_Z1) + (size_t)row0 * FH + 224 * (cb - 6));
    const int ld = (cb < 2) ? D_ : FH;
#pragma unroll
    for (int i = 0; i < 6; ++i) {
      const int id = tid + 640 * i;
      if (id < 128 * 28) {
        const int s = id / 28, c = id % 28;
        *(u32x4*)(dstb + (size_t)s * ld + c * 8) = *(const u32x4*)(sm + s * RST + c * 16);
      }
    }
  } else {
    if (wave < 8) {
#pragma unroll
      for (int mt = 0; mt < 2; ++mt) {
        unsigned zp[7][2];
        float s = 0.f;
#pragma unroll
        for (int nt = 0; nt < 7; ++nt) {
          zp[nt][0] = gelu_pk(pk2(acc[nt][mt][0], acc[nt][mt][1]));
          zp[nt][1] = gelu_pk(pk2(acc[nt][mt][2], acc[nt][mt][3]));
          s = dot2(zp[nt][0], 0x3F803F80u, s);
          s = dot2(zp[nt][1], 0x3F803F80u, s);
        }
        s = x32_add(x16_add(s));
        const float mean = s * (1.f / 112.f);
        float q = 0.f;
#pragma unroll
        for (int nt = 0; nt < 7; ++nt) {
          float a = lo_f(zp[nt][0]) - mean, c = hi_f(zp[nt][0]) - mean;
          float d = lo_f(zp[nt][1]) - mean, e = hi_f(zp[nt][1]) - mean;
          q += (a * a + c * c) + (d * d + e * e);
        }
        q = x32_add(x16_add(q));
        if (g == 0) {
          f32x2* st = (f32x2*)(ws + WS_STAT) + (size_t)(2 * (cb - 10) + wn) * S_ + row0 + 32 * wm + 16 * mt + r16;
          *st = f32x2{mean, q};
        }
#pragma unroll
        for (int nt = 0; nt < 7; ++nt) {
          char* d0 = sm + (112 * wn + 16 * nt + 4 * g) * TST + (32 * wm + 16 * mt + r16) * 2;
          *(u16*)(d0) = (u16)zp[nt][0]; *(u16*)(d0 + TST) = (u16)(zp[nt][0] >> 16);
          *(u16*)(d0 + 2 * TST) = (u16)zp[nt][1]; *(u16*)(d0 + 3 * TST) = (u16)(zp[nt][1] >> 16);
        }
      }
    }
    __syncthreads();
    u16* z2t = (u16*)(ws + WS_Z2T);
#pragma unroll
    for (int i = 0; i < 6; ++i) {
      const int id = tid + 640 * i;
      if (id < 224 * 16) {
        const int f = id >> 4, c = id & 15;
        *(u32x4*)(z2t + (size_t)(224 * (cb - 10) + f) * S_ + row0 + c * 8) = *(const u32x4*)(sm + f * TST + c * 16);
      }
    }
  }
}

__global__ __launch_bounds__(512) void k2_kernel(const u16* __restrict__ wphi, const u16* __restrict__ wsp,
                                                  char* __restrict__ ws) {
  __shared__ __attribute__((aligned(16))) char sm[32 * 144 + 2048];
  const int tid = threadIdx.x, lane = tid & 63, wave = tid >> 6;
  const int b = blockIdx.x;
  if (b < 128) {
    const int h = b & 15, rt = b >> 4;
    const int g = lane >> 4, r16 = lane & 15;
    f32x4 part[16];
    {
      const int te = (tid < 448) ? tid : 0;
      const float* kvp = (const float*)(ws + WS_KVP) + (size_t)h * 56 * 32 + te * 4;
#pragma unroll
      for (int p = 0; p < 16; ++p) part[p] = *(const f32x4*)(kvp + (size_t)p * 16 * 56 * 32);
    }
    u32x4 wraw = {0u, 0u, 0u, 0u};
    if (tid < 98) wraw = *(const u32x4*)(wphi + (size_t)h * 784 + tid * 8);
    const u16* qb = (const u16*)(ws + WS_Q);
    u32x2 qv[2][2];
#pragma unroll
    for (int st = 0; st < 2; ++st) {
      const u16* qp = qb + (size_t)(rt * 256 + wave * 32 + st * 16 + r16) * D_ + h * 28 + 8 * g;
      qv[st][0] = *(const u32x2*)(qp);
      qv[st][1] = *(const u32x2*)(qp + 4);
    }
    char* WP = sm + 32 * 144;
    if (tid < 98) *(u32x4*)(WP + tid * 16) = wraw;
    if (tid < 448) {
      f32x4 s = (part[0] + part[1]) + (part[2] + part[3]);
      s += (part[4] + part[5]) + (part[6] + part[7]);
      s += (part[8] + part[9]) + (part[10] + part[11]);
      s += (part[12] + part[13]) + (part[14] + part[15]);
      const int fp = tid >> 3, d0 = 4 * (tid & 7);
      const int mf = (fp >= 28) ? 1 : 0, f = fp - 28 * mf;
      const int pos = mf * 32 + ((f & 15) >> 2) * 8 + (f & 3) + 4 * (f >> 4);
      const unsigned a = pk2(s[0], s[1]), c = pk2(s[2], s[3]);
      char* d = sm + d0 * 144 + pos * 2;
      *(u16*)(d) = (u16)a; *(u16*)(d + 144) = (u16)(a >> 16);
      *(u16*)(d + 288) = (u16)c; *(u16*)(d + 432) = (u16)(c >> 16);
    } else {
      const int i = tid - 448, d = i >> 1, mf = i & 1;
      *(u32x2*)(sm + d * 144 + (mf * 32 + 28) * 2) = u32x2{0u, 0u};
    }
    __syncthreads();
    bf16x8 af[2][2];
#pragma unroll
    for (int dt = 0; dt < 2; ++dt)
#pragma unroll
      for (int mf = 0; mf < 2; ++mf) af[dt][mf] = *(const bf16x8*)(sm + (16 * dt + r16) * 144 + (32 * mf + 8 * g) * 2);
    const char* wp = WP + (8 * g + ((lane >> 2) & 3)) * 56 + (lane & 3) * 8;
    bf16x8 wp0 = tr_frag(wp, wp + 4 * 56);
    bf16x8 wp1 = tr_frag(wp + 32, wp + 4 * 56 + 32);
    if (g == 3) {
#pragma unroll
      for (int j = 4; j < 8; ++j) { wp0[j] = 0; wp1[j] = 0; }
    }
    u16* oh = (u16*)(ws + WS_OH);
#pragma unroll
    for (int st = 0; st < 2; ++st) {
      const int s = rt * 256 + wave * 32 + st * 16 + r16;
      u32x4 q4 = {qv[st][0][0], qv[st][0][1], qv[st][1][0], qv[st][1][1]};
      if (g == 3) { q4[2] = 0u; q4[3] = 0u; }
      unsigned pp[2][2], pm[2][2];
      phi_map(__builtin_bit_cast(bf16x8, q4), wp0, wp1, g, pp, pm);
      u32x4 b1 = {pp[0][0], pp[0][1], pp[1][0], pp[1][1]};
      u32x4 b2 = {pm[0][0], pm[0][1], pm[1][0], pm[1][1]};
      f32x4 a0 = {0.f, 0.f, 0.f, 0.f}, a1 = {0.f, 0.f, 0.f, 0.f};
      a0 = mfma16(af[0][0], __builtin_bit_cast(bf16x8, b1), a0);
      a1 = mfma16(af[1][0], __builtin_bit_cast(bf16x8, b1), a1);
      a0 = mfma16(af[0][1], __builtin_bit_cast(bf16x8, b2), a0);
      a1 = mfma16(af[1][1], __builtin_bit_cast(bf16x8, b2), a1);
      float den = __shfl(a1[0], 48 + r16, 64);
      den = rbf(rbf(den) + 1e-6f);
      const float inv = 1.f / den;
      u16* dst = oh + (size_t)s * KO + h * 28 + 4 * g;
      *(u32x2*)(dst) = u32x2{pk2(rbf(a0[0]) * inv, rbf(a0[1]) * inv), pk2(rbf(a0[2]) * inv, rbf(a0[3]) * inv)};
      if (g != 3)
        *(u32x2*)(dst + 16) = u32x2{pk2(rbf(a1[0]) * inv, rbf(a1[1]) * inv), pk2(rbf(a1[2]) * inv, rbf(a1[3]) * inv)};
    }
  } else if (b < 256) {
    const int bb = b - 128;
    const int ft = bb >> 4, stile = bb & 15;
    const u16* z2t = (const u16*)(ws + WS_Z2T);
    u16* z2n = (u16*)(ws + WS_Z2N);
    const int c = tid & 15;
    u32x4 v[4];
#pragma unroll
    for (int i = 0; i < 4; ++i) {
      int f = (tid >> 4) + 32 * i;
      if (f >= 112) f = tid >> 4;
      v[i] = *(const u32x4*)(z2t + (size_t)(ft * 112 + f) * S_ + stile * 128 + c * 8);
    }
    f32x2* ms = (f32x2*)sm;
    if (tid < 128) {
      const f32x2* st = (const f32x2*)(ws + WS_STAT) + stile * 128 + tid;
      f32x2 p[8];
#pragma unroll
      for (int j = 0; j < 8; ++j) p[j] = st[(size_t)j * S_];
      float m = 0.f;
#pragma unroll
      for (int j = 0; j < 8; ++j) m += p[j][0];
      m *= 0.125f;
      float q = 0.f;
#pragma unroll
      for (int j = 0; j < 8; ++j) { float d = p[j][0] - m; q += p[j][1] + 112.f * d * d; }
      const float mb = rbf(m);
      const float vb = rbf(q * (1.f / 896.f));
      const float sd = rbf(sqrtf(rbf(vb + 1e-5f)));
      ms[tid] = f32x2{mb, 1.f / sd};
    }
    __syncthreads();
    f32x2 st[8];
#pragma unroll
    for (int j = 0; j < 8; ++j) st[j] = ms[c * 8 + j];
#pragma unroll
    for (int i = 0; i < 4; ++i) {
      const int f = (tid >> 4) + 32 * i;
      if (f < 112) {
        u32x4 o;
#pragma unroll
        for (int j = 0; j < 4; ++j) {
          unsigned d = pk2(lo_f(v[i][j]) - st[2 * j][0], hi_f(v[i][j]) - st[2 * j + 1][0]);
          o[j] = pk2(lo_f(d) * st[2 * j][1], hi_f(d) * st[2 * j + 1][1]);
        }
        *(u32x4*)(z2n + (size_t)(ft * 112 + f) * S_ + stile * 128 + c * 8) = o;
      }
    }
  } else {
    const int bb = b - 256;
    const int xcd = bb & 7, part = bb >> 3;
    const char* p = (const char*)wsp + (size_t)(xcd >> 1) * (2u << 20) + (size_t)part * (256u << 10) + tid * 16;
    unsigned acc = 0;
#pragma unroll
    for (int i = 0; i < 32; ++i) {
      u32x4 v = *(const u32x4*)(p + i * 8192);
      acc |= v[0] & v[1] & v[2] & v[3];
    }
    asm volatile("" ::"v"(acc));
  }
}

constexpr int K3_STAGE = 176 * 256;
__global__ __launch_bounds__(768) void k3_kernel(const u16* __restrict__ wsp, const u16* __restrict__ bsp,
                                                  char* __restrict__ ws) {
  __shared__ __attribute__((aligned(16))) char sm[3 * K3_STAGE];
  const int tid = threadIdx.x, lane = tid & 63, wave = __builtin_amdgcn_readfirstlane(tid >> 6);
  const int b = blockIdx.x;
  const int xcd = b & 7, idx = b >> 3;
  const int mt_ = (xcd >> 1) * 8 + (idx & 7);
  const int nt_ = (xcd & 1) * 4 + (idx >> 3);
  const int t0 = mt_ * 64, f0 = nt_ * 112;
  const u16* z2n = (const u16*)(ws + WS_Z2N);
  const u16* z1 = (const u16*)(ws + WS_Z1);
  u16* oh = (u16*)(ws + WS_OH);
  constexpr int RS = 464;
  u32x4 zv[2];
  float bb[2];

  if (wave >= 8) {
    const int lw = wave - 8;
    const u16* src[11];
#pragma unroll
    for (int j = 0; j < 11; ++j) {
      const int i = lw + 4 * j;
      const int row = 4 * i + (lane >> 4);
      const int c = (lane & 15) ^ (row & 15);
      const u16* base = (row < 64) ? (wsp + (size_t)(t0 + row) * S_) : (z2n + (size_t)(f0 + row - 64) * S_);
      src[j] = base + c * 8;
    }
    char* dst0 = sm + lw * 1024;
#pragma unroll
    for (int s = 0; s < 3; ++s)
#pragma unroll
      for (int j = 0; j < 11; ++j) glds16(src[j] + s * 128, dst0 + s * K3_STAGE + j * 4096);
    asm volatile("s_waitcnt vmcnt(22)" ::: "memory");
    __builtin_amdgcn_s_barrier();
    int nb = 0;
    for (int t = 0; t < 16; ++t) {
      if (t + 2 < 16) asm volatile("s_waitcnt vmcnt(11)" ::: "memory");
      else asm volatile("s_waitcnt vmcnt(0)" ::: "memory");
      __builtin_amdgcn_s_barrier();
      if (t + 3 < 16) {
        char* dst = dst0 + nb * K3_STAGE;
#pragma unroll
        for (int j = 0; j < 11; ++j) glds16(src[j] + (t + 3) * 128, dst + j * 4096);
        nb = (nb == 2) ? 0 : nb + 1;
      }
    }
    {
      const int t = tid / 14, c = tid % 14;
      zv[0] = *(const u32x4*)(z1 + (size_t)(t0 + t) * FH + f0 + c * 8);
      bb[0] = bf2f(bsp[t0 + t]);
      zv[1] = zv[0]; bb[1] = bb[0];
    }
    __builtin_amdgcn_s_barrier();
  } else {
#pragma unroll
    for (int i = 0; i < 2; ++i) {
      int id = tid + 768 * i;
      if (id >= 896) id = tid;
      const int t = id / 14, c = id % 14;
      zv[i] = *(const u32x4*)(z1 + (size_t)(t0 + t) * FH + f0 + c * 8);
      bb[i] = bf2f(bsp[t0 + t]);
    }
    const int g = lane >> 4, r16 = lane & 15;
    const int kg = wave >> 2, wm = (wave >> 1) & 1, wn = (wave & 1) ^ kg;
    f32x4 acc[4][2];
#pragma unroll
    for (int n = 0; n < 4; ++n) { acc[n][0] = f32x4{0.f, 0.f, 0.f, 0.f}; acc[n][1] = f32x4{0.f, 0.f, 0.f, 0.f}; }
    const int co0 = ((8 * kg + g) ^ r16) << 4, co1 = ((8 * kg + 4 + g) ^ r16) << 4;
    const int arow = (32 * wm + r16) * 256;
    const int brow = (64 + 64 * wn + r16) * 256;
    bf16x8 aA[2][2], bA[2][4], aB[2][2], bB[2][4];
#define K3_READ(AF, BF, BUF)                                                                   \
  do {                                                                                         \
    const char* sb_ = sm + (BUF)*K3_STAGE;                                                     \
    _Pragma("unroll") for (int kk = 0; kk < 2; ++kk) {                                         \
      const int co_ = kk ? co1 : co0;                                                          \
      AF[kk][0] = *(const bf16x8*)(sb_ + arow + co_);                                          \
      _Pragma("unroll") for (int n = 0; n < 3; ++n) BF[kk][n] = *(const bf16x8*)(sb_ + brow + n * 4096 + co_); \
      AF[kk][1] = *(const bf16x8*)(sb_ + arow + 4096 + co_);                                   \
    }                                                                                          \
    if (wn == 0) {                                                                             \
      BF[0][3] = *(const bf16x8*)(sb_ + brow + 3 * 4096 + co0);                                \
      BF[1][3] = *(const bf16x8*)(sb_ + brow + 3 * 4096 + co1);                                \
    }                                                                                          \
  } while (0)
#define K3_MMA(AF, BF)                                                                         \
  do {                                                                                         \
    _Pragma("unroll") for (int kk = 0; kk < 2; ++kk) {                                         \
      _Pragma("unroll") for (int n = 0; n < 3; ++n) acc[n][0] = mfma16(BF[kk][n], AF[kk][0], acc[n][0]); \
      _Pragma("unroll") for (int n = 0; n < 3; ++n) acc[n][1] = mfma16(BF[kk][n], AF[kk][1], acc[n][1]); \
    }                                                                                          \
    if (wn == 0) {                                                                             \
      _Pragma("unroll") for (int kk = 0; kk < 2; ++kk) {                                       \
        acc[3][0] = mfma16(BF[kk][3], AF[kk][0], acc[3][0]);                                   \
        acc[3][1] = mfma16(BF[kk][3], AF[kk][1], acc[3][1]);                                   \
      }                                                                                        \
    }                                                                                          \
  } while (0)
    BAR();
    K3_READ(aA, bA, 0);
    int rb = 1;
#pragma unroll 1
    for (int t = 0; t < 16; t += 2) {
      BAR();
      K3_READ(aB, bB, rb);
      rb = (rb == 2) ? 0 : rb + 1;
      K3_MMA(aA, bA);
      BAR();
      if (t + 2 < 16) {
        K3_READ(aA, bA, rb);
        rb = (rb == 2) ? 0 : rb + 1;
      }
      K3_MMA(aB, bB);
    }
    BAR();
    char* rd = sm + kg * (64 * RS);
#pragma unroll
    for (int m = 0; m < 2; ++m)
#pragma unroll
      for (int n = 0; n < 4; ++n)
        if (n < 3 || wn == 0) *(f32x4*)(rd + (32 * wm + 16 * m + r16) * RS + (64 * wn + 16 * n + 4 * g) * 4) = acc[n][m];
  }
  __syncthreads();
#pragma unroll
  for (int i = 0; i < 2; ++i) {
    const int id = tid + 768 * i;
    if (id < 896) {
      const int t = id / 14, c = id % 14;
      const char* p = sm + t * RS + c * 32;
      f32x4 v0 = *(const f32x4*)(p) + *(const f32x4*)(p + 64 * RS);
      f32x4 v1 = *(const f32x4*)(p + 16) + *(const f32x4*)(p + 64 * RS + 16);
      u32x4 o;
      o[0] = pk2(lo_f(zv[i][0]) * rbf(rbf(v0[0]) + bb[i]), hi_f(zv[i][0]) * rbf(rbf(v0[1]) + bb[i]));
      o[1] = pk2(lo_f(zv[i][1]) * rbf(rbf(v0[2]) + bb[i]), hi_f(zv[i][1]) * rbf(rbf(v0[3]) + bb[i]));
      o[2] = pk2(lo_f(zv[i][2]) * rbf(rbf(v1[0]) + bb[i]), hi_f(zv[i][2]) * rbf(rbf(v1[1]) + bb[i]));
      o[3] = pk2(lo_f(zv[i][3]) * rbf(rbf(v1[2]) + bb[i]), hi_f(zv[i][3]) * rbf(rbf(v1[3]) + bb[i]));
      *(u32x4*)(oh + (size_t)(t0 + t) * KO + 448 + f0 + c * 8) = o;
    }
  }
}

constexpr int K4_STAGE = 144 * 128;
constexpr int K4_NB = 8;
__global__ __launch_bounds__(448) void k4_kernel(const u16* __restrict__ x, const char* __restrict__ ws,
                                                  u16* __restrict__ out) {
  __shared__ __attribute__((aligned(16))) char sm[K4_NB * K4_STAGE];
  const int tid = threadIdx.x, lane = tid & 63, wave = __builtin_amdgcn_readfirstlane(tid >> 6);
  const int b = blockIdx.x;
  const int xcd = b & 7, idx = b >> 3;
  const int mt_ = xcd * 8 + (idx & 7), nt_ = idx >> 3;
  const int t0 = mt_ * 32, n0 = nt_ * 112;
  const u16* oh = (const u16*)(ws + WS_OH);
  const u16* wt = (const u16*)(ws + WS_WT);
  constexpr int RS = 464;
  const int et = tid / 14, ec = tid % 14;
  u32x4 xv = *(const u32x4*)(x + (size_t)(t0 + et) * D_ + n0 + ec * 8);

  if (wave >= 4) {
    const int lw = wave - 4;
    const u16* src[6];
#pragma unroll
    for (int j = 0; j < 6; ++j) {
      const int i = lw + 3 * j;
      const int row = 8 * i + (lane >> 3);
      const int c = (lane & 7) ^ ((row >> 1) & 7);
      const u16* base = (row < 32) ? (oh + (size_t)(t0 + row) * KO) : (wt + (size_t)(n0 + row - 32) * KO);
      src[j] = base + c * 8;
    }
    char* dst0 = sm + lw * 1024;
#pragma unroll
    for (int s = 0; s < K4_NB - 1; ++s)
#pragma unroll
      for (int j = 0; j < 6; ++j) glds16(src[j] + s * 64, dst0 + s * K4_STAGE + j * 3072);
    int nb = K4_NB - 1;
    for (int t = 0; t < 21; ++t) {
      if (t + K4_NB - 1 <= 21) asm volatile("s_waitcnt vmcnt(36)" ::: "memory");
      else {
        const int rem = 20 - t;
        if (rem >= 5) asm volatile("s_waitcnt vmcnt(30)" ::: "memory");
        else if (rem == 4) asm volatile("s_waitcnt vmcnt(24)" ::: "memory");
        else if (rem == 3) asm volatile("s_waitcnt vmcnt(18)" ::: "memory");
        else if (rem == 2) asm volatile("s_waitcnt vmcnt(12)" ::: "memory");
        else if (rem == 1) asm volatile("s_waitcnt vmcnt(6)" ::: "memory");
        else asm volatile("s_waitcnt vmcnt(0)" ::: "memory");
      }
      __builtin_amdgcn_s_barrier();
      if (t + K4_NB - 1 < 21) {
        char* dst = dst0 + nb * K4_STAGE;
#pragma unroll
        for (int j = 0; j < 6; ++j) glds16(src[j] + (t + K4_NB - 1) * 64, dst + j * 3072);
        nb = (nb == K4_NB - 1) ? 0 : nb + 1;
      }
    }
    __builtin_amdgcn_s_barrier();
  } else {
    const int g = lane >> 4, r16 = lane & 15;
    const int kg = wave >> 1, wn = wave & 1;
    f32x4 acc[4][2];
#pragma unroll
    for (int n = 0; n < 4; ++n) { acc[n][0] = f32x4{0.f, 0.f, 0.f, 0.f}; acc[n][1] = f32x4{0.f, 0.f, 0.f, 0.f}; }
    const int co = ((4 * kg + g) ^ ((r16 >> 1) & 7)) << 4;
    const int arow = r16 * 128 + co;
    const int brow = (32 + 64 * wn + r16) * 128 + co;
    int cb = 0;
    for (int t = 0; t < 21; ++t) {
      BAR();
      const char* sb = sm + cb * K4_STAGE;
      cb = (cb == K4_NB - 1) ? 0 : cb + 1;
      bf16x8 a0 = *(const bf16x8*)(sb + arow);
      bf16x8 bf[4];
#pragma unroll
      for (int n = 0; n < 3; ++n) bf[n] = *(const bf16x8*)(sb + brow + n * 16 * 128);
      bf16x8 a1 = *(const bf16x8*)(sb + arow + 16 * 128);
      if (wn == 0) bf[3] = *(const bf16x8*)(sb + brow + 3 * 16 * 128);
#pragma unroll
      for (int n = 0; n < 3; ++n) acc[n][0] = mfma16(bf[n], a0, acc[n][0]);
#pragma unroll
      for (int n = 0; n < 3; ++n) acc[n][1] = mfma16(bf[n], a1, acc[n][1]);
      if (wn == 0) {
        acc[3][0] = mfma16(bf[3], a0, acc[3][0]);
        acc[3][1] = mfma16(bf[3], a1, acc[3][1]);
      }
    }
    BAR();
    char* rd = sm + kg * (32 * RS);
#pragma unroll
    for (int m = 0; m < 2; ++m)
#pragma unroll
      for (int n = 0; n < 4; ++n)
        if (n < 3 || wn == 0) *(f32x4*)(rd + (16 * m + r16) * RS + (64 * wn + 16 * n + 4 * g) * 4) = acc[n][m];
  }
  __syncthreads();
  {
    const char* p = sm + et * RS + ec * 32;
    f32x4 v0 = *(const f32x4*)(p) + *(const f32x4*)(p + 32 * RS);
    f32x4 v1 = *(const f32x4*)(p + 16) + *(const f32x4*)(p + 32 * RS + 16);
    u32x4 o;
    o[0] = pk2(lo_f(xv[0]) + v0[0], hi_f(xv[0]) + v0[1]);
    o[1] = pk2(lo_f(xv[1]) + v0[2], hi_f(xv[1]) + v0[3]);
    o[2] = pk2(lo_f(xv[2]) + v1[0], hi_f(xv[2]) + v1[1]);
    o[3] = pk2(lo_f(xv[3]) + v1[2], hi_f(xv[3]) + v1[3]);
    *(u32x4*)(out + (size_t)(t0 + et) * D_ + n0 + ec * 8) = o;
  }
}

extern "C" void kernel_launch(void* const* d_in, const int* in_sizes, int n_in, void* d_out, int out_size,
                              void* d_ws, size_t ws_size, hipStream_t stream) {
  const u16* x = (const u16*)d_in[0];
  const u16* wqkv = (const u16*)d_in[1];
  const u16* wphi = (const u16*)d_in[2];
  const u16* wo = (const u16*)d_in[3];
  const u16* win = (const u16*)d_in[5];
  const u16* wsp = (const u16*)d_in[6];
  const u16* bsp = (const u16*)d_in[7];
  const u16* wout = (const u16*)d_in[8];
  char* ws = (char*)d_ws;
  k1_kernel<<<245, 640, 0, stream>>>(x, wqkv, wphi, wo, win, wout, ws);
  k2_kernel<<<320, 512, 0, stream>>>(wphi, wsp, ws);
  k3_kernel<<<256, 768, 0, stream>>>(wsp, bsp, ws);
  k4_kernel<<<256, 448, 0, stream>>>(x, ws, (u16*)d_out);
}
```

```cpp
#include <hip/hip_runtime.h>
#include <hip/hip_bf16.h>
#include <stdint.h>

typedef __attribute__((ext_vector_type(8))) short bf16x8;
typedef __attribute__((ext_vector_type(4))) short s16x4;
typedef __attribute__((ext_vector_type(4))) float f32x4;
typedef __attribute__((ext_vector_type(2))) float f32x2;
typedef __attribute__((ext_vector_type(4))) unsigned u32x4;
typedef __attribute__((ext_vector_type(2))) unsigned u32x2;
typedef __attribute__((ext_vector_type(2))) __bf16 bf2_t;
typedef unsigned short u16;
#define LDS_AS __attribute__((address_space(3)))

constexpr int S_ = 2048, D_ = 448, NQKV = 1344, NIN = 1792, FH = 896, KO = 1344;

__device__ __forceinline__ unsigned pk2(float a, float b) {
  f32x2 v = {a, b};
  return __builtin_bit_cast(unsigned, __builtin_convertvector(v, bf2_t));
}
__device__ __forceinline__ float lo_f(unsigned u) { return __uint_as_float(u << 16); }
__device__ __forceinline__ float hi_f(unsigned u) { return __uint_as_float(u & 0xffff0000u); }
__device__ __forceinline__ float rbf(float a) { return lo_f(pk2(a, 0.f)); }
__device__ __forceinline__ float bf2f(u16 v) { return __uint_as_float(((unsigned)v) << 16); }
__device__ __forceinline__ float dot2(unsigned a, unsigned b, float c) {
  return __builtin_amdgcn_fdot2_f32_bf16(__builtin_bit_cast(bf2_t, a), __builtin_bit_cast(bf2_t, b), c, false);
}

__device__ __forceinline__ float x16_max(float v) {
  auto r = __builtin_amdgcn_permlane16_swap(__float_as_uint(v), __float_as_uint(v), false, false);
  return fmaxf(__uint_as_float(r[0]), __uint_as_float(r[1]));
}
__device__ __forceinline__ float x32_max(float v) {
  auto r = __builtin_amdgcn_permlane32_swap(__float_as_uint(v), __float_as_uint(v), false, false);
  return fmaxf(__uint_as_float(r[0]), __uint_as_float(r[1]));
}
__device__ __forceinline__ float x16_min(float v) {
  auto r = __builtin_amdgcn_permlane16_swap(__float_as_uint(v), __float_as_uint(v), false, false);
  return fminf(__uint_as_float(r[0]), __uint_as_float(r[1]));
}
__device__ __forceinline__ float x32_min(float v) {
  auto r = __builtin_amdgcn_permlane32_swap(__float_as_uint(v), __float_as_uint(v), false, false);
  return fminf(__uint_as_float(r[0]), __uint_as_float(r[1]));
}
__device__ __forceinline__ float x16_add(float v) {
  auto r = __builtin_amdgcn_permlane16_swap(__float_as_uint(v), __float_as_uint(v), false, false);
  return __uint_as_float(r[0]) + __uint_as_float(r[1]);
}
__device__ __forceinline__ float x32_add(float v) {
  auto r = __builtin_amdgcn_permlane32_swap(__float_as_uint(v), __float_as_uint(v), false, false);
  return __uint_as_float(r[0]) + __uint_as_float(r[1]);
}
__device__ __forceinline__ float red8_add(float v) {
  v += __uint_as_float(__builtin_amdgcn_mov_dpp(__float_as_uint(v), 0xB1, 0xf, 0xf, true));
  v += __uint_as_float(__builtin_amdgcn_mov_dpp(__float_as_uint(v), 0x4E, 0xf, 0xf, true));
  v += __uint_as_float(__builtin_amdgcn_mov_dpp(__float_as_uint(v), 0x141, 0xf, 0xf, true));
  return v;
}
__device__ __forceinline__ f32x4 mfma16(bf16x8 a, bf16x8 b, f32x4 c) {
  return __builtin_amdgcn_mfma_f32_16x16x32_bf16(a, b, c, 0, 0, 0);
}
__device__ __forceinline__ unsigned gelu_pk(unsigned p) {
  const f32x2 u = {lo_f(p), hi_f(p)};
  const f32x2 c1 = {-2.3022081986f, -2.3022081986f};
  const f32x2 c2 = {-0.1029432397f, -0.1029432397f};
  f32x2 t = u * u;
  f32x2 a = u * (t * c2 + c1);
  f32x2 d = {1.f + __builtin_amdgcn_exp2f(a[0]), 1.f + __builtin_amdgcn_exp2f(a[1])};
  f32x2 r = {__builtin_amdgcn_rcpf(d[0]), __builtin_amdgcn_rcpf(d[1])};
  f32x2 o = u * r;
  return pk2(o[0], o[1]);
}
#define SCHED() __builtin_amdgcn_sched_barrier(0)
#define BAR() do { SCHED(); asm volatile("" ::: "memory"); __builtin_amdgcn_s_waitcnt(0xC07F); __builtin_amdgcn_s_barrier(); asm volatile("" ::: "memory"); SCHED(); } while (0)
__device__ __forceinline__ void glds16(const void* g, char* l) {
  __builtin_amdgcn_global_load_lds((const unsigned*)g, (LDS_AS unsigned*)l, 16, 0, 0);
}
__device__ __forceinline__ bf16x8 tr_frag(const char* p0, const char* p1) {
  s16x4 a = __builtin_amdgcn_ds_read_tr16_b64_v4i16((LDS_AS s16x4*)p0);
  s16x4 c = __builtin_amdgcn_ds_read_tr16_b64_v4i16((LDS_AS s16x4*)p1);
  return __builtin_shufflevector(a, c, 0, 1, 2, 3, 4, 5, 6, 7);
}

constexpr int YLD = 3136;
constexpr size_t WS_Y = 0;
constexpr size_t WS_Z2N = WS_Y + (size_t)S_ * YLD * 2 + 256;
constexpr size_t WS_OH = WS_Z2N + (size_t)S_ * 896 * 2;
constexpr size_t WS_WT = WS_OH + (size_t)S_ * KO * 2;
constexpr size_t WS_KVP = WS_WT + (size_t)D_ * KO * 2;
constexpr size_t WS_END = WS_KVP + (size_t)4 * 16 * 56 * 32 * 4;

#define SGB(mask, n) __builtin_amdgcn_sched_group_barrier(mask, n, 0)

__device__ __forceinline__ void phi_map(bf16x8 qf, bf16x8 wp0, bf16x8 wp1, int g, unsigned (&pp)[2][2],
                                        unsigned (&pm)[2][2]) {
  f32x4 z = {0.f, 0.f, 0.f, 0.f};
  f32x4 p0 = mfma16(wp0, qf, z);
  f32x4 p1 = mfma16(wp1, qf, z);
  float v[8];
  {
    unsigned a = pk2(p0[0], p0[1]), b = pk2(p0[2], p0[3]), c = pk2(p1[0], p1[1]), d = pk2(p1[2], p1[3]);
    v[0] = lo_f(a); v[1] = hi_f(a); v[2] = lo_f(b); v[3] = hi_f(b);
    v[4] = lo_f(c); v[5] = hi_f(c); v[6] = lo_f(d); v[7] = hi_f(d);
  }
  const bool valid1 = (g != 3);
  if (!valid1) { v[4] = v[0]; v[5] = v[0]; v[6] = v[0]; v[7] = v[0]; }
  float mx = fmaxf(fmaxf(fmaxf(v[0], v[1]), fmaxf(v[2], v[3])), fmaxf(fmaxf(v[4], v[5]), fmaxf(v[6], v[7])));
  float mn = fminf(fminf(fminf(v[0], v[1]), fminf(v[2], v[3])), fminf(fminf(v[4], v[5]), fminf(v[6], v[7])));
  mx = x32_max(x16_max(mx));
  mn = x32_min(x16_min(mn));
  const float L2E = 1.4426950408889634f;
  const float mxl = mx * L2E, mnl = mn * L2E;
  float ep[8], em[8];
  float sp = 0.f, sm = 0.f;
#pragma unroll
  for (int j = 0; j < 8; ++j) {
    ep[j] = __builtin_amdgcn_exp2f(__builtin_fmaf(v[j], L2E, -mxl));
    em[j] = __builtin_amdgcn_exp2f(__builtin_fmaf(v[j], -L2E, mnl));
    if (j >= 4) { ep[j] = valid1 ? ep[j] : 0.f; em[j] = valid1 ? em[j] : 0.f; }
    sp += ep[j];
    sm += em[j];
  }
  sp = x32_add(x16_add(sp));
  sm = x32_add(x16_add(sm));
  const float rp = __builtin_amdgcn_rcpf(sp), rm = __builtin_amdgcn_rcpf(sm);
  pp[0][0] = pk2(ep[0] * rp, ep[1] * rp); pp[0][1] = pk2(ep[2] * rp, ep[3] * rp);
  pp[1][0] = pk2(ep[4] * rp, ep[5] * rp); pp[1][1] = pk2(ep[6] * rp, ep[7] * rp);
  pm[0][0] = pk2(em[0] * rm, em[1] * rm); pm[0][1] = pk2(em[2] * rm, em[3] * rm);
  pm[1][0] = pk2(em[4] * rm, em[5] * rm); pm[1][1] = pk2(em[6] * rm, em[7] * rm);
}

constexpr int K1_XB = 128 * 896;
constexpr int K1_WST = 32 * 448;
constexpr int K1_LDS = K1_XB + 3 * K1_WST;

__global__ __launch_bounds__(640) void k1_kernel(const u16* __restrict__ x, const u16* __restrict__ wqkv,
                                                  const u16* __restrict__ wo, const u16* __restrict__ win,
                                                  const u16* __restrict__ wout, const u16* __restrict__ wsp,
                                                  char* __restrict__ ws) {
  __shared__ __attribute__((aligned(16))) char sm[K1_LDS];
  const int tid = threadIdx.x, lane = tid & 63, wave = __builtin_amdgcn_readfirstlane(tid >> 6);
  const int b = blockIdx.x;

  if (b >= 224) {
    const int kb = b - 224;
    const int k0 = kb * 64;
    if (kb < 21) {
    u16* T = (u16*)sm;
    constexpr int TS = 456;
    u32x4 v[6];
#pragma unroll
    for (int j = 0; j < 6; ++j) {
      int i = tid + 640 * j;
      if (i >= 64 * 56) i = tid;
      const int r = i / 56, c = i % 56;
      const int k = k0 + r;
      const u16* src = (k < 448) ? (wo + (size_t)k * 448) : (wout + (size_t)(k - 448) * 448);
      v[j] = *(const u32x4*)(src + c * 8);
    }
#pragma unroll
    for (int j = 0; j < 6; ++j) {
      const int i = tid + 640 * j;
      if (i < 64 * 56) *(u32x4*)(T + (i / 56) * TS + (i % 56) * 8) = v[j];
    }
    __syncthreads();
    u16* wT = (u16*)(ws + WS_WT);
    for (int i = tid; i < 448 * 8; i += 640) {
      const int n = i >> 3, c = i & 7;
      u16 t[8];
#pragma unroll
      for (int j = 0; j < 8; ++j) t[j] = T[(c * 8 + j) * TS + n];
      u32x4 o;
#pragma unroll
      for (int j = 0; j < 4; ++j) o[j] = (unsigned)t[2 * j] | ((unsigned)t[2 * j + 1] << 16);
      *(u32x4*)(wT + (size_t)n * KO + k0 + c * 8) = o;
    }
    }
    {
      const char* p = (const char*)wsp + (size_t)kb * 262144 + tid * 16;
      unsigned a = 0;
#pragma unroll
      for (int i = 0; i < 25; ++i) {
        u32x4 v = *(const u32x4*)(p + i * 10240);
        a |= v[0] & v[1] & v[2] & v[3];
      }
      asm volatile("" ::"v"(a));
    }
    return;
  }

  const int xcd = b & 7, idx = b >> 3;
  const int cb = (xcd & 1) * 7 + idx % 7;
  const int rt = (xcd >> 1) * 4 + idx / 7;
  const int row0 = rt * 128;
  char* wring = sm + K1_XB;
  const int g = lane >> 4, r16 = lane & 15;
  const int wm = (wave >> 1) & 3, wn = wave & 1;
  constexpr int RST = 464;

  if (wave >= 8) {
    const int lw = wave - 8;
    const u16* W = (cb < 6) ? (wqkv + 224 * cb) : (win + 224 * (cb - 6));
    const int ldw = (cb < 6) ? NQKV : NIN;
    const u16* src[7];
#pragma unroll
    for (int j = 0; j < 7; ++j) {
      const int o = (lw + 2 * j) * 1024 + lane * 16;
      const int k = o / 448, cp = (o % 448) >> 4;
      const int G = (cp >> 1) ^ ((k >> 3) & 1);
      const int c = 2 * G + (cp & 1);
      src[j] = W + (size_t)k * ldw + 8 * c;
    }
    const size_t wstep = (size_t)32 * ldw;
    char* dst0 = wring + lw * 1024;
#pragma unroll
    for (int s = 0; s < 3; ++s)
#pragma unroll
      for (int j = 0; j < 7; ++j) glds16(src[j] + s * wstep, dst0 + s * K1_WST + j * 2048);
    asm volatile("s_waitcnt vmcnt(14)" ::: "memory");
    __builtin_amdgcn_s_barrier();
    int nb = 0;
    for (int t = 0; t < 14; ++t) {
      if (t + 2 < 14) asm volatile("s_waitcnt vmcnt(7)" ::: "memory");
      else asm volatile("s_waitcnt vmcnt(0)" ::: "memory");
      __builtin_amdgcn_s_barrier();
      if (t + 3 < 14) {
        char* dst = dst0 + nb * K1_WST;
#pragma unroll
        for (int j = 0; j < 7; ++j) glds16(src[j] + (t + 3) * wstep, dst + j * 2048);
        nb = (nb == 2) ? 0 : nb + 1;
      }
    }
    __builtin_amdgcn_s_barrier();
  } else {
    u32x4 xv[2][7];
    const int rr = lane >> 3, cc = lane & 7;
#pragma unroll
    for (int p = 0; p < 2; ++p) {
      const u16* xp = x + (size_t)(row0 + 16 * wave + 8 * p + rr) * D_ + cc * 8;
#pragma unroll
      for (int i = 0; i < 7; ++i) xv[p][i] = *(const u32x4*)(xp + 64 * i);
    }
#pragma unroll
    for (int p = 0; p < 2; ++p) {
      float s0 = 0.f, s1 = 0.f;
#pragma unroll
      for (int i = 0; i < 7; ++i) {
        s0 = dot2(xv[p][i][0], 0x3F803F80u, s0); s1 = dot2(xv[p][i][1], 0x3F803F80u, s1);
        s0 = dot2(xv[p][i][2], 0x3F803F80u, s0); s1 = dot2(xv[p][i][3], 0x3F803F80u, s1);
      }
      const float mean = red8_add(s0 + s1) * (1.f / 448.f);
      const float mb = rbf(mean);
      float q0 = 0.f, q1 = 0.f;
#pragma unroll
      for (int i = 0; i < 7; ++i)
#pragma unroll
        for (int j = 0; j < 4; ++j) {
          const unsigned d = pk2(lo_f(xv[p][i][j]) - mb, hi_f(xv[p][i][j]) - mb);
          xv[p][i][j] = d;
          if (j & 1) q1 = dot2(d, d, q1); else q0 = dot2(d, d, q0);
        }
      const float q = red8_add(q0 + q1);
      const float dm = mean - mb;
      const float var = fmaxf(q * (1.f / 448.f) - dm * dm, 0.f);
      const float sd = rbf(sqrtf(rbf(rbf(var) + 1e-5f)));
      const float inv = 1.f / sd;
      const int row = 16 * wave + 8 * p + rr;
      char* dst = sm + row * 896 + ((cc ^ ((row >> 1) & 7)) << 4);
#pragma unroll
      for (int i = 0; i < 7; ++i) {
        u32x4 o;
#pragma unroll
        for (int j = 0; j < 4; ++j) o[j] = pk2(lo_f(xv[p][i][j]) * inv, hi_f(xv[p][i][j]) * inv);
        *(u32x4*)(dst + 128 * i) = o;
      }
    }
    f32x4 acc[7][2];
#pragma unroll
    for (int nt = 0; nt < 7; ++nt) { acc[nt][0] = f32x4{0.f, 0.f, 0.f, 0.f}; acc[nt][1] = f32x4{0.f, 0.f, 0.f, 0.f}; }
    const int sw = (r16 >> 1) & 7;
    const char* xbase = sm + (32 * wm + r16) * 896;
    const int xoE = ((g ^ sw) << 4), xoO = (((4 | g) ^ sw) << 4);
    const int wrow = (8 * g + ((lane >> 2) & 3)) * 448 + (lane & 3) * 8 + 7 * wn * 32;
    const int wE = wrow + ((wn & 1) ? -32 * (g & 1) : 32 * (g & 1));
    const int wO = wrow + ((wn & 1) ? 32 * (g & 1) : -32 * (g & 1));
    bf16x8 xa[2], wa[7], xb[2], wb[7];
#define K1_READ(XF, WF, T, BUF)                                                              \
  do {                                                                                       \
    const char* xp_ = xbase + ((T) >> 1) * 128 + (((T)&1) ? xoO : xoE);                      \
    const char* wb_ = wring + (BUF)*K1_WST;                                                  \
    XF[0] = *(const bf16x8*)(xp_);                                                           \
    XF[1] = *(const bf16x8*)(xp_ + 16 * 896);                                                \
    _Pragma("unroll") for (int nt = 0; nt < 7; ++nt) {                                       \
      const char* p_ = wb_ + ((nt & 1) ? wO : wE) + nt * 32;                                 \
      WF[nt] = tr_frag(p_, p_ + 4 * 448);                                                    \
    }                                                                                        \
  } while (0)
#define K1_MMA(XF, WF)                                                                       \
  do {                                                                                       \
    _Pragma("unroll") for (int nt = 0; nt < 7; ++nt) acc[nt][0] = mfma16(WF[nt], XF[0], acc[nt][0]); \
    _Pragma("unroll") for (int nt = 0; nt < 7; ++nt) acc[nt][1] = mfma16(WF[nt], XF[1], acc[nt][1]); \
  } while (0)
#define K1_MIX()                                                                             \
  do {                                                                                       \
    SGB(0x100, 2);                                                                           \
    _Pragma("unroll") for (int i_ = 0; i_ < 14; ++i_) { SGB(0x008, 1); SGB(0x100, 1); }      \
  } while (0)
    BAR();
    K1_READ(xa, wa, 0, 0);
    int rb = 1;
#pragma unroll 1
    for (int t = 0; t < 12; t += 2) {
      BAR();
      K1_READ(xb, wb, t + 1, rb);
      rb = (rb == 2) ? 0 : rb + 1;
      K1_MMA(xa, wa);
      K1_MIX();
      BAR();
      K1_READ(xa, wa, t + 2, rb);
      rb = (rb == 2) ? 0 : rb + 1;
      K1_MMA(xb, wb);
      K1_MIX();
    }
    BAR();
    K1_READ(xb, wb, 13, 1);
    K1_MMA(xa, wa);
    K1_MIX();
    BAR();
    K1_MMA(xb, wb);
    BAR();
#pragma unroll
    for (int mt = 0; mt < 2; ++mt) {
      char* dst = sm + (32 * wm + 16 * mt + r16) * RST + (112 * wn + 4 * g) * 2;
#pragma unroll
      for (int nt = 0; nt < 7; ++nt)
        *(u32x2*)(dst + 32 * nt) = u32x2{pk2(acc[nt][mt][0], acc[nt][mt][1]), pk2(acc[nt][mt][2], acc[nt][mt][3])};
    }
  }
  __syncthreads();
  u16* dstb = (u16*)(ws + WS_Y) + (size_t)row0 * YLD + 224 * cb;
#pragma unroll
  for (int i = 0; i < 6; ++i) {
    const int id = tid + 640 * i;
    if (id < 128 * 28) {
      const int s = id / 28, c = id % 28;
      *(u32x4*)(dstb + (size_t)s * YLD + c * 8) = *(const u32x4*)(sm + s * RST + c * 16);
    }
  }
}

__device__ __forceinline__ float red64_add(float v) {
  v = red8_add(v);
  v += __uint_as_float(__builtin_amdgcn_mov_dpp(__float_as_uint(v), 0x140, 0xf, 0xf, true));
  return x32_add(x16_add(v));
}

constexpr int K2_PW = 64 * 80 + 32 * 64;
__global__ __launch_bounds__(512) void k2_kernel(const u16* __restrict__ wphi, char* __restrict__ ws) {
  __shared__ __attribute__((aligned(16))) char sm[8 * 56 * 32 * 4 + 2048];
  const int tid = threadIdx.x, lane = tid & 63, wave = __builtin_amdgcn_readfirstlane(tid >> 6);
  const int b = blockIdx.x;
  const u16* Y = (const u16*)(ws + WS_Y);
  if (b < 64) {
    const int h = b & 15, rg = b >> 4;
    const int g = lane >> 4, r16 = lane & 15;
    const int rowb = rg * 512 + wave * 64;
    u32x2 kv_[4][2];
#pragma unroll
    for (int st = 0; st < 4; ++st) {
      const u16* kp = Y + (size_t)(rowb + st * 16 + r16) * YLD + 448 + h * 28 + 8 * g;
      kv_[st][0] = *(const u32x2*)(kp);
      kv_[st][1] = *(const u32x2*)(kp + 4);
    }
    u32x2 vv[8];
#pragma unroll
    for (int i = 0; i < 8; ++i) {
      const int row = (lane >> 3) + 8 * i, pc = lane & 7;
      vv[i] = *(const u32x2*)(Y + (size_t)(rowb + row) * YLD + 896 + h * 28 + 4 * (pc < 7 ? pc : 0));
      if (pc == 7) vv[i] = u32x2{0x00003F80u, 0u};
    }
    u32x4 wraw = {0u, 0u, 0u, 0u};
    if (tid < 98) wraw = *(const u32x4*)(wphi + (size_t)h * 784 + tid * 8);
    char* WP = sm + 8 * 56 * 32 * 4;
    if (tid < 98) *(u32x4*)(WP + tid * 16) = wraw;
    char* PK = sm + wave * K2_PW;
    char* VS = PK + 64 * 80;
    __syncthreads();
    const char* wp = WP + (8 * g + ((lane >> 2) & 3)) * 56 + (lane & 3) * 8;
    bf16x8 wp0 = tr_frag(wp, wp + 4 * 56);
    bf16x8 wp1 = tr_frag(wp + 32, wp + 4 * 56 + 32);
    if (g == 3) {
#pragma unroll
      for (int j = 4; j < 8; ++j) { wp0[j] = 0; wp1[j] = 0; }
    }
    f32x4 kacc[4][2];
#pragma unroll
    for (int a = 0; a < 4; ++a) { kacc[a][0] = f32x4{0.f, 0.f, 0.f, 0.f}; kacc[a][1] = f32x4{0.f, 0.f, 0.f, 0.f}; }
#pragma unroll
    for (int pr = 0; pr < 2; ++pr) {
#pragma unroll
      for (int i = 0; i < 4; ++i) {
        const int row = (lane >> 3) + 8 * i;
        const int slot = 2 * (row & 15) + (row >> 4);
        *(u32x2*)(VS + slot * 64 + (lane & 7) * 8) = vv[4 * pr + i];
      }
      unsigned pp[2][2][2], pm[2][2][2];
#pragma unroll
      for (int mt = 0; mt < 2; ++mt) {
        u32x4 q4 = {kv_[2 * pr + mt][0][0], kv_[2 * pr + mt][0][1], kv_[2 * pr + mt][1][0], kv_[2 * pr + mt][1][1]};
        if (g == 3) { q4[2] = 0u; q4[3] = 0u; }
        phi_map(__builtin_bit_cast(bf16x8, q4), wp0, wp1, g, pp[mt], pm[mt]);
      }
#pragma unroll
      for (int ft = 0; ft < 2; ++ft) {
        if (ft == 0 || g != 3) {
          char* d0 = PK + (16 * ft + 4 * g) * 80 + r16 * 4;
#pragma unroll
          for (int j = 0; j < 2; ++j) {
            *(unsigned*)(d0 + (2 * j) * 80) = (pp[0][ft][j] & 0xffffu) | (pp[1][ft][j] << 16);
            *(unsigned*)(d0 + (2 * j + 1) * 80) = (pp[0][ft][j] >> 16) | (pp[1][ft][j] & 0xffff0000u);
            *(unsigned*)(d0 + (28 + 2 * j) * 80) = (pm[0][ft][j] & 0xffffu) | (pm[1][ft][j] << 16);
            *(unsigned*)(d0 + (28 + 2 * j + 1) * 80) = (pm[0][ft][j] >> 16) | (pm[1][ft][j] & 0xffff0000u);
          }
        }
      }
      __builtin_amdgcn_s_waitcnt(0xC07F);
      __builtin_amdgcn_wave_barrier();
      bf16x8 bfr[2];
      {
        const char* vp = VS + (8 * g + ((lane >> 2) & 3)) * 64 + (lane & 3) * 8;
        bfr[0] = tr_frag(vp, vp + 4 * 64);
        bfr[1] = tr_frag(vp + 32, vp + 4 * 64 + 32);
      }
#pragma unroll
      for (int a = 0; a < 4; ++a) {
        bf16x8 af = *(const bf16x8*)(PK + (16 * a + r16) * 80 + g * 16);
        kacc[a][0] = mfma16(af, bfr[0], kacc[a][0]);
        kacc[a][1] = mfma16(af, bfr[1], kacc[a][1]);
      }
      __builtin_amdgcn_s_waitcnt(0xC07F);
      __builtin_amdgcn_wave_barrier();
    }
    __syncthreads();
    float* red = (float*)sm + wave * (56 * 32);
#pragma unroll
    for (int a = 0; a < 4; ++a)
#pragma unroll
      for (int c = 0; c < 2; ++c)
#pragma unroll
        for (int j = 0; j < 4; ++j) {
          const int f = 16 * a + 4 * g + j;
          if (f < 56) red[f * 32 + 16 * c + r16] = kacc[a][c][j];
        }
    __syncthreads();
    if (tid < 448) {
      const f32x4* rp = (const f32x4*)sm + tid;
      f32x4 s = (rp[0] + rp[448]) + (rp[2 * 448] + rp[3 * 448]);
      s += (rp[4 * 448] + rp[5 * 448]) + (rp[6 * 448] + rp[7 * 448]);
      *((f32x4*)(ws + WS_KVP) + (size_t)(rg * 16 + h) * 448 + tid) = s;
    }
  } else {
    const int row0 = (b - 64) * 16 + wave * 2;
    u16* z2n = (u16*)(ws + WS_Z2N);
    const int le = (lane < 56) ? lane : 0;
    u32x4 v[2][2];
#pragma unroll
    for (int r = 0; r < 2; ++r)
#pragma unroll
      for (int c = 0; c < 2; ++c) v[r][c] = *(const u32x4*)(Y + (size_t)(row0 + r) * YLD + 2240 + (le + 56 * c) * 8);
#pragma unroll
    for (int r = 0; r < 2; ++r) {
      float s = 0.f;
#pragma unroll
      for (int c = 0; c < 2; ++c)
#pragma unroll
        for (int j = 0; j < 4; ++j) {
          v[r][c][j] = gelu_pk(v[r][c][j]);
          s = dot2(v[r][c][j], 0x3F803F80u, s);
        }
      if (lane >= 56) s = 0.f;
      const float mean = red64_add(s) * (1.f / 896.f);
      const float mb = rbf(mean);
      float q = 0.f;
#pragma unroll
      for (int c = 0; c < 2; ++c)
#pragma unroll
        for (int j = 0; j < 4; ++j) {
          const float a = lo_f(v[r][c][j]) - mean, e = hi_f(v[r][c][j]) - mean;
          q += a * a + e * e;
          v[r][c][j] = pk2(lo_f(v[r][c][j]) - mb, hi_f(v[r][c][j]) - mb);
        }
      if (lane >= 56) q = 0.f;
      const float var = red64_add(q) * (1.f / 896.f);
      const float sd = rbf(sqrtf(rbf(rbf(var) + 1e-5f)));
      const float inv = 1.f / sd;
      if (lane < 56) {
#pragma unroll
        for (int c = 0; c < 2; ++c) {
          u32x4 o;
#pragma unroll
          for (int j = 0; j < 4; ++j) o[j] = pk2(lo_f(v[r][c][j]) * inv, hi_f(v[r][c][j]) * inv);
          *(u32x4*)(z2n + (size_t)(row0 + r) * FH + (lane + 56 * c) * 8) = o;
        }
      }
    }
  }
}

constexpr int K3_STAGE = 64 * 256 + 128 * 224;
constexpr int K3_AUX = 3 * K3_STAGE;
__global__ __launch_bounds__(768) void k3_kernel(const u16* __restrict__ wsp, const u16* __restrict__ bsp,
                                                  const u16* __restrict__ wphi, char* __restrict__ ws) {
  __shared__ __attribute__((aligned(16))) char sm[3 * K3_STAGE + 32 * 144 + 2048];
  const int tid = threadIdx.x, lane = tid & 63, wave = __builtin_amdgcn_readfirstlane(tid >> 6);
  const int b = blockIdx.x;
  const int xcd = b & 7, idx = b >> 3;
  const int mt_ = (xcd >> 1) * 8 + (idx & 7);
  const int nt_ = (xcd & 1) * 4 + (idx >> 3);
  const int t0 = mt_ * 64, f0 = nt_ * 112;
  const u16* z2n = (const u16*)(ws + WS_Z2N);
  const u16* Y = (const u16*)(ws + WS_Y);
  u16* oh = (u16*)(ws + WS_OH);
  constexpr int RS = 464;
  u32x4 zv[2];
  float bb[2];

  if (wave >= 8) {
    const int lw = wave - 8;
    const u16* src[11];
    int step[11];
#pragma unroll
    for (int j = 0; j < 11; ++j) {
      const int i = lw + 4 * j;
      if (j < 4) {
        const int row = 4 * i + (lane >> 4);
        const int c = (lane & 15) ^ (row & 15);
        src[j] = wsp + (size_t)(t0 + row) * S_ + c * 8;
        step[j] = 128;
      } else {
        const int o = (i - 16) * 1024 + lane * 16;
        const int p = o / 224, c = (o % 224) >> 4;
        const int k = (p & ~31) + 8 * ((p >> 2) & 3) + 4 * ((p >> 4) & 1) + (p & 3);
        src[j] = z2n + (size_t)k * FH + f0 + c * 8;
        step[j] = 128 * FH;
      }
    }
    char* dst0 = sm + lw * 1024;
#pragma unroll
    for (int s = 0; s < 3; ++s)
#pragma unroll
      for (int j = 0; j < 11; ++j) glds16(src[j] + s * step[j], dst0 + s * K3_STAGE + j * 4096);
    __builtin_amdgcn_s_barrier();
    asm volatile("s_waitcnt vmcnt(22)" ::: "memory");
    __builtin_amdgcn_s_barrier();
    int nb = 0;
    for (int t = 0; t < 16; ++t) {
      if (t + 2 < 16) asm volatile("s_waitcnt vmcnt(11)" ::: "memory");
      else asm volatile("s_waitcnt vmcnt(0)" ::: "memory");
      __builtin_amdgcn_s_barrier();
      if (t + 3 < 16) {
        char* dst = dst0 + nb * K3_STAGE;
#pragma unroll
        for (int j = 0; j < 11; ++j) glds16(src[j] + (t + 3) * step[j], dst + j * 4096);
        nb = (nb == 2) ? 0 : nb + 1;
      }
    }
    {
      const int t = tid / 14, c = tid % 14;
      zv[0] = *(const u32x4*)(Y + (size_t)(t0 + t) * YLD + 1344 + f0 + c * 8);
      bb[0] = bf2f(bsp[t0 + t]);
      zv[1] = zv[0]; bb[1] = bb[0];
    }
    __builtin_amdgcn_s_barrier();
  } else {
    const int g = lane >> 4, r16 = lane & 15;
    {
      const int h = b & 15, srow = (b >> 4) * 128 + wave * 16 + r16;
      f32x4 part[4];
      {
        const int te = (tid < 448) ? tid : 0;
        const f32x4* kvp = (const f32x4*)(ws + WS_KVP) + (size_t)h * 448 + te;
#pragma unroll
        for (int p = 0; p < 4; ++p) part[p] = kvp[(size_t)p * 16 * 448];
      }
      u32x4 wraw = {0u, 0u, 0u, 0u};
      if (tid < 98) wraw = *(const u32x4*)(wphi + (size_t)h * 784 + tid * 8);
      const u16* qp = Y + (size_t)srow * YLD + h * 28 + 8 * g;
      const u32x2 q0 = *(const u32x2*)(qp), q1 = *(const u32x2*)(qp + 4);
      char* KT = sm + K3_AUX;
      char* WP = KT + 32 * 144;
      if (tid < 98) *(u32x4*)(WP + tid * 16) = wraw;
      if (tid < 448) {
        const f32x4 s = (part[0] + part[1]) + (part[2] + part[3]);
        const int fp = tid >> 3, d0 = 4 * (tid & 7);
        const int mf = (fp >= 28) ? 1 : 0, f = fp - 28 * mf;
        const int pos = mf * 32 + ((f & 15) >> 2) * 8 + (f & 3) + 4 * (f >> 4);
        const unsigned a = pk2(s[0], s[1]), c = pk2(s[2], s[3]);
        char* d = KT + d0 * 144 + pos * 2;
        *(u16*)(d) = (u16)a; *(u16*)(d + 144) = (u16)(a >> 16);
        *(u16*)(d + 288) = (u16)c; *(u16*)(d + 432) = (u16)(c >> 16);
      } else {
        const int i = tid - 448, d = i >> 1, mf = i & 1;
        *(u32x2*)(KT + d * 144 + (mf * 32 + 28) * 2) = u32x2{0u, 0u};
      }
      BAR();
      bf16x8 af[2][2];
#pragma unroll
      for (int dt = 0; dt < 2; ++dt)
#pragma unroll
        for (int mf = 0; mf < 2; ++mf) af[dt][mf] = *(const bf16x8*)(KT + (16 * dt + r16) * 144 + (32 * mf + 8 * g) * 2);
      const char* wp = WP + (8 * g + ((lane >> 2) & 3)) * 56 + (lane & 3) * 8;
      bf16x8 wp0 = tr_frag(wp, wp + 4 * 56);
      bf16x8 wp1 = tr_frag(wp + 32, wp + 4 * 56 + 32);
      if (g == 3) {
#pragma unroll
        for (int j = 4; j < 8; ++j) { wp0[j] = 0; wp1[j] = 0; }
      }
      u32x4 q4 = {q0[0], q0[1], q1[0], q1[1]};
      if (g == 3) { q4[2] = 0u; q4[3] = 0u; }
      unsigned pp[2][2], pm[2][2];
      phi_map(__builtin_bit_cast(bf16x8, q4), wp0, wp1, g, pp, pm);
      u32x4 b1 = {pp[0][0], pp[0][1], pp[1][0], pp[1][1]};
      u32x4 b2 = {pm[0][0], pm[0][1], pm[1][0], pm[1][1]};
      f32x4 a0 = {0.f, 0.f, 0.f, 0.f}, a1 = {0.f, 0.f, 0.f, 0.f};
      a0 = mfma16(af[0][0], __builtin_bit_cast(bf16x8, b1), a0);
      a1 = mfma16(af[1][0], __builtin_bit_cast(bf16x8, b1), a1);
      a0 = mfma16(af[0][1], __builtin_bit_cast(bf16x8, b2), a0);
      a1 = mfma16(af[1][1], __builtin_bit_cast(bf16x8, b2), a1);
      float den = __shfl(a1[0], 48 + r16, 64);
      den = rbf(rbf(den) + 1e-6f);
      const float inv = 1.f / den;
      u16* dst = oh + (size_t)srow * KO + h * 28 + 4 * g;
      *(u32x2*)(dst) = u32x2{pk2(rbf(a0[0]) * inv, rbf(a0[1]) * inv), pk2(rbf(a0[2]) * inv, rbf(a0[3]) * inv)};
      if (g != 3)
        *(u32x2*)(dst + 16) = u32x2{pk2(rbf(a1[0]) * inv, rbf(a1[1]) * inv), pk2(rbf(a1[2]) * inv, rbf(a1[3]) * inv)};
    }
#pragma unroll
    for (int i = 0; i < 2; ++i) {
      int id = tid + 768 * i;
      if (id >= 896) id = tid;
      const int t = id / 14, c = id % 14;
      zv[i] = *(const u32x4*)(Y + (size_t)(t0 + t) * YLD + 1344 + f0 + c * 8);
      bb[i] = bf2f(bsp[t0 + t]);
    }
    const int kg = wave >> 2, wm = (wave >> 1) & 1, wn = (wave & 1) ^ kg;
    f32x4 acc[4][2];
#pragma unroll
    for (int n = 0; n < 4; ++n) { acc[n][0] = f32x4{0.f, 0.f, 0.f, 0.f}; acc[n][1] = f32x4{0.f, 0.f, 0.f, 0.f}; }
    const int co0 = ((8 * kg + g) ^ r16) << 4, co1 = ((8 * kg + 4 + g) ^ r16) << 4;
    const int arow = (32 * wm + r16) * 256;
    const int brow = 64 * 256 + (64 * kg + 4 * g + ((lane >> 2) & 3)) * 224 + (lane & 3) * 8 + 128 * wn;
#define K3_READ(AF, BF, BUF, NT)                                                               \
  do {                                                                                         \
    const char* sb_ = sm + (BUF)*K3_STAGE;                                                     \
    _Pragma("unroll") for (int kk = 0; kk < 2; ++kk) {                                         \
      const int co_ = kk ? co1 : co0;                                                          \
      AF[kk][0] = *(const bf16x8*)(sb_ + arow + co_);                                          \
      AF[kk][1] = *(const bf16x8*)(sb_ + arow + 4096 + co_);                                   \
    }                                                                                          \
    _Pragma("unroll") for (int kk = 0; kk < 2; ++kk) {                                         \
      _Pragma("unroll") for (int n = 0; n < NT; ++n) {                                         \
        const char* p_ = sb_ + brow + kk * 32 * 224 + n * 32;                                  \
        BF[kk][n] = tr_frag(p_, p_ + 16 * 224);                                                \
      }                                                                                        \
    }                                                                                          \
  } while (0)
#define K3_MMA(AF, BF, NT)                                                                     \
  do {                                                                                         \
    _Pragma("unroll") for (int kk = 0; kk < 2; ++kk) {                                         \
      _Pragma("unroll") for (int n = 0; n < NT; ++n) acc[n][0] = mfma16(BF[kk][n], AF[kk][0], acc[n][0]); \
      _Pragma("unroll") for (int n = 0; n < NT; ++n) acc[n][1] = mfma16(BF[kk][n], AF[kk][1], acc[n][1]); \
    }                                                                                          \
  } while (0)
#define K3_MIX(NT)                                                                             \
  do {                                                                                         \
    SGB(0x100, 4);                                                                             \
    _Pragma("unroll") for (int i_ = 0; i_ < 4 * NT; ++i_) { SGB(0x008, 1); SGB(0x100, 1); }    \
  } while (0)
#define K3_LOOP(NT)                                                                            \
  do {                                                                                         \
    bf16x8 aA[2][2], bA[2][NT], aB[2][2], bB[2][NT];                                           \
    BAR();                                                                                     \
    K3_READ(aA, bA, 0, NT);                                                                    \
    int rb = 1;                                                                                \
    _Pragma("unroll 1") for (int t = 0; t < 14; t += 2) {                                      \
      BAR();                                                                                   \
      K3_READ(aB, bB, rb, NT);                                                                 \
      rb = (rb == 2) ? 0 : rb + 1;                                                             \
      K3_MMA(aA, bA, NT);                                                                      \
      K3_MIX(NT);                                                                              \
      BAR();                                                                                   \
      K3_READ(aA, bA, rb, NT);                                                                 \
      rb = (rb == 2) ? 0 : rb + 1;                                                             \
      K3_MMA(aB, bB, NT);                                                                      \
      K3_MIX(NT);                                                                              \
    }                                                                                          \
    BAR();                                                                                     \
    K3_READ(aB, bB, 0, NT);                                                                    \
    K3_MMA(aA, bA, NT);                                                                        \
    K3_MIX(NT);                                                                                \
    BAR();                                                                                     \
    K3_MMA(aB, bB, NT);                                                                        \
  } while (0)
    if (wn == 0) K3_LOOP(4);
    else K3_LOOP(3);
    BAR();
    char* rd = sm + kg * (64 * RS);
#pragma unroll
    for (int m = 0; m < 2; ++m)
#pragma unroll
      for (int n = 0; n < 4; ++n)
        if (n < 3 || wn == 0) *(f32x4*)(rd + (32 * wm + 16 * m + r16) * RS + (64 * wn + 16 * n + 4 * g) * 4) = acc[n][m];
  }
  __syncthreads();
#pragma unroll
  for (int i = 0; i < 2; ++i) {
    const int id = tid + 768 * i;
    if (id < 896) {
      const int t = id / 14, c = id % 14;
      const char* p = sm + t * RS + c * 32;
      f32x4 v0 = *(const f32x4*)(p) + *(const f32x4*)(p + 64 * RS);
      f32x4 v1 = *(const f32x4*)(p + 16) + *(const f32x4*)(p + 64 * RS + 16);
      u32x4 z;
#pragma unroll
      for (int j = 0; j < 4; ++j) z[j] = gelu_pk(zv[i][j]);
      u32x4 o;
      o[0] = pk2(lo_f(z[0]) * rbf(rbf(v0[0]) + bb[i]), hi_f(z[0]) * rbf(rbf(v0[1]) + bb[i]));
      o[1] = pk2(lo_f(z[1]) * rbf(rbf(v0[2]) + bb[i]), hi_f(z[1]) * rbf(rbf(v0[3]) + bb[i]));
      o[2] = pk2(lo_f(z[2]) * rbf(rbf(v1[0]) + bb[i]), hi_f(z[2]) * rbf(rbf(v1[1]) + bb[i]));
      o[3] = pk2(lo_f(z[3]) * rbf(rbf(v1[2]) + bb[i]), hi_f(z[3]) * rbf(rbf(v1[3]) + bb[i]));
      *(u32x4*)(oh + (size_t)(t0 + t) * KO + 448 + f0 + c * 8) = o;
    }
  }
}

constexpr int K4_STAGE = 144 * 128;
constexpr int K4_NB = 8;
__global__ __launch_bounds__(448) void k4_kernel(const u16* __restrict__ x, const char* __restrict__ ws,
                                                  u16* __restrict__ out) {
  __shared__ __attribute__((aligned(16))) char sm[K4_NB * K4_STAGE];
  const int tid = threadIdx.x, lane = tid & 63, wave = __builtin_amdgcn_readfirstlane(tid >> 6);
  const int b = blockIdx.x;
  const int xcd = b & 7, idx = b >> 3;
  const int mt_ = xcd * 8 + (idx & 7), nt_ = idx >> 3;
  const int t0 = mt_ * 32, n0 = nt_ * 112;
  const u16* oh = (const u16*)(ws + WS_OH);
  const u16* wt = (const u16*)(ws + WS_WT);
  constexpr int RS = 464;
  const int et = tid / 14, ec = tid % 14;
  u32x4 xv = *(const u32x4*)(x + (size_t)(t0 + et) * D_ + n0 + ec * 8);

  if (wave >= 4) {
    const int lw = wave - 4;
    const u16* src[6];
#pragma unroll
    for (int j = 0; j < 6; ++j) {
      const int i = lw + 3 * j;
      const int row = 8 * i + (lane >> 3);
      const int c = (lane & 7) ^ ((row >> 1) & 7);
      const u16* base = (row < 32) ? (oh + (size_t)(t0 + row) * KO) : (wt + (size_t)(n0 + row - 32) * KO);
      src[j] = base + c * 8;
    }
    char* dst0 = sm + lw * 1024;
#pragma unroll
    for (int s = 0; s < K4_NB - 1; ++s)
#pragma unroll
      for (int j = 0; j < 6; ++j) glds16(src[j] + s * 64, dst0 + s * K4_STAGE + j * 3072);
    int nb = K4_NB - 1;
    for (int t = 0; t < 21; ++t) {
      if (t + K4_NB - 1 <= 21) asm volatile("s_waitcnt vmcnt(36)" ::: "memory");
      else {
        const int rem = 20 - t;
        if (rem >= 5) asm volatile("s_waitcnt vmcnt(30)" ::: "memory");
        else if (rem == 4) asm volatile("s_waitcnt vmcnt(24)" ::: "memory");
        else if (rem == 3) asm volatile("s_waitcnt vmcnt(18)" ::: "memory");
        else if (rem == 2) asm volatile("s_waitcnt vmcnt(12)" ::: "memory");
        else if (rem == 1) asm volatile("s_waitcnt vmcnt(6)" ::: "memory");
        else asm volatile("s_waitcnt vmcnt(0)" ::: "memory");
      }
      __builtin_amdgcn_s_barrier();
      if (t + K4_NB - 1 < 21) {
        char* dst = dst0 + nb * K4_STAGE;
#pragma unroll
        for (int j = 0; j < 6; ++j) glds16(src[j] + (t + K4_NB - 1) * 64, dst + j * 3072);
        nb = (nb == K4_NB - 1) ? 0 : nb + 1;
      }
    }
    __builtin_amdgcn_s_barrier();
  } else {
    const int g = lane >> 4, r16 = lane & 15;
    const int kg = wave >> 1, wn = wave & 1;
    f32x4 acc[4][2];
#pragma unroll
    for (int n = 0; n < 4; ++n) { acc[n][0] = f32x4{0.f, 0.f, 0.f, 0.f}; acc[n][1] = f32x4{0.f, 0.f, 0.f, 0.f}; }
    const int co = ((4 * kg + g) ^ ((r16 >> 1) & 7)) << 4;
    const int arow = r16 * 128 + co;
    const int brow = (32 + 64 * wn + r16) * 128 + co;
    int cb = 0;
    for (int t = 0; t < 21; ++t) {
      BAR();
      const char* sb = sm + cb * K4_STAGE;
      cb = (cb == K4_NB - 1) ? 0 : cb + 1;
      bf16x8 a0 = *(const bf16x8*)(sb + arow);
      bf16x8 bf[4];
#pragma unroll
      for (int n = 0; n < 3; ++n) bf[n] = *(const bf16x8*)(sb + brow + n * 16 * 128);
      bf16x8 a1 = *(const bf16x8*)(sb + arow + 16 * 128);
      if (wn == 0) bf[3] = *(const bf16x8*)(sb + brow + 3 * 16 * 128);
#pragma unroll
      for (int n = 0; n < 3; ++n) acc[n][0] = mfma16(bf[n], a0, acc[n][0]);
#pragma unroll
      for (int n = 0; n < 3; ++n) acc[n][1] = mfma16(bf[n], a1, acc[n][1]);
      if (wn == 0) {
        acc[3][0] = mfma16(bf[3], a0, acc[3][0]);
        acc[3][1] = mfma16(bf[3], a1, acc[3][1]);
      }
    }
    BAR();
    char* rd = sm + kg * (32 * RS);
#pragma unroll
    for (int m = 0; m < 2; ++m)
#pragma unroll
      for (int n = 0; n < 4; ++n)
        if (n < 3 || wn == 0) *(f32x4*)(rd + (16 * m + r16) * RS + (64 * wn + 16 * n + 4 * g) * 4) = acc[n][m];
  }
  __syncthreads();
  {
    const char* p = sm + et * RS + ec * 32;
    f32x4 v0 = *(const f32x4*)(p) + *(const f32x4*)(p + 32 * RS);
    f32x4 v1 = *(const f32x4*)(p + 16) + *(const f32x4*)(p + 32 * RS + 16);
    u32x4 o;
    o[0] = pk2(lo_f(xv[0]) + v0[0], hi_f(xv[0]) + v0[1]);
    o[1] = pk2(lo_f(xv[1]) + v0[2], hi_f(xv[1]) + v0[3]);
    o[2] = pk2(lo_f(xv[2]) + v1[0], hi_f(xv[2]) + v1[1]);
    o[3] = pk2(lo_f(xv[3]) + v1[2], hi_f(xv[3]) + v1[3]);
    *(u32x4*)(out + (size_t)(t0 + et) * D_ + n0 + ec * 8) = o;
  }
}

extern "C" void kernel_launch(void* const* d_in, const int* in_sizes, int n_in, void* d_out, int out_size,
                              void* d_ws, size_t ws_size, hipStream_t stream) {
  const u16* x = (const u16*)d_in[0];
  const u16* wqkv = (const u16*)d_in[1];
  const u16* wphi = (const u16*)d_in[2];
  const u16* wo = (const u16*)d_in[3];
  const u16* win = (const u16*)d_in[5];
  const u16* wsp = (const u16*)d_in[6];
  const u16* bsp = (const u16*)d_in[7];
  const u16* wout = (const u16*)d_in[8];
  char* ws = (char*)d_ws;
  k1_kernel<<<256, 640, 0, stream>>>(x, wqkv, wo, win, wout, wsp, ws);
  k2_kernel<<<192, 512, 0, stream>>>(wphi, ws);
  k3_kernel<<<256, 768, 0, stream>>>(wsp, bsp, wphi, ws);
  k4_kernel<<<256, 448, 0, stream>>>(x, ws, (u16*)d_out);
}
```

```cpp
#include <hip/hip_runtime.h>
#include <hip/hip_bf16.h>
#include <stdint.h>

typedef __attribute__((ext_vector_type(8))) short bf16x8;
typedef __attribute__((ext_vector_type(4))) short s16x4;
typedef __attribute__((ext_vector_type(4))) float f32x4;
typedef __attribute__((ext_vector_type(2))) float f32x2;
typedef __attribute__((ext_vector_type(4))) unsigned u32x4;
typedef __attribute__((ext_vector_type(2))) unsigned u32x2;
typedef __attribute__((ext_vector_type(2))) __bf16 bf2_t;
typedef unsigned short u16;
#define LDS_AS __attribute__((address_space(3)))

constexpr int S_ = 2048, D_ = 448, NQKV = 1344, NIN = 1792, FH = 896, KO = 1344;

__device__ __forceinline__ unsigned pk2(float a, float b) {
  f32x2 v = {a, b};
  return __builtin_bit_cast(unsigned, __builtin_convertvector(v, bf2_t));
}
__device__ __forceinline__ float lo_f(unsigned u) { return __uint_as_float(u << 16); }
__device__ __forceinline__ float hi_f(unsigned u) { return __uint_as_float(u & 0xffff0000u); }
__device__ __forceinline__ float rbf(float a) { return lo_f(pk2(a, 0.f)); }
__device__ __forceinline__ float bf2f(u16 v) { return __uint_as_float(((unsigned)v) << 16); }
__device__ __forceinline__ float dot2(unsigned a, unsigned b, float c) {
  return __builtin_amdgcn_fdot2_f32_bf16(__builtin_bit_cast(bf2_t, a), __builtin_bit_cast(bf2_t, b), c, false);
}

__device__ __forceinline__ float x16_max(float v) {
  auto r = __builtin_amdgcn_permlane16_swap(__float_as_uint(v), __float_as_uint(v), false, false);
  return fmaxf(__uint_as_float(r[0]), __uint_as_float(r[1]));
}
__device__ __forceinline__ float x32_max(float v) {
  auto r = __builtin_amdgcn_permlane32_swap(__float_as_uint(v), __float_as_uint(v), false, false);
  return fmaxf(__uint_as_float(r[0]), __uint_as_float(r[1]));
}
__device__ __forceinline__ float x16_min(float v) {
  auto r = __builtin_amdgcn_permlane16_swap(__float_as_uint(v), __float_as_uint(v), false, false);
  return fminf(__uint_as_float(r[0]), __uint_as_float(r[1]));
}
__device__ __forceinline__ float x32_min(float v) {
  auto r = __builtin_amdgcn_permlane32_swap(__float_as_uint(v), __float_as_uint(v), false, false);
  return fminf(__uint_as_float(r[0]), __uint_as_float(r[1]));
}
__device__ __forceinline__ float x16_add(float v) {
  auto r = __builtin_amdgcn_permlane16_swap(__float_as_uint(v), __float_as_uint(v), false, false);
  return __uint_as_float(r[0]) + __uint_as_float(r[1]);
}
__device__ __forceinline__ float x32_add(float v) {
  auto r = __builtin_amdgcn_permlane32_swap(__float_as_uint(v), __float_as_uint(v), false, false);
  return __uint_as_float(r[0]) + __uint_as_float(r[1]);
}
__device__ __forceinline__ float red8_add(float v) {
  v += __uint_as_float(__builtin_amdgcn_mov_dpp(__float_as_uint(v), 0xB1, 0xf, 0xf, true));
  v += __uint_as_float(__builtin_amdgcn_mov_dpp(__float_as_uint(v), 0x4E, 0xf, 0xf, true));
  v += __uint_as_float(__builtin_amdgcn_mov_dpp(__float_as_uint(v), 0x141, 0xf, 0xf, true));
  return v;
}
__device__ __forceinline__ f32x4 mfma16(bf16x8 a, bf16x8 b, f32x4 c) {
  return __builtin_amdgcn_mfma_f32_16x16x32_bf16(a, b, c, 0, 0, 0);
}
__device__ __forceinline__ unsigned gelu_pk(unsigned p) {
  const f32x2 u = {lo_f(p), hi_f(p)};
  const f32x2 c1 = {-2.3022081986f, -2.3022081986f};
  const f32x2 c2 = {-0.1029432397f, -0.1029432397f};
  f32x2 t = u * u;
  f32x2 a = u * (t * c2 + c1);
  f32x2 d = {1.f + __builtin_amdgcn_exp2f(a[0]), 1.f + __builtin_amdgcn_exp2f(a[1])};
  f32x2 r = {__builtin_amdgcn_rcpf(d[0]), __builtin_amdgcn_rcpf(d[1])};
  f32x2 o = u * r;
  return pk2(o[0], o[1]);
}
#define SCHED() __builtin_amdgcn_sched_barrier(0)
#define BAR() do { SCHED(); asm volatile("" ::: "memory"); __builtin_amdgcn_s_waitcnt(0xC07F); __builtin_amdgcn_s_barrier(); asm volatile("" ::: "memory"); SCHED(); } while (0)
__device__ __forceinline__ void glds16(const void* g, char* l) {
  __builtin_amdgcn_global_load_lds((const unsigned*)g, (LDS_AS unsigned*)l, 16, 0, 0);
}
__device__ __forceinline__ bf16x8 tr_frag(const char* p0, const char* p1) {
  s16x4 a = __builtin_amdgcn_ds_read_tr16_b64_v4i16((LDS_AS s16x4*)p0);
  s16x4 c = __builtin_amdgcn_ds_read_tr16_b64_v4i16((LDS_AS s16x4*)p1);
  return __builtin_shufflevector(a, c, 0, 1, 2, 3, 4, 5, 6, 7);
}

constexpr int YLD = 3136;
constexpr size_t WS_Y = 0;
constexpr size_t WS_Z2N = WS_Y + (size_t)S_ * YLD * 2 + 256;
constexpr size_t WS_OH = WS_Z2N + (size_t)S_ * 896 * 2;
constexpr size_t WS_WT = WS_OH + (size_t)S_ * KO * 2;
constexpr size_t WS_KVP = WS_WT + (size_t)D_ * KO * 2;
constexpr size_t WS_END = WS_KVP + (size_t)8 * 16 * 56 * 32 * 4;

#define STAMP(K, I) do { } while (0)
#define SGB(mask, n) __builtin_amdgcn_sched_group_barrier(mask, n, 0)

__device__ __forceinline__ void phi_map(bf16x8 qf, bf16x8 wp0, bf16x8 wp1, int g, unsigned (&pp)[2][2],
                                        unsigned (&pm)[2][2]) {
  f32x4 z = {0.f, 0.f, 0.f, 0.f};
  f32x4 p0 = mfma16(wp0, qf, z);
  f32x4 p1 = mfma16(wp1, qf, z);
  float v[8];
  {
    unsigned a = pk2(p0[0], p0[1]), b = pk2(p0[2], p0[3]), c = pk2(p1[0], p1[1]), d = pk2(p1[2], p1[3]);
    v[0] = lo_f(a); v[1] = hi_f(a); v[2] = lo_f(b); v[3] = hi_f(b);
    v[4] = lo_f(c); v[5] = hi_f(c); v[6] = lo_f(d); v[7] = hi_f(d);
  }
  const bool valid1 = (g != 3);
  if (!valid1) { v[4] = v[0]; v[5] = v[0]; v[6] = v[0]; v[7] = v[0]; }
  float mx = fmaxf(fmaxf(fmaxf(v[0], v[1]), fmaxf(v[2], v[3])), fmaxf(fmaxf(v[4], v[5]), fmaxf(v[6], v[7])));
  float mn = fminf(fminf(fminf(v[0], v[1]), fminf(v[2], v[3])), fminf(fminf(v[4], v[5]), fminf(v[6], v[7])));
  mx = x32_max(x16_max(mx));
  mn = x32_min(x16_min(mn));
  const float L2E = 1.4426950408889634f;
  const float mxl = mx * L2E, mnl = mn * L2E;
  float ep[8], em[8];
  float sp = 0.f, sm = 0.f;
#pragma unroll
  for (int j = 0; j < 8; ++j) {
    ep[j] = __builtin_amdgcn_exp2f(__builtin_fmaf(v[j], L2E, -mxl));
    em[j] = __builtin_amdgcn_exp2f(__builtin_fmaf(v[j], -L2E, mnl));
    if (j >= 4) { ep[j] = valid1 ? ep[j] : 0.f; em[j] = valid1 ? em[j] : 0.f; }
    sp += ep[j];
    sm += em[j];
  }
  sp = x32_add(x16_add(sp));
  sm = x32_add(x16_add(sm));
  const float rp = __builtin_amdgcn_rcpf(sp), rm = __builtin_amdgcn_rcpf(sm);
  pp[0][0] = pk2(ep[0] * rp, ep[1] * rp); pp[0][1] = pk2(ep[2] * rp, ep[3] * rp);
  pp[1][0] = pk2(ep[4] * rp, ep[5] * rp); pp[1][1] = pk2(ep[6] * rp, ep[7] * rp);
  pm[0][0] = pk2(em[0] * rm, em[1] * rm); pm[0][1] = pk2(em[2] * rm, em[3] * rm);
  pm[1][0] = pk2(em[4] * rm, em[5] * rm); pm[1][1] = pk2(em[6] * rm, em[7] * rm);
}

constexpr int K1_XB = 128 * 896;
constexpr int K1_WST = 32 * 448;
constexpr int K1_LDS = K1_XB + 3 * K1_WST;

__global__ __launch_bounds__(640) void k1_kernel(const u16* __restrict__ x, const u16* __restrict__ wqkv,
                                                  const u16* __restrict__ wo, const u16* __restrict__ win,
                                                  const u16* __restrict__ wout, const u16* __restrict__ wsp,
                                                  char* __restrict__ ws) {
  __shared__ __attribute__((aligned(16))) char sm[K1_LDS];
  const int tid = threadIdx.x, lane = tid & 63, wave = __builtin_amdgcn_readfirstlane(tid >> 6);
  const int b = blockIdx.x;
  STAMP(0, 0);
  if (b >= 224) {
    const int kb = b - 224;
    const int k0 = kb * 64;
    if (kb < 21) {
    u16* T = (u16*)sm;
    constexpr int TS = 456;
    u32x4 v[6];
#pragma unroll
    for (int j = 0; j < 6; ++j) {
      int i = tid + 640 * j;
      if (i >= 64 * 56) i = tid;
      const int r = i / 56, c = i % 56;
      const int k = k0 + r;
      const u16* src = (k < 448) ? (wo + (size_t)k * 448) : (wout + (size_t)(k - 448) * 448);
      v[j] = *(const u32x4*)(src + c * 8);
    }
#pragma unroll
    for (int j = 0; j < 6; ++j) {
      const int i = tid + 640 * j;
      if (i < 64 * 56) *(u32x4*)(T + (i / 56) * TS + (i % 56) * 8) = v[j];
    }
    __syncthreads();
    u16* wT = (u16*)(ws + WS_WT);
    for (int i = tid; i < 448 * 8; i += 640) {
      const int n = i >> 3, c = i & 7;
      u16 t[8];
#pragma unroll
      for (int j = 0; j < 8; ++j) t[j] = T[(c * 8 + j) * TS + n];
      u32x4 o;
#pragma unroll
      for (int j = 0; j < 4; ++j) o[j] = (unsigned)t[2 * j] | ((unsigned)t[2 * j + 1] << 16);
      *(u32x4*)(wT + (size_t)n * KO + k0 + c * 8) = o;
    }
    }
    STAMP(0, 1);
    {
      const char* p = (const char*)wsp + (size_t)kb * 262144 + tid * 16;
      unsigned a = 0;
#pragma unroll
      for (int i = 0; i < 25; ++i) {
        u32x4 v = *(const u32x4*)(p + i * 10240);
        a |= v[0] & v[1] & v[2] & v[3];
      }
      asm volatile("" ::"v"(a));
    }
    STAMP(0, 31);
    return;
  }

  const int xcd = b & 7, idx = b >> 3;
  const int cb = (xcd & 1) * 7 + idx % 7;
  const int rt = (xcd >> 1) * 4 + idx / 7;
  const int row0 = rt * 128;
  char* wring = sm + K1_XB;
  const int g = lane >> 4, r16 = lane & 15;
  const int wm = (wave >> 1) & 3, wn = wave & 1;
  constexpr int RST = 464;

  if (wave >= 8) {
    const int lw = wave - 8;
    const u16* W = (cb < 6) ? (wqkv + 224 * cb) : (win + 224 * (cb - 6));
    const int ldw = (cb < 6) ? NQKV : NIN;
    const u16* src[7];
#pragma unroll
    for (int j = 0; j < 7; ++j) {
      const int o = (lw + 2 * j) * 1024 + lane * 16;
      const int k = o / 448, cp = (o % 448) >> 4;
      const int G = (cp >> 1) ^ ((k >> 3) & 1);
      const int c = 2 * G + (cp & 1);
      src[j] = W + (size_t)k * ldw + 8 * c;
    }
    const size_t wstep = (size_t)32 * ldw;
    char* dst0 = wring + lw * 1024;
#pragma unroll
    for (int s = 0; s < 3; ++s)
#pragma unroll
      for (int j = 0; j < 7; ++j) glds16(src[j] + s * wstep, dst0 + s * K1_WST + j * 2048);
    asm volatile("s_waitcnt vmcnt(14)" ::: "memory");
    __builtin_amdgcn_s_barrier();
    int nb = 0;
    for (int t = 0; t < 14; ++t) {
      if (t + 2 < 14) asm volatile("s_waitcnt vmcnt(7)" ::: "memory");
      else asm volatile("s_waitcnt vmcnt(0)" ::: "memory");
      __builtin_amdgcn_s_barrier();
      if (t + 3 < 14) {
        char* dst = dst0 + nb * K1_WST;
#pragma unroll
        for (int j = 0; j < 7; ++j) glds16(src[j] + (t + 3) * wstep, dst + j * 2048);
        nb = (nb == 2) ? 0 : nb + 1;
      }
    }
    __builtin_amdgcn_s_barrier();
    __builtin_amdgcn_s_barrier();
  } else {
#pragma unroll
    for (int j = 0; j < 14; ++j) {
      const int o = (wave + 8 * j) * 1024 + lane * 16;
      const int row = o / 896, p = (o % 896) >> 4;
      const int c = p ^ ((row >> 1) & 7);
      glds16(x + (size_t)(row0 + row) * D_ + c * 8, sm + (wave + 8 * j) * 1024);
    }
    float sx[2] = {0.f, 0.f}, sq[2] = {0.f, 0.f}, cs[7] = {0.f, 0.f, 0.f, 0.f, 0.f, 0.f, 0.f};
    asm volatile("s_waitcnt vmcnt(0)" ::: "memory");
    f32x4 acc[7][2];
#pragma unroll
    for (int nt = 0; nt < 7; ++nt) { acc[nt][0] = f32x4{0.f, 0.f, 0.f, 0.f}; acc[nt][1] = f32x4{0.f, 0.f, 0.f, 0.f}; }
    const int sw = (r16 >> 1) & 7;
    const char* xbase = sm + (32 * wm + r16) * 896;
    const int xoE = ((g ^ sw) << 4), xoO = (((4 | g) ^ sw) << 4);
    const int wrow = (8 * g + ((lane >> 2) & 3)) * 448 + (lane & 3) * 8 + 7 * wn * 32;
    const int wE = wrow + ((wn & 1) ? -32 * (g & 1) : 32 * (g & 1));
    const int wO = wrow + ((wn & 1) ? 32 * (g & 1) : -32 * (g & 1));
    bf16x8 xa[2], wa[7], xb[2], wb[7];
#define K1_READ(XF, WF, T, BUF)                                                              \
  do {                                                                                       \
    const char* xp_ = xbase + ((T) >> 1) * 128 + (((T)&1) ? xoO : xoE);                      \
    const char* wb_ = wring + (BUF)*K1_WST;                                                  \
    XF[0] = *(const bf16x8*)(xp_);                                                           \
    XF[1] = *(const bf16x8*)(xp_ + 16 * 896);                                                \
    _Pragma("unroll") for (int nt = 0; nt < 7; ++nt) {                                       \
      const char* p_ = wb_ + ((nt & 1) ? wO : wE) + nt * 32;                                 \
      WF[nt] = tr_frag(p_, p_ + 4 * 448);                                                    \
    }                                                                                        \
  } while (0)
#define K1_MMA(XF, WF, T)                                                                    \
  do {                                                                                       \
    _Pragma("unroll") for (int nt = 0; nt < 7; ++nt) acc[nt][0] = mfma16(WF[nt], XF[0], acc[nt][0]); \
    _Pragma("unroll") for (int nt = 0; nt < 7; ++nt) acc[nt][1] = mfma16(WF[nt], XF[1], acc[nt][1]); \
    _Pragma("unroll") for (int mt = 0; mt < 2; ++mt) {                                       \
      const u32x4 u_ = __builtin_bit_cast(u32x4, XF[mt]);                                    \
      _Pragma("unroll") for (int j = 0; j < 4; ++j) {                                        \
        sx[mt] = dot2(u_[j], 0x3F803F80u, sx[mt]);                                           \
        sq[mt] = dot2(u_[j], u_[j], sq[mt]);                                                 \
      }                                                                                      \
    }                                                                                        \
    if (((T)&3) == wm) {                                                                     \
      _Pragma("unroll") for (int nt = 0; nt < 7; ++nt) {                                     \
        const u32x4 u_ = __builtin_bit_cast(u32x4, WF[nt]);                                  \
        _Pragma("unroll") for (int j = 0; j < 4; ++j) cs[nt] = dot2(u_[j], 0x3F803F80u, cs[nt]); \
      }                                                                                      \
    }                                                                                        \
  } while (0)
#define K1_MIX()                                                                             \
  do {                                                                                       \
    SGB(0x100, 2);                                                                           \
    _Pragma("unroll") for (int i_ = 0; i_ < 7; ++i_) { SGB(0x008, 1); SGB(0x100, 2); }       \
    _Pragma("unroll") for (int i_ = 0; i_ < 7; ++i_) { SGB(0x008, 1); SGB(0x002, 2); }       \
  } while (0)
    BAR();
    STAMP(0, 2);
    K1_READ(xa, wa, 0, 0);
    int rb = 1;
#pragma unroll 1
    for (int t = 0; t < 12; t += 2) {
      BAR();
      STAMP(0, 3 + t);
      K1_READ(xb, wb, t + 1, rb);
      rb = (rb == 2) ? 0 : rb + 1;
      K1_MMA(xa, wa, t);
      K1_MIX();
      BAR();
      STAMP(0, 4 + t);
      K1_READ(xa, wa, t + 2, rb);
      rb = (rb == 2) ? 0 : rb + 1;
      K1_MMA(xb, wb, t + 1);
      K1_MIX();
    }
    BAR();
    K1_READ(xb, wb, 13, 1);
    K1_MMA(xa, wa, 12);
    K1_MIX();
    BAR();
    K1_MMA(xb, wb, 13);
    BAR();
    STAMP(0, 17);
    float* CS = (float*)(sm + 128 * RST);
#pragma unroll
    for (int nt = 0; nt < 7; ++nt) {
      const float v = x32_add(x16_add(cs[nt]));
      if (g == 0) CS[wm * 224 + 112 * wn + 16 * nt + r16] = v;
    }
    float rs[2], rm[2];
#pragma unroll
    for (int mt = 0; mt < 2; ++mt) {
      const float mean = x32_add(x16_add(sx[mt])) * (1.f / 448.f);
      const float ex2 = x32_add(x16_add(sq[mt])) * (1.f / 448.f);
      const float var = fmaxf(ex2 - mean * mean, 0.f);
      const float sd = rbf(sqrtf(rbf(rbf(var) + 1e-5f)));
      rs[mt] = 1.f / sd;
      rm[mt] = rs[mt] * rbf(mean);
    }
    BAR();
#pragma unroll
    for (int nt = 0; nt < 7; ++nt) {
      const f32x4* cp = (const f32x4*)(CS + 112 * wn + 16 * nt + 4 * g);
      const f32x4 c4 = (cp[0] + cp[56]) + (cp[112] + cp[168]);
#pragma unroll
      for (int mt = 0; mt < 2; ++mt) {
        char* dst = sm + (32 * wm + 16 * mt + r16) * RST + (112 * wn + 4 * g) * 2 + 32 * nt;
        const f32x4 v = acc[nt][mt] * rs[mt] - c4 * rm[mt];
        *(u32x2*)(dst) = u32x2{pk2(v[0], v[1]), pk2(v[2], v[3])};
      }
    }
  }
  __syncthreads();
  u16* dstb = (u16*)(ws + WS_Y) + (size_t)row0 * YLD + 224 * cb;
#pragma unroll
  for (int i = 0; i < 6; ++i) {
    const int id = tid + 640 * i;
    if (id < 128 * 28) {
      const int s = id / 28, c = id % 28;
      *(u32x4*)(dstb + (size_t)s * YLD + c * 8) = *(const u32x4*)(sm + s * RST + c * 16);
    }
  }
  STAMP(0, 31);
}

__device__ __forceinline__ float red64_add(float v) {
  v = red8_add(v);
  v += __uint_as_float(__builtin_amdgcn_mov_dpp(__float_as_uint(v), 0x140, 0xf, 0xf, true));
  return x32_add(x16_add(v));
}

constexpr int K2_PW = 64 * 80 + 32 * 64;
__global__ __launch_bounds__(512) void k2_kernel(const u16* __restrict__ wphi, char* __restrict__ ws) {
  __shared__ __attribute__((aligned(16))) char sm[8 * 56 * 32 * 4 + 2048];
  const int tid = threadIdx.x, lane = tid & 63, wave = __builtin_amdgcn_readfirstlane(tid >> 6);
  const int b = blockIdx.x;
  const u16* Y = (const u16*)(ws + WS_Y);
  STAMP(1, 0);
  if (b < 128) {
    const int h = b & 15, rg = b >> 4;
    const int g = lane >> 4, r16 = lane & 15;
    const int rowb = rg * 256 + wave * 32;
    u32x2 kv_[2][2];
#pragma unroll
    for (int st = 0; st < 2; ++st) {
      const u16* kp = Y + (size_t)(rowb + st * 16 + r16) * YLD + 448 + h * 28 + 8 * g;
      kv_[st][0] = *(const u32x2*)(kp);
      kv_[st][1] = *(const u32x2*)(kp + 4);
    }
    u32x2 vv[4];
#pragma unroll
    for (int i = 0; i < 4; ++i) {
      const int row = (lane >> 3) + 8 * i, pc = lane & 7;
      vv[i] = *(const u32x2*)(Y + (size_t)(rowb + row) * YLD + 896 + h * 28 + 4 * (pc < 7 ? pc : 0));
      if (pc == 7) vv[i] = u32x2{0x00003F80u, 0u};
    }
    u32x4 wraw = {0u, 0u, 0u, 0u};
    if (tid < 98) wraw = *(const u32x4*)(wphi + (size_t)h * 784 + tid * 8);
    char* WP = sm + 8 * 56 * 32 * 4;
    if (tid < 98) *(u32x4*)(WP + tid * 16) = wraw;
    char* PK = sm + wave * K2_PW;
    char* VS = PK + 64 * 80;
    __syncthreads();
    STAMP(1, 1);
    const char* wp = WP + (8 * g + ((lane >> 2) & 3)) * 56 + (lane & 3) * 8;
    bf16x8 wp0 = tr_frag(wp, wp + 4 * 56);
    bf16x8 wp1 = tr_frag(wp + 32, wp + 4 * 56 + 32);
    if (g == 3) {
#pragma unroll
      for (int j = 4; j < 8; ++j) { wp0[j] = 0; wp1[j] = 0; }
    }
    f32x4 kacc[4][2];
#pragma unroll
    for (int a = 0; a < 4; ++a) { kacc[a][0] = f32x4{0.f, 0.f, 0.f, 0.f}; kacc[a][1] = f32x4{0.f, 0.f, 0.f, 0.f}; }
#pragma unroll
    for (int pr = 0; pr < 1; ++pr) {
#pragma unroll
      for (int i = 0; i < 4; ++i) {
        const int row = (lane >> 3) + 8 * i;
        const int slot = 2 * (row & 15) + (row >> 4);
        *(u32x2*)(VS + slot * 64 + (lane & 7) * 8) = vv[4 * pr + i];
      }
      unsigned pp[2][2][2], pm[2][2][2];
#pragma unroll
      for (int mt = 0; mt < 2; ++mt) {
        u32x4 q4 = {kv_[2 * pr + mt][0][0], kv_[2 * pr + mt][0][1], kv_[2 * pr + mt][1][0], kv_[2 * pr + mt][1][1]};
        if (g == 3) { q4[2] = 0u; q4[3] = 0u; }
        phi_map(__builtin_bit_cast(bf16x8, q4), wp0, wp1, g, pp[mt], pm[mt]);
      }
#pragma unroll
      for (int ft = 0; ft < 2; ++ft) {
        if (ft == 0 || g != 3) {
          char* d0 = PK + (16 * ft + 4 * g) * 80 + r16 * 4;
#pragma unroll
          for (int j = 0; j < 2; ++j) {
            *(unsigned*)(d0 + (2 * j) * 80) = (pp[0][ft][j] & 0xffffu) | (pp[1][ft][j] << 16);
            *(unsigned*)(d0 + (2 * j + 1) * 80) = (pp[0][ft][j] >> 16) | (pp[1][ft][j] & 0xffff0000u);
            *(unsigned*)(d0 + (28 + 2 * j) * 80) = (pm[0][ft][j] & 0xffffu) | (pm[1][ft][j] << 16);
            *(unsigned*)(d0 + (28 + 2 * j + 1) * 80) = (pm[0][ft][j] >> 16) | (pm[1][ft][j] & 0xffff0000u);
          }
        }
      }
      __builtin_amdgcn_s_waitcnt(0xC07F);
      __builtin_amdgcn_wave_barrier();
      bf16x8 bfr[2];
      {
        const char* vp = VS + (8 * g + ((lane >> 2) & 3)) * 64 + (lane & 3) * 8;
        bfr[0] = tr_frag(vp, vp + 4 * 64);
        bfr[1] = tr_frag(vp + 32, vp + 4 * 64 + 32);
      }
#pragma unroll
      for (int a = 0; a < 4; ++a) {
        bf16x8 af = *(const bf16x8*)(PK + (16 * a + r16) * 80 + g * 16);
        kacc[a][0] = mfma16(af, bfr[0], kacc[a][0]);
        kacc[a][1] = mfma16(af, bfr[1], kacc[a][1]);
      }
      __builtin_amdgcn_s_waitcnt(0xC07F);
      __builtin_amdgcn_wave_barrier();
    }
    STAMP(1, 2);
    __syncthreads();
    float* red = (float*)sm + wave * (56 * 32);
#pragma unroll
    for (int a = 0; a < 4; ++a)
#pragma unroll
      for (int c = 0; c < 2; ++c)
#pragma unroll
        for (int j = 0; j < 4; ++j) {
          const int f = 16 * a + 4 * g + j;
          if (f < 56) red[f * 32 + 16 * c + r16] = kacc[a][c][j];
        }
    __syncthreads();
    if (tid < 448) {
      const f32x4* rp = (const f32x4*)sm + tid;
      f32x4 s = (rp[0] + rp[448]) + (rp[2 * 448] + rp[3 * 448]);
      s += (rp[4 * 448] + rp[5 * 448]) + (rp[6 * 448] + rp[7 * 448]);
      *((f32x4*)(ws + WS_KVP) + (size_t)(rg * 16 + h) * 448 + tid) = s;
    }
  } else {
    const int row0 = (b - 128) * 16 + wave * 2;
    u16* z2n = (u16*)(ws + WS_Z2N);
    const int le = (lane < 56) ? lane : 0;
    u32x4 v[2][2];
#pragma unroll
    for (int r = 0; r < 2; ++r)
#pragma unroll
      for (int c = 0; c < 2; ++c) v[r][c] = *(const u32x4*)(Y + (size_t)(row0 + r) * YLD + 2240 + (le + 56 * c) * 8);
#pragma unroll
    for (int r = 0; r < 2; ++r) {
      float s = 0.f;
#pragma unroll
      for (int c = 0; c < 2; ++c)
#pragma unroll
        for (int j = 0; j < 4; ++j) {
          v[r][c][j] = gelu_pk(v[r][c][j]);
          s = dot2(v[r][c][j], 0x3F803F80u, s);
        }
      if (lane >= 56) s = 0.f;
      const float mean = red64_add(s) * (1.f / 896.f);
      const float mb = rbf(mean);
      float q = 0.f;
#pragma unroll
      for (int c = 0; c < 2; ++c)
#pragma unroll
        for (int j = 0; j < 4; ++j) {
          const float a = lo_f(v[r][c][j]) - mean, e = hi_f(v[r][c][j]) - mean;
          q += a * a + e * e;
          v[r][c][j] = pk2(lo_f(v[r][c][j]) - mb, hi_f(v[r][c][j]) - mb);
        }
      if (lane >= 56) q = 0.f;
      const float var = red64_add(q) * (1.f / 896.f);
      const float sd = rbf(sqrtf(rbf(rbf(var) + 1e-5f)));
      const float inv = 1.f / sd;
      if (lane < 56) {
#pragma unroll
        for (int c = 0; c < 2; ++c) {
          u32x4 o;
#pragma unroll
          for (int j = 0; j < 4; ++j) o[j] = pk2(lo_f(v[r][c][j]) * inv, hi_f(v[r][c][j]) * inv);
          const int ch = lane + 56 * c, pn = ch / 14, wi = ch % 14;
          const int sr = row0 + r;
          const int pos = (sr & ~31) + ((sr >> 2) & 1) * 16 + ((sr >> 3) & 3) * 4 + (sr & 3);
          *(u32x4*)(z2n + ((size_t)pn * S_ + pos) * 112 + wi * 8) = o;
        }
      }
    }
  }
  STAMP(1, 31);
}

constexpr int K3_STAGE = 64 * 256 + 128 * 224;
constexpr int K3_AUX = 3 * K3_STAGE;
__global__ __launch_bounds__(768) void k3_kernel(const u16* __restrict__ wsp, const u16* __restrict__ bsp,
                                                  const u16* __restrict__ wphi, char* __restrict__ ws) {
  __shared__ __attribute__((aligned(16))) char sm[3 * K3_STAGE + 32 * 144 + 2048];
  const int tid = threadIdx.x, lane = tid & 63, wave = __builtin_amdgcn_readfirstlane(tid >> 6);
  const int b = blockIdx.x;
  const int xcd = b & 7, idx = b >> 3;
  const int mt_ = (xcd >> 1) * 8 + (idx & 7);
  const int nt_ = (xcd & 1) * 4 + (idx >> 3);
  const int t0 = mt_ * 64, f0 = nt_ * 112;
  const u16* z2n = (const u16*)(ws + WS_Z2N);
  const u16* Y = (const u16*)(ws + WS_Y);
  u16* oh = (u16*)(ws + WS_OH);
  constexpr int RS = 464;
  u32x4 zv[2];
  float bb[2];
  STAMP(2, 0);
  if (wave >= 8) {
    const int lw = wave - 8;
    const u16* src[11];
    int step[11];
#pragma unroll
    for (int j = 0; j < 11; ++j) {
      const int i = lw + 4 * j;
      if (j < 4) {
        const int row = 4 * i + (lane >> 4);
        const int c = (lane & 15) ^ (row & 15);
        src[j] = wsp + (size_t)(t0 + row) * S_ + c * 8;
        step[j] = 128;
      } else {
        src[j] = z2n + (size_t)nt_ * S_ * 112 + (i - 16) * 512 + lane * 8;
        step[j] = 128 * 112;
      }
    }
    char* dst0 = sm + lw * 1024;
#pragma unroll
    for (int s = 0; s < 3; ++s)
#pragma unroll
      for (int j = 0; j < 11; ++j) glds16(src[j] + s * step[j], dst0 + s * K3_STAGE + j * 4096);
    asm volatile("s_waitcnt vmcnt(22)" ::: "memory");
    __builtin_amdgcn_s_barrier();
    int nb = 0;
    for (int t = 0; t < 16; ++t) {
      if (t + 2 < 16) asm volatile("s_waitcnt vmcnt(11)" ::: "memory");
      else asm volatile("s_waitcnt vmcnt(0)" ::: "memory");
      __builtin_amdgcn_s_barrier();
      STAMP(2, 5 + t);
      if (t + 3 < 16) {
        char* dst = dst0 + nb * K3_STAGE;
#pragma unroll
        for (int j = 0; j < 11; ++j) glds16(src[j] + (t + 3) * step[j], dst + j * 4096);
        nb = (nb == 2) ? 0 : nb + 1;
      }
    }
    {
      const int t = tid / 14, c = tid % 14;
      zv[0] = *(const u32x4*)(Y + (size_t)(t0 + t) * YLD + 1344 + f0 + c * 8);
      bb[0] = bf2f(bsp[t0 + t]);
      zv[1] = zv[0]; bb[1] = bb[0];
    }
    __builtin_amdgcn_s_barrier();
  } else {
    const int g = lane >> 4, r16 = lane & 15;
    {
      const int h = b & 15, srow = (b >> 4) * 128 + wave * 16 + r16;
      f32x4 part[8];
      {
        const int te = (tid < 448) ? tid : 0;
        const f32x4* kvp = (const f32x4*)(ws + WS_KVP) + (size_t)h * 448 + te;
#pragma unroll
        for (int p = 0; p < 8; ++p) part[p] = kvp[(size_t)p * 16 * 448];
      }
      u32x4 wraw = {0u, 0u, 0u, 0u};
      if (tid < 98) wraw = *(const u32x4*)(wphi + (size_t)h * 784 + tid * 8);
      const u16* qp = Y + (size_t)srow * YLD + h * 28 + 8 * g;
      const u32x2 q0 = *(const u32x2*)(qp), q1 = *(const u32x2*)(qp + 4);
      char* KT = sm + K3_AUX;
      char* WP = KT + 32 * 144;
      if (tid < 98) *(u32x4*)(WP + tid * 16) = wraw;
      if (tid < 448) {
        const f32x4 s = ((part[0] + part[1]) + (part[2] + part[3])) + ((part[4] + part[5]) + (part[6] + part[7]));
        const int fp = tid >> 3, d0 = 4 * (tid & 7);
        const int mf = (fp >= 28) ? 1 : 0, f = fp - 28 * mf;
        const int pos = mf * 32 + ((f & 15) >> 2) * 8 + (f & 3) + 4 * (f >> 4);
        const unsigned a = pk2(s[0], s[1]), c = pk2(s[2], s[3]);
        char* d = KT + d0 * 144 + pos * 2;
        *(u16*)(d) = (u16)a; *(u16*)(d + 144) = (u16)(a >> 16);
        *(u16*)(d + 288) = (u16)c; *(u16*)(d + 432) = (u16)(c >> 16);
      } else {
        const int i = tid - 448, d = i >> 1, mf = i & 1;
        *(u32x2*)(KT + d * 144 + (mf * 32 + 28) * 2) = u32x2{0u, 0u};
      }
      STAMP(2, 1);
      BAR();
      STAMP(2, 2);
      bf16x8 af[2][2];
#pragma unroll
      for (int dt = 0; dt < 2; ++dt)
#pragma unroll
        for (int mf = 0; mf < 2; ++mf) af[dt][mf] = *(const bf16x8*)(KT + (16 * dt + r16) * 144 + (32 * mf + 8 * g) * 2);
      const char* wp = WP + (8 * g + ((lane >> 2) & 3)) * 56 + (lane & 3) * 8;
      bf16x8 wp0 = tr_frag(wp, wp + 4 * 56);
      bf16x8 wp1 = tr_frag(wp + 32, wp + 4 * 56 + 32);
      if (g == 3) {
#pragma unroll
        for (int j = 4; j < 8; ++j) { wp0[j] = 0; wp1[j] = 0; }
      }
      u32x4 q4 = {q0[0], q0[1], q1[0], q1[1]};
      if (g == 3) { q4[2] = 0u; q4[3] = 0u; }
      unsigned pp[2][2], pm[2][2];
      phi_map(__builtin_bit_cast(bf16x8, q4), wp0, wp1, g, pp, pm);
      u32x4 b1 = {pp[0][0], pp[0][1], pp[1][0], pp[1][1]};
      u32x4 b2 = {pm[0][0], pm[0][1], pm[1][0], pm[1][1]};
      f32x4 a0 = {0.f, 0.f, 0.f, 0.f}, a1 = {0.f, 0.f, 0.f, 0.f};
      a0 = mfma16(af[0][0], __builtin_bit_cast(bf16x8, b1), a0);
      a1 = mfma16(af[1][0], __builtin_bit_cast(bf16x8, b1), a1);
      a0 = mfma16(af[0][1], __builtin_bit_cast(bf16x8, b2), a0);
      a1 = mfma16(af[1][1], __builtin_bit_cast(bf16x8, b2), a1);
      float den = __shfl(a1[0], 48 + r16, 64);
      den = rbf(rbf(den) + 1e-6f);
      const float inv = 1.f / den;
      u16* dst = oh + (size_t)srow * KO + h * 28 + 4 * g;
      *(u32x2*)(dst) = u32x2{pk2(rbf(a0[0]) * inv, rbf(a0[1]) * inv), pk2(rbf(a0[2]) * inv, rbf(a0[3]) * inv)};
      if (g != 3)
        *(u32x2*)(dst + 16) = u32x2{pk2(rbf(a1[0]) * inv, rbf(a1[1]) * inv), pk2(rbf(a1[2]) * inv, rbf(a1[3]) * inv)};
    }
#pragma unroll
    for (int i = 0; i < 2; ++i) {
      int id = tid + 768 * i;
      if (id >= 896) id = tid;
      const int t = id / 14, c = id % 14;
      zv[i] = *(const u32x4*)(Y + (size_t)(t0 + t) * YLD + 1344 + f0 + c * 8);
      bb[i] = bf2f(bsp[t0 + t]);
    }
    STAMP(2, 3);
    const int kg = wave >> 2, wm = (wave >> 1) & 1, wn = (wave & 1) ^ kg;
    f32x4 acc[4][2];
#pragma unroll
    for (int n = 0; n < 4; ++n) { acc[n][0] = f32x4{0.f, 0.f, 0.f, 0.f}; acc[n][1] = f32x4{0.f, 0.f, 0.f, 0.f}; }
    const int co0 = ((8 * kg + g) ^ r16) << 4, co1 = ((8 * kg + 4 + g) ^ r16) << 4;
    const int arow = (32 * wm + r16) * 256;
    const int brow = 64 * 256 + (64 * kg + 4 * g + ((lane >> 2) & 3)) * 224 + (lane & 3) * 8 + 128 * wn;
#define K3_READ(AF, BF, BUF, NT)                                                               \
  do {                                                                                         \
    const char* sb_ = sm + (BUF)*K3_STAGE;                                                     \
    _Pragma("unroll") for (int kk = 0; kk < 2; ++kk) {                                         \
      const int co_ = kk ? co1 : co0;                                                          \
      AF[kk][0] = *(const bf16x8*)(sb_ + arow + co_);                                          \
      AF[kk][1] = *(const bf16x8*)(sb_ + arow + 4096 + co_);                                   \
    }                                                                                          \
    _Pragma("unroll") for (int kk = 0; kk < 2; ++kk) {                                         \
      _Pragma("unroll") for (int n = 0; n < NT; ++n) {                                         \
        const char* p_ = sb_ + brow + kk * 32 * 224 + n * 32;                                  \
        BF[kk][n] = tr_frag(p_, p_ + 16 * 224);                                                \
      }                                                                                        \
    }                                                                                          \
  } while (0)
#define K3_MMA(AF, BF, NT)                                                                     \
  do {                                                                                         \
    _Pragma("unroll") for (int kk = 0; kk < 2; ++kk) {                                         \
      _Pragma("unroll") for (int n = 0; n < NT; ++n) acc[n][0] = mfma16(BF[kk][n], AF[kk][0], acc[n][0]); \
      _Pragma("unroll") for (int n = 0; n < NT; ++n) acc[n][1] = mfma16(BF[kk][n], AF[kk][1], acc[n][1]); \
    }                                                                                          \
  } while (0)
#define K3_MIX(NT)                                                                             \
  do {                                                                                         \
    SGB(0x100, 4);                                                                             \
    _Pragma("unroll") for (int i_ = 0; i_ < 2 * NT; ++i_) { SGB(0x008, 1); SGB(0x100, 2); }    \
  } while (0)
#define K3_LOOP(NT)                                                                            \
  do {                                                                                         \
    bf16x8 aA[2][2], bA[2][NT], aB[2][2], bB[2][NT];                                           \
    STAMP(2, 4);                                                                               \
    K3_READ(aA, bA, 0, NT);                                                                    \
    int rb = 1;                                                                                \
    _Pragma("unroll 1") for (int t = 0; t < 14; t += 2) {                                      \
      BAR();                                                                                   \
      STAMP(2, 5 + t);                                                                         \
      K3_READ(aB, bB, rb, NT);                                                                 \
      rb = (rb == 2) ? 0 : rb + 1;                                                             \
      K3_MMA(aA, bA, NT);                                                                      \
      K3_MIX(NT);                                                                              \
      BAR();                                                                                   \
      STAMP(2, 6 + t);                                                                         \
      K3_READ(aA, bA, rb, NT);                                                                 \
      rb = (rb == 2) ? 0 : rb + 1;                                                             \
      K3_MMA(aB, bB, NT);                                                                      \
      K3_MIX(NT);                                                                              \
    }                                                                                          \
    BAR();                                                                                     \
    K3_READ(aB, bB, 0, NT);                                                                    \
    K3_MMA(aA, bA, NT);                                                                        \
    K3_MIX(NT);                                                                                \
    BAR();                                                                                     \
    K3_MMA(aB, bB, NT);                                                                        \
  } while (0)
    if (wn == 0) K3_LOOP(4);
    else K3_LOOP(3);
    BAR();
    STAMP(2, 21);
    char* rd = sm + kg * (64 * RS);
#pragma unroll
    for (int m = 0; m < 2; ++m)
#pragma unroll
      for (int n = 0; n < 4; ++n)
        if (n < 3 || wn == 0) *(f32x4*)(rd + (32 * wm + 16 * m + r16) * RS + (64 * wn + 16 * n + 4 * g) * 4) = acc[n][m];
  }
  __syncthreads();
#pragma unroll
  for (int i = 0; i < 2; ++i) {
    const int id = tid + 768 * i;
    if (id < 896) {
      const int t = id / 14, c = id % 14;
      const char* p = sm + t * RS + c * 32;
      f32x4 v0 = *(const f32x4*)(p) + *(const f32x4*)(p + 64 * RS);
      f32x4 v1 = *(const f32x4*)(p + 16) + *(const f32x4*)(p + 64 * RS + 16);
      u32x4 z;
#pragma unroll
      for (int j = 0; j < 4; ++j) z[j] = gelu_pk(zv[i][j]);
      u32x4 o;
      o[0] = pk2(lo_f(z[0]) * rbf(rbf(v0[0]) + bb[i]), hi_f(z[0]) * rbf(rbf(v0[1]) + bb[i]));
      o[1] = pk2(lo_f(z[1]) * rbf(rbf(v0[2]) + bb[i]), hi_f(z[1]) * rbf(rbf(v0[3]) + bb[i]));
      o[2] = pk2(lo_f(z[2]) * rbf(rbf(v1[0]) + bb[i]), hi_f(z[2]) * rbf(rbf(v1[1]) + bb[i]));
      o[3] = pk2(lo_f(z[3]) * rbf(rbf(v1[2]) + bb[i]), hi_f(z[3]) * rbf(rbf(v1[3]) + bb[i]));
      *(u32x4*)(oh + (size_t)(t0 + t) * KO + 448 + f0 + c * 8) = o;
    }
  }
  STAMP(2, 31);
}

constexpr int K4_STAGE = 144 * 128;
constexpr int K4_NB = 8;
__global__ __launch_bounds__(448) void k4_kernel(const u16* __restrict__ x, const char* __restrict__ ws,
                                                  u16* __restrict__ out) {
  __shared__ __attribute__((aligned(16))) char sm[K4_NB * K4_STAGE];
  const int tid = threadIdx.x, lane = tid & 63, wave = __builtin_amdgcn_readfirstlane(tid >> 6);
  const int b = blockIdx.x;
  const int xcd = b & 7, idx = b >> 3;
  const int mt_ = xcd * 8 + (idx & 7), nt_ = idx >> 3;
  const int t0 = mt_ * 32, n0 = nt_ * 112;
  const u16* oh = (const u16*)(ws + WS_OH);
  const u16* wt = (const u16*)(ws + WS_WT);
  constexpr int RS = 464;
  const int et = tid / 14, ec = tid % 14;
  STAMP(3, 0);
  u32x4 xv = *(const u32x4*)(x + (size_t)(t0 + et) * D_ + n0 + ec * 8);

  if (wave >= 4) {
    const int lw = wave - 4;
    const u16* src[6];
#pragma unroll
    for (int j = 0; j < 6; ++j) {
      const int i = lw + 3 * j;
      const int row = 8 * i + (lane >> 3);
      const int c = (lane & 7) ^ ((row >> 1) & 7);
      const u16* base = (row < 32) ? (oh + (size_t)(t0 + row) * KO) : (wt + (size_t)(n0 + row - 32) * KO);
      src[j] = base + c * 8;
    }
    char* dst0 = sm + lw * 1024;
#pragma unroll
    for (int s = 0; s < K4_NB - 1; ++s)
#pragma unroll
      for (int j = 0; j < 6; ++j) glds16(src[j] + s * 64, dst0 + s * K4_STAGE + j * 3072);
    int nb = K4_NB - 1;
    for (int t = 0; t < 21; ++t) {
      if (t + K4_NB - 1 <= 21) asm volatile("s_waitcnt vmcnt(36)" ::: "memory");
      else {
        const int rem = 20 - t;
        if (rem >= 5) asm volatile("s_waitcnt vmcnt(30)" ::: "memory");
        else if (rem == 4) asm volatile("s_waitcnt vmcnt(24)" ::: "memory");
        else if (rem == 3) asm volatile("s_waitcnt vmcnt(18)" ::: "memory");
        else if (rem == 2) asm volatile("s_waitcnt vmcnt(12)" ::: "memory");
        else if (rem == 1) asm volatile("s_waitcnt vmcnt(6)" ::: "memory");
        else asm volatile("s_waitcnt vmcnt(0)" ::: "memory");
      }
      __builtin_amdgcn_s_barrier();
      if (t + K4_NB - 1 < 21) {
        char* dst = dst0 + nb * K4_STAGE;
#pragma unroll
        for (int j = 0; j < 6; ++j) glds16(src[j] + (t + K4_NB - 1) * 64, dst + j * 3072);
        nb = (nb == K4_NB - 1) ? 0 : nb + 1;
      }
    }
    __builtin_amdgcn_s_barrier();
  } else {
    const int g = lane >> 4, r16 = lane & 15;
    const int kg = wave >> 1, wn = wave & 1;
    f32x4 acc[4][2];
#pragma unroll
    for (int n = 0; n < 4; ++n) { acc[n][0] = f32x4{0.f, 0.f, 0.f, 0.f}; acc[n][1] = f32x4{0.f, 0.f, 0.f, 0.f}; }
    const int co = ((4 * kg + g) ^ ((r16 >> 1) & 7)) << 4;
    const int arow = r16 * 128 + co;
    const int brow = (32 + 64 * wn + r16) * 128 + co;
    int cb = 0;
    for (int t = 0; t < 21; ++t) {
      BAR();
      STAMP(3, 1 + t);
      const char* sb = sm + cb * K4_STAGE;
      cb = (cb == K4_NB - 1) ? 0 : cb + 1;
      bf16x8 a0 = *(const bf16x8*)(sb + arow);
      bf16x8 bf[4];
#pragma unroll
      for (int n = 0; n < 3; ++n) bf[n] = *(const bf16x8*)(sb + brow + n * 16 * 128);
      bf16x8 a1 = *(const bf16x8*)(sb + arow + 16 * 128);
      if (wn == 0) bf[3] = *(const bf16x8*)(sb + brow + 3 * 16 * 128);
#pragma unroll
      for (int n = 0; n < 3; ++n) acc[n][0] = mfma16(bf[n], a0, acc[n][0]);
#pragma unroll
      for (int n = 0; n < 3; ++n) acc[n][1] = mfma16(bf[n], a1, acc[n][1]);
      if (wn == 0) {
        acc[3][0] = mfma16(bf[3], a0, acc[3][0]);
        acc[3][1] = mfma16(bf[3], a1, acc[3][1]);
      }
    }
    BAR();
    char* rd = sm + kg * (32 * RS);
#pragma unroll
    for (int m = 0; m < 2; ++m)
#pragma unroll
      for (int n = 0; n < 4; ++n)
        if (n < 3 || wn == 0) *(f32x4*)(rd + (16 * m + r16) * RS + (64 * wn + 16 * n + 4 * g) * 4) = acc[n][m];
  }
  __syncthreads();
  {
    const char* p = sm + et * RS + ec * 32;
    f32x4 v0 = *(const f32x4*)(p) + *(const f32x4*)(p + 32 * RS);
    f32x4 v1 = *(const f32x4*)(p + 16) + *(const f32x4*)(p + 32 * RS + 16);
    u32x4 o;
    o[0] = pk2(lo_f(xv[0]) + v0[0], hi_f(xv[0]) + v0[1]);
    o[1] = pk2(lo_f(xv[1]) + v0[2], hi_f(xv[1]) + v0[3]);
    o[2] = pk2(lo_f(xv[2]) + v1[0], hi_f(xv[2]) + v1[1]);
    o[3] = pk2(lo_f(xv[3]) + v1[2], hi_f(xv[3]) + v1[3]);
    *(u32x4*)(out + (size_t)(t0 + et) * D_ + n0 + ec * 8) = o;
  }
  STAMP(3, 31);
}

extern "C" void kernel_launch(void* const* d_in, const int* in_sizes, int n_in, void* d_out, int out_size,
                              void* d_ws, size_t ws_size, hipStream_t stream) {
  const u16* x = (const u16*)d_in[0];
  const u16* wqkv = (const u16*)d_in[1];
  const u16* wphi = (const u16*)d_in[2];
  const u16* wo = (const u16*)d_in[3];
  const u16* win = (const u16*)d_in[5];
  const u16* wsp = (const u16*)d_in[6];
  const u16* bsp = (const u16*)d_in[7];
  const u16* wout = (const u16*)d_in[8];
  char* ws = (char*)d_ws;
  k1_kernel<<<256, 640, 0, stream>>>(x, wqkv, wo, win, wout, wsp, ws);
  k2_kernel<<<256, 512, 0, stream>>>(wphi, ws);
  k3_kernel<<<256, 768, 0, stream>>>(wsp, bsp, wphi, ws);
  k4_kernel<<<256, 448, 0, stream>>>(x, ws, (u16*)d_out);
}
```

```cpp
#include <hip/hip_runtime.h>
#include <hip/hip_bf16.h>
#include <stdint.h>

typedef __attribute__((ext_vector_type(8))) short bf16x8;
typedef __attribute__((ext_vector_type(4))) short s16x4;
typedef __attribute__((ext_vector_type(4))) float f32x4;
typedef __attribute__((ext_vector_type(2))) float f32x2;
typedef __attribute__((ext_vector_type(4))) unsigned u32x4;
typedef __attribute__((ext_vector_type(2))) unsigned u32x2;
typedef __attribute__((ext_vector_type(2))) __bf16 bf2_t;
typedef unsigned short u16;
#define LDS_AS __attribute__((address_space(3)))

constexpr int S_ = 2048, D_ = 448, NQKV = 1344, NIN = 1792, FH = 896, KO = 1344;

__device__ __forceinline__ unsigned pk2(float a, float b) {
  f32x2 v = {a, b};
  return __builtin_bit_cast(unsigned, __builtin_convertvector(v, bf2_t));
}
__device__ __forceinline__ float lo_f(unsigned u) { return __uint_as_float(u << 16); }
__device__ __forceinline__ float hi_f(unsigned u) { return __uint_as_float(u & 0xffff0000u); }
__device__ __forceinline__ float rbf(float a) { return lo_f(pk2(a, 0.f)); }
__device__ __forceinline__ float bf2f(u16 v) { return __uint_as_float(((unsigned)v) << 16); }
__device__ __forceinline__ float dot2(unsigned a, unsigned b, float c) {
  return __builtin_amdgcn_fdot2_f32_bf16(__builtin_bit_cast(bf2_t, a), __builtin_bit_cast(bf2_t, b), c, false);
}

__device__ __forceinline__ float x16_max(float v) {
  auto r = __builtin_amdgcn_permlane16_swap(__float_as_uint(v), __float_as_uint(v), false, false);
  return fmaxf(__uint_as_float(r[0]), __uint_as_float(r[1]));
}
__device__ __forceinline__ float x32_max(float v) {
  auto r = __builtin_amdgcn_permlane32_swap(__float_as_uint(v), __float_as_uint(v), false, false);
  return fmaxf(__uint_as_float(r[0]), __uint_as_float(r[1]));
}
__device__ __forceinline__ float x16_min(float v) {
  auto r = __builtin_amdgcn_permlane16_swap(__float_as_uint(v), __float_as_uint(v), false, false);
  return fminf(__uint_as_float(r[0]), __uint_as_float(r[1]));
}
__device__ __forceinline__ float x32_min(float v) {
  auto r = __builtin_amdgcn_permlane32_swap(__float_as_uint(v), __float_as_uint(v), false, false);
  return fminf(__uint_as_float(r[0]), __uint_as_float(r[1]));
}
__device__ __forceinline__ float x16_add(float v) {
  auto r = __builtin_amdgcn_permlane16_swap(__float_as_uint(v), __float_as_uint(v), false, false);
  return __uint_as_float(r[0]) + __uint_as_float(r[1]);
}
__device__ __forceinline__ float x32_add(float v) {
  auto r = __builtin_amdgcn_permlane32_swap(__float_as_uint(v), __float_as_uint(v), false, false);
  return __uint_as_float(r[0]) + __uint_as_float(r[1]);
}
__device__ __forceinline__ float red8_add(float v) {
  v += __uint_as_float(__builtin_amdgcn_mov_dpp(__float_as_uint(v), 0xB1, 0xf, 0xf, true));
  v += __uint_as_float(__builtin_amdgcn_mov_dpp(__float_as_uint(v), 0x4E, 0xf, 0xf, true));
  v += __uint_as_float(__builtin_amdgcn_mov_dpp(__float_as_uint(v), 0x141, 0xf, 0xf, true));
  return v;
}
__device__ __forceinline__ f32x4 mfma16(bf16x8 a, bf16x8 b, f32x4 c) {
  return __builtin_amdgcn_mfma_f32_16x16x32_bf16(a, b, c, 0, 0, 0);
}
__device__ __forceinline__ unsigned gelu_pk(unsigned p) {
  const f32x2 u = {lo_f(p), hi_f(p)};
  const f32x2 c1 = {-2.3022081986f, -2.3022081986f};
  const f32x2 c2 = {-0.1029432397f, -0.1029432397f};
  f32x2 t = u * u;
  f32x2 a = u * (t * c2 + c1);
  f32x2 d = {1.f + __builtin_amdgcn_exp2f(a[0]), 1.f + __builtin_amdgcn_exp2f(a[1])};
  f32x2 r = {__builtin_amdgcn_rcpf(d[0]), __builtin_amdgcn_rcpf(d[1])};
  f32x2 o = u * r;
  return pk2(o[0], o[1]);
}
#define SCHED() __builtin_amdgcn_sched_barrier(0)
#define BAR() do { SCHED(); asm volatile("" ::: "memory"); __builtin_amdgcn_s_waitcnt(0xC07F); __builtin_amdgcn_s_barrier(); asm volatile("" ::: "memory"); SCHED(); } while (0)
__device__ __forceinline__ void glds16(const void* g, char* l) {
  __builtin_amdgcn_global_load_lds((const unsigned*)g, (LDS_AS unsigned*)l, 16, 0, 0);
}
__device__ __forceinline__ bf16x8 tr_frag(const char* p0, const char* p1) {
  s16x4 a = __builtin_amdgcn_ds_read_tr16_b64_v4i16((LDS_AS s16x4*)p0);
  s16x4 c = __builtin_amdgcn_ds_read_tr16_b64_v4i16((LDS_AS s16x4*)p1);
  return __builtin_shufflevector(a, c, 0, 1, 2, 3, 4, 5, 6, 7);
}

constexpr int YLD = 3136;
constexpr size_t WS_Y = 0;
constexpr size_t WS_Z2N = WS_Y + (size_t)S_ * YLD * 2 + 256;
constexpr size_t WS_OH = WS_Z2N + (size_t)S_ * 896 * 2;
constexpr size_t WS_WT = WS_OH + (size_t)S_ * KO * 2;
constexpr size_t WS_KVP = WS_WT + (size_t)D_ * KO * 2;
constexpr size_t WS_END = WS_KVP + (size_t)8 * 16 * 56 * 32 * 4;

#define STAMP(K, I) do { } while (0)
#define SGB(mask, n) __builtin_amdgcn_sched_group_barrier(mask, n, 0)

__device__ __forceinline__ void phi_map(bf16x8 qf, bf16x8 wp0, bf16x8 wp1, int g, unsigned (&pp)[2][2],
                                        unsigned (&pm)[2][2]) {
  f32x4 z = {0.f, 0.f, 0.f, 0.f};
  f32x4 p0 = mfma16(wp0, qf, z);
  f32x4 p1 = mfma16(wp1, qf, z);
  float v[8];
  {
    unsigned a = pk2(p0[0], p0[1]), b = pk2(p0[2], p0[3]), c = pk2(p1[0], p1[1]), d = pk2(p1[2], p1[3]);
    v[0] = lo_f(a); v[1] = hi_f(a); v[2] = lo_f(b); v[3] = hi_f(b);
    v[4] = lo_f(c); v[5] = hi_f(c); v[6] = lo_f(d); v[7] = hi_f(d);
  }
  const bool valid1 = (g != 3);
  if (!valid1) { v[4] = v[0]; v[5] = v[0]; v[6] = v[0]; v[7] = v[0]; }
  float mx = fmaxf(fmaxf(fmaxf(v[0], v[1]), fmaxf(v[2], v[3])), fmaxf(fmaxf(v[4], v[5]), fmaxf(v[6], v[7])));
  float mn = fminf(fminf(fminf(v[0], v[1]), fminf(v[2], v[3])), fminf(fminf(v[4], v[5]), fminf(v[6], v[7])));
  mx = x32_max(x16_max(mx));
  mn = x32_min(x16_min(mn));
  const float L2E = 1.4426950408889634f;
  const float mxl = mx * L2E, mnl = mn * L2E;
  float ep[8], em[8];
  float sp = 0.f, sm = 0.f;
#pragma unroll
  for (int j = 0; j < 8; ++j) {
    ep[j] = __builtin_amdgcn_exp2f(__builtin_fmaf(v[j], L2E, -mxl));
    em[j] = __builtin_amdgcn_exp2f(__builtin_fmaf(v[j], -L2E, mnl));
    if (j >= 4) { ep[j] = valid1 ? ep[j] : 0.f; em[j] = valid1 ? em[j] : 0.f; }
    sp += ep[j];
    sm += em[j];
  }
  sp = x32_add(x16_add(sp));
  sm = x32_add(x16_add(sm));
  const float rp = __builtin_amdgcn_rcpf(sp), rm = __builtin_amdgcn_rcpf(sm);
  pp[0][0] = pk2(ep[0] * rp, ep[1] * rp); pp[0][1] = pk2(ep[2] * rp, ep[3] * rp);
  pp[1][0] = pk2(ep[4] * rp, ep[5] * rp); pp[1][1] = pk2(ep[6] * rp, ep[7] * rp);
  pm[0][0] = pk2(em[0] * rm, em[1] * rm); pm[0][1] = pk2(em[2] * rm, em[3] * rm);
  pm[1][0] = pk2(em[4] * rm, em[5] * rm); pm[1][1] = pk2(em[6] * rm, em[7] * rm);
}

constexpr int K1_XB = 128 * 896;
constexpr int K1_WST = 32 * 448;
constexpr int K1_LDS = K1_XB + 3 * K1_WST;

__global__ __launch_bounds__(640) void k1_kernel(const u16* __restrict__ x, const u16* __restrict__ wqkv,
                                                  const u16* __restrict__ wo, const u16* __restrict__ win,
                                                  const u16* __restrict__ wout, const u16* __restrict__ wsp,
                                                  char* __restrict__ ws) {
  __shared__ __attribute__((aligned(16))) char sm[K1_LDS];
  const int tid = threadIdx.x, lane = tid & 63, wave = __builtin_amdgcn_readfirstlane(tid >> 6);
  const int b = blockIdx.x;
  STAMP(0, 0);
  if (b >= 224) {
    const int kb = b - 224;
    const int k0 = kb * 64;
    if (kb < 21) {
    u16* T = (u16*)sm;
    constexpr int TS = 456;
    u32x4 v[6];
#pragma unroll
    for (int j = 0; j < 6; ++j) {
      int i = tid + 640 * j;
      if (i >= 64 * 56) i = tid;
      const int r = i / 56, c = i % 56;
      const int k = k0 + r;
      const u16* src = (k < 448) ? (wo + (size_t)k * 448) : (wout + (size_t)(k - 448) * 448);
      v[j] = *(const u32x4*)(src + c * 8);
    }
#pragma unroll
    for (int j = 0; j < 6; ++j) {
      const int i = tid + 640 * j;
      if (i < 64 * 56) *(u32x4*)(T + (i / 56) * TS + (i % 56) * 8) = v[j];
    }
    __syncthreads();
    u16* wT = (u16*)(ws + WS_WT);
    for (int i = tid; i < 448 * 8; i += 640) {
      const int n = i >> 3, c = i & 7;
      u16 t[8];
#pragma unroll
      for (int j = 0; j < 8; ++j) t[j] = T[(c * 8 + j) * TS + n];
      u32x4 o;
#pragma unroll
      for (int j = 0; j < 4; ++j) o[j] = (unsigned)t[2 * j] | ((unsigned)t[2 * j + 1] << 16);
      *(u32x4*)(wT + (size_t)n * KO + k0 + c * 8) = o;
    }
    }
    STAMP(0, 1);
    {
      const char* p = (const char*)wsp + (size_t)kb * 262144 + tid * 16;
      unsigned a = 0;
#pragma unroll
      for (int i = 0; i < 25; ++i) {
        u32x4 v = *(const u32x4*)(p + i * 10240);
        a |= v[0] & v[1] & v[2] & v[3];
      }
      asm volatile("" ::"v"(a));
    }
    STAMP(0, 31);
    return;
  }

  const int xcd = b & 7, idx = b >> 3;
  const int cb = (xcd & 1) * 7 + idx % 7;
  const int rt = (xcd >> 1) * 4 + idx / 7;
  const int row0 = rt * 128;
  char* wring = sm + K1_XB;
  const int g = lane >> 4, r16 = lane & 15;
  const int wm = (wave >> 1) & 3, wn = wave & 1;
  constexpr int RST = 464;

  if (wave >= 8) {
    const int lw = wave - 8;
    const u16* W = (cb < 6) ? (wqkv + 224 * cb) : (win + 224 * (cb - 6));
    const int ldw = (cb < 6) ? NQKV : NIN;
    const u16* src[7];
#pragma unroll
    for (int j = 0; j < 7; ++j) {
      const int o = (lw + 2 * j) * 1024 + lane * 16;
      const int k = o / 448, cp = (o % 448) >> 4;
      const int G = (cp >> 1) ^ ((k >> 3) & 1);
      const int c = 2 * G + (cp & 1);
      src[j] = W + (size_t)k * ldw + 8 * c;
    }
    const size_t wstep = (size_t)32 * ldw;
    char* dst0 = wring + lw * 1024;
#pragma unroll
    for (int s = 0; s < 3; ++s)
#pragma unroll
      for (int j = 0; j < 7; ++j) glds16(src[j] + s * wstep, dst0 + s * K1_WST + j * 2048);
    asm volatile("s_waitcnt vmcnt(14)" ::: "memory");
    __builtin_amdgcn_s_barrier();
    int nb = 0;
    for (int t = 0; t < 14; ++t) {
      if (t + 2 < 14) asm volatile("s_waitcnt vmcnt(7)" ::: "memory");
      else asm volatile("s_waitcnt vmcnt(0)" ::: "memory");
      __builtin_amdgcn_s_barrier();
      if (t + 3 < 14) {
        char* dst = dst0 + nb * K1_WST;
#pragma unroll
        for (int j = 0; j < 7; ++j) glds16(src[j] + (t + 3) * wstep, dst + j * 2048);
        nb = (nb == 2) ? 0 : nb + 1;
      }
    }
    __builtin_amdgcn_s_barrier();
    __builtin_amdgcn_s_barrier();
  } else {
#pragma unroll
    for (int j = 0; j < 14; ++j) {
      const int o = (wave + 8 * j) * 1024 + lane * 16;
      const int row = o / 896, p = (o % 896) >> 4;
      const int c = p ^ ((row >> 1) & 7);
      glds16(x + (size_t)(row0 + row) * D_ + c * 8, sm + (wave + 8 * j) * 1024);
    }
    float sx[2] = {0.f, 0.f}, sq[2] = {0.f, 0.f}, cs[7] = {0.f, 0.f, 0.f, 0.f, 0.f, 0.f, 0.f};
    asm volatile("s_waitcnt vmcnt(0)" ::: "memory");
    f32x4 acc[7][2];
#pragma unroll
    for (int nt = 0; nt < 7; ++nt) { acc[nt][0] = f32x4{0.f, 0.f, 0.f, 0.f}; acc[nt][1] = f32x4{0.f, 0.f, 0.f, 0.f}; }
    const int sw = (r16 >> 1) & 7;
    const char* xbase = sm + (32 * wm + r16) * 896;
    const int xoE = ((g ^ sw) << 4), xoO = (((4 | g) ^ sw) << 4);
    const int wrow = (8 * g + ((lane >> 2) & 3)) * 448 + (lane & 3) * 8 + 7 * wn * 32;
    const int wE = wrow + ((wn & 1) ? -32 * (g & 1) : 32 * (g & 1));
    const int wO = wrow + ((wn & 1) ? 32 * (g & 1) : -32 * (g & 1));
    bf16x8 xa[2], wa[7], xb[2], wb[7];
#define K1_READ(XF, WF, T, BUF)                                                              \
  do {                                                                                       \
    const char* xp_ = xbase + ((T) >> 1) * 128 + (((T)&1) ? xoO : xoE);                      \
    const char* wb_ = wring + (BUF)*K1_WST;                                                  \
    XF[0] = *(const bf16x8*)(xp_);                                                           \
    XF[1] = *(const bf16x8*)(xp_ + 16 * 896);                                                \
    _Pragma("unroll") for (int nt = 0; nt < 7; ++nt) {                                       \
      const char* p_ = wb_ + ((nt & 1) ? wO : wE) + nt * 32;                                 \
      WF[nt] = tr_frag(p_, p_ + 4 * 448);                                                    \
    }                                                                                        \
  } while (0)
#define K1_CS(WF)                                                                            \
  do {                                                                                       \
    if (wm == 0) { _Pragma("unroll") for (int nt = 0; nt < 7; ++nt) cs[nt] = dot2(__builtin_bit_cast(u32x4, WF[nt])[0], 0x3F803F80u, cs[nt]); } \
    else if (wm == 1) { _Pragma("unroll") for (int nt = 0; nt < 7; ++nt) cs[nt] = dot2(__builtin_bit_cast(u32x4, WF[nt])[1], 0x3F803F80u, cs[nt]); } \
    else if (wm == 2) { _Pragma("unroll") for (int nt = 0; nt < 7; ++nt) cs[nt] = dot2(__builtin_bit_cast(u32x4, WF[nt])[2], 0x3F803F80u, cs[nt]); } \
    else { _Pragma("unroll") for (int nt = 0; nt < 7; ++nt) cs[nt] = dot2(__builtin_bit_cast(u32x4, WF[nt])[3], 0x3F803F80u, cs[nt]); } \
    SCHED();                                                                                 \
  } while (0)
#define K1_MMA(XF, WF, T)                                                                    \
  do {                                                                                       \
    _Pragma("unroll") for (int nt = 0; nt < 7; ++nt) acc[nt][0] = mfma16(WF[nt], XF[0], acc[nt][0]); \
    _Pragma("unroll") for (int nt = 0; nt < 7; ++nt) acc[nt][1] = mfma16(WF[nt], XF[1], acc[nt][1]); \
    _Pragma("unroll") for (int mt = 0; mt < 2; ++mt) {                                       \
      const u32x4 u_ = __builtin_bit_cast(u32x4, XF[mt]);                                    \
      _Pragma("unroll") for (int j = 0; j < 4; ++j) {                                        \
        sx[mt] = dot2(u_[j], 0x3F803F80u, sx[mt]);                                           \
        sq[mt] = dot2(u_[j], u_[j], sq[mt]);                                                 \
      }                                                                                      \
    }                                                                                        \
    K1_MIX();                                                                                \
  } while (0)
#define K1_MIX()                                                                             \
  do {                                                                                       \
    SGB(0x100, 2);                                                                           \
    _Pragma("unroll") for (int i_ = 0; i_ < 7; ++i_) { SGB(0x008, 1); SGB(0x100, 2); }       \
    _Pragma("unroll") for (int i_ = 0; i_ < 7; ++i_) { SGB(0x008, 1); SGB(0x002, 2); }       \
  } while (0)
    BAR();
    STAMP(0, 2);
    K1_READ(xa, wa, 0, 0);
    int rb = 1;
#pragma unroll 1
    for (int t = 0; t < 12; t += 2) {
      BAR();
      STAMP(0, 3 + t);
      K1_CS(wa);
      K1_READ(xb, wb, t + 1, rb);
      rb = (rb == 2) ? 0 : rb + 1;
      K1_MMA(xa, wa, t);
      BAR();
      STAMP(0, 4 + t);
      K1_CS(wb);
      K1_READ(xa, wa, t + 2, rb);
      rb = (rb == 2) ? 0 : rb + 1;
      K1_MMA(xb, wb, t + 1);
    }
    BAR();
    K1_CS(wa);
    K1_READ(xb, wb, 13, 1);
    K1_MMA(xa, wa, 12);
    BAR();
    K1_CS(wb);
    K1_MMA(xb, wb, 13);
    BAR();
    STAMP(0, 17);
    float* CS = (float*)(sm + 128 * RST);
#pragma unroll
    for (int nt = 0; nt < 7; ++nt) {
      const float v = x32_add(x16_add(cs[nt]));
      if (g == 0) CS[wm * 224 + 112 * wn + 16 * nt + r16] = v;
    }
    float rs[2], rm[2];
#pragma unroll
    for (int mt = 0; mt < 2; ++mt) {
      const float mean = x32_add(x16_add(sx[mt])) * (1.f / 448.f);
      const float ex2 = x32_add(x16_add(sq[mt])) * (1.f / 448.f);
      const float var = fmaxf(ex2 - mean * mean, 0.f);
      const float sd = rbf(sqrtf(rbf(rbf(var) + 1e-5f)));
      rs[mt] = 1.f / sd;
      rm[mt] = rs[mt] * rbf(mean);
    }
    BAR();
#pragma unroll
    for (int nt = 0; nt < 7; ++nt) {
      const f32x4* cp = (const f32x4*)(CS + 112 * wn + 16 * nt + 4 * g);
      const f32x4 c4 = (cp[0] + cp[56]) + (cp[112] + cp[168]);
#pragma unroll
      for (int mt = 0; mt < 2; ++mt) {
        char* dst = sm + (32 * wm + 16 * mt + r16) * RST + (112 * wn + 4 * g) * 2 + 32 * nt;
        const f32x4 v = acc[nt][mt] * rs[mt] - c4 * rm[mt];
        *(u32x2*)(dst) = u32x2{pk2(v[0], v[1]), pk2(v[2], v[3])};
      }
    }
  }
  __syncthreads();
  u16* dstb = (u16*)(ws + WS_Y) + (size_t)row0 * YLD + 224 * cb;
#pragma unroll
  for (int i = 0; i < 6; ++i) {
    const int id = tid + 640 * i;
    if (id < 128 * 28) {
      const int s = id / 28, c = id % 28;
      *(u32x4*)(dstb + (size_t)s * YLD + c * 8) = *(const u32x4*)(sm + s * RST + c * 16);
    }
  }
  STAMP(0, 31);
}

__device__ __forceinline__ float red64_add(float v) {
  v = red8_add(v);
  v += __uint_as_float(__builtin_amdgcn_mov_dpp(__float_as_uint(v), 0x140, 0xf, 0xf, true));
  return x32_add(x16_add(v));
}

constexpr int K2_PW = 64 * 80 + 32 * 64;
__global__ __launch_bounds__(512) void k2_kernel(const u16* __restrict__ wphi, char* __restrict__ ws) {
  __shared__ __attribute__((aligned(16))) char sm[8 * 56 * 32 * 4 + 2048];
  const int tid = threadIdx.x, lane = tid & 63, wave = __builtin_amdgcn_readfirstlane(tid >> 6);
  const int b = blockIdx.x;
  const u16* Y = (const u16*)(ws + WS_Y);
  STAMP(1, 0);
  if (b < 128) {
    const int h = b & 15, rg = b >> 4;
    const int g = lane >> 4, r16 = lane & 15;
    const int rowb = rg * 256 + wave * 32;
    u32x2 kv_[2][2];
#pragma unroll
    for (int st = 0; st < 2; ++st) {
      const u16* kp = Y + (size_t)(rowb + st * 16 + r16) * YLD + 448 + h * 28 + 8 * g;
      kv_[st][0] = *(const u32x2*)(kp);
      kv_[st][1] = *(const u32x2*)(kp + 4);
    }
    u32x2 vv[4];
#pragma unroll
    for (int i = 0; i < 4; ++i) {
      const int row = (lane >> 3) + 8 * i, pc = lane & 7;
      vv[i] = *(const u32x2*)(Y + (size_t)(rowb + row) * YLD + 896 + h * 28 + 4 * (pc < 7 ? pc : 0));
      if (pc == 7) vv[i] = u32x2{0x00003F80u, 0u};
    }
    u32x4 wraw = {0u, 0u, 0u, 0u};
    if (tid < 98) wraw = *(const u32x4*)(wphi + (size_t)h * 784 + tid * 8);
    char* WP = sm + 8 * 56 * 32 * 4;
    if (tid < 98) *(u32x4*)(WP + tid * 16) = wraw;
    char* PK = sm + wave * K2_PW;
    char* VS = PK + 64 * 80;
    __syncthreads();
    STAMP(1, 1);
    const char* wp = WP + (8 * g + ((lane >> 2) & 3)) * 56 + (lane & 3) * 8;
    bf16x8 wp0 = tr_frag(wp, wp + 4 * 56);
    bf16x8 wp1 = tr_frag(wp + 32, wp + 4 * 56 + 32);
    if (g == 3) {
#pragma unroll
      for (int j = 4; j < 8; ++j) { wp0[j] = 0; wp1[j] = 0; }
    }
    f32x4 kacc[4][2];
#pragma unroll
    for (int a = 0; a < 4; ++a) { kacc[a][0] = f32x4{0.f, 0.f, 0.f, 0.f}; kacc[a][1] = f32x4{0.f, 0.f, 0.f, 0.f}; }
#pragma unroll
    for (int pr = 0; pr < 1; ++pr) {
#pragma unroll
      for (int i = 0; i < 4; ++i) {
        const int row = (lane >> 3) + 8 * i;
        const int slot = 2 * (row & 15) + (row >> 4);
        *(u32x2*)(VS + slot * 64 + (lane & 7) * 8) = vv[4 * pr + i];
      }
      unsigned pp[2][2][2], pm[2][2][2];
#pragma unroll
      for (int mt = 0; mt < 2; ++mt) {
        u32x4 q4 = {kv_[2 * pr + mt][0][0], kv_[2 * pr + mt][0][1], kv_[2 * pr + mt][1][0], kv_[2 * pr + mt][1][1]};
        if (g == 3) { q4[2] = 0u; q4[3] = 0u; }
        phi_map(__builtin_bit_cast(bf16x8, q4), wp0, wp1, g, pp[mt], pm[mt]);
      }
#pragma unroll
      for (int ft = 0; ft < 2; ++ft) {
        if (ft == 0 || g != 3) {
          char* d0 = PK + (16 * ft + 4 * g) * 80 + r16 * 4;
#pragma unroll
          for (int j = 0; j < 2; ++j) {
            *(unsigned*)(d0 + (2 * j) * 80) = (pp[0][ft][j] & 0xffffu) | (pp[1][ft][j] << 16);
            *(unsigned*)(d0 + (2 * j + 1) * 80) = (pp[0][ft][j] >> 16) | (pp[1][ft][j] & 0xffff0000u);
            *(unsigned*)(d0 + (28 + 2 * j) * 80) = (pm[0][ft][j] & 0xffffu) | (pm[1][ft][j] << 16);
            *(unsigned*)(d0 + (28 + 2 * j + 1) * 80) = (pm[0][ft][j] >> 16) | (pm[1][ft][j] & 0xffff0000u);
          }
        }
      }
      __builtin_amdgcn_s_waitcnt(0xC07F);
      __builtin_amdgcn_wave_barrier();
      bf16x8 bfr[2];
      {
        const char* vp = VS + (8 * g + ((lane >> 2) & 3)) * 64 + (lane & 3) * 8;
        bfr[0] = tr_frag(vp, vp + 4 * 64);
        bfr[1] = tr_frag(vp + 32, vp + 4 * 64 + 32);
      }
#pragma unroll
      for (int a = 0; a < 4; ++a) {
        bf16x8 af = *(const bf16x8*)(PK + (16 * a + r16) * 80 + g * 16);
        kacc[a][0] = mfma16(af, bfr[0], kacc[a][0]);
        kacc[a][1] = mfma16(af, bfr[1], kacc[a][1]);
      }
      __builtin_amdgcn_s_waitcnt(0xC07F);
      __builtin_amdgcn_wave_barrier();
    }
    STAMP(1, 2);
    __syncthreads();
    float* red = (float*)sm + wave * (56 * 32);
#pragma unroll
    for (int a = 0; a < 4; ++a)
#pragma unroll
      for (int c = 0; c < 2; ++c)
#pragma unroll
        for (int j = 0; j < 4; ++j) {
          const int f = 16 * a + 4 * g + j;
          if (f < 56) red[f * 32 + 16 * c + r16] = kacc[a][c][j];
        }
    __syncthreads();
    if (tid < 448) {
      const f32x4* rp = (const f32x4*)sm + tid;
      f32x4 s = (rp[0] + rp[448]) + (rp[2 * 448] + rp[3 * 448]);
      s += (rp[4 * 448] + rp[5 * 448]) + (rp[6 * 448] + rp[7 * 448]);
      *((f32x4*)(ws + WS_KVP) + (size_t)(rg * 16 + h) * 448 + tid) = s;
    }
  } else {
    const int row0 = (b - 128) * 16 + wave * 2;
    u16* z2n = (u16*)(ws + WS_Z2N);
    const int le = (lane < 56) ? lane : 0;
    u32x4 v[2][2];
#pragma unroll
    for (int r = 0; r < 2; ++r)
#pragma unroll
      for (int c = 0; c < 2; ++c) v[r][c] = *(const u32x4*)(Y + (size_t)(row0 + r) * YLD + 2240 + (le + 56 * c) * 8);
#pragma unroll
    for (int r = 0; r < 2; ++r) {
      float s = 0.f;
#pragma unroll
      for (int c = 0; c < 2; ++c)
#pragma unroll
        for (int j = 0; j < 4; ++j) {
          v[r][c][j] = gelu_pk(v[r][c][j]);
          s = dot2(v[r][c][j], 0x3F803F80u, s);
        }
      if (lane >= 56) s = 0.f;
      const float mean = red64_add(s) * (1.f / 896.f);
      const float mb = rbf(mean);
      float q = 0.f;
#pragma unroll
      for (int c = 0; c < 2; ++c)
#pragma unroll
        for (int j = 0; j < 4; ++j) {
          const float a = lo_f(v[r][c][j]) - mean, e = hi_f(v[r][c][j]) - mean;
          q += a * a + e * e;
          v[r][c][j] = pk2(lo_f(v[r][c][j]) - mb, hi_f(v[r][c][j]) - mb);
        }
      if (lane >= 56) q = 0.f;
      const float var = red64_add(q) * (1.f / 896.f);
      const float sd = rbf(sqrtf(rbf(rbf(var) + 1e-5f)));
      const float inv = 1.f / sd;
      if (lane < 56) {
#pragma unroll
        for (int c = 0; c < 2; ++c) {
          u32x4 o;
#pragma unroll
          for (int j = 0; j < 4; ++j) o[j] = pk2(lo_f(v[r][c][j]) * inv, hi_f(v[r][c][j]) * inv);
          const int ch = lane + 56 * c, pn = ch / 14, wi = ch % 14;
          const int sr = row0 + r;
          const int pos = (sr & ~31) + ((sr >> 2) & 1) * 16 + ((sr >> 3) & 3) * 4 + (sr & 3);
          *(u32x4*)(z2n + ((size_t)pn * S_ + pos) * 112 + wi * 8) = o;
        }
      }
    }
  }
  STAMP(1, 31);
}

constexpr int K3_STAGE = 64 * 128 + 64 * 224;
constexpr int K3_NB = 6;
constexpr int K3_AUX = K3_NB * K3_STAGE;

template <int NI>
__device__ __forceinline__ void k3_wait_inflight(int stages) {
  switch (stages) {
    case 4: asm volatile("s_waitcnt vmcnt(%0)" ::"n"(4 * NI) : "memory"); break;
    case 3: asm volatile("s_waitcnt vmcnt(%0)" ::"n"(3 * NI) : "memory"); break;
    case 2: asm volatile("s_waitcnt vmcnt(%0)" ::"n"(2 * NI) : "memory"); break;
    case 1: asm volatile("s_waitcnt vmcnt(%0)" ::"n"(1 * NI) : "memory"); break;
    default: asm volatile("s_waitcnt vmcnt(0)" ::: "memory"); break;
  }
}

template <int NI>
__device__ __forceinline__ void k3_loader(char* sm, char* ws, const u16* wsp, const u16* z2n, const u16* Y, u16* oh,
                                          int lw, int lane, int t0, int nt_, int rot, int b) {
  const u16* src[NI];
  int step[NI];
#pragma unroll
  for (int j = 0; j < NI; ++j) {
    const int i = lw + 4 * j;
    if (j < 2) {
      const int row = 8 * i + (lane >> 3);
      const int c = (lane & 7) ^ ((row >> 1) & 7);
      src[j] = wsp + (size_t)(t0 + row) * S_ + c * 8;
      step[j] = 64;
    } else {
      src[j] = z2n + (size_t)nt_ * S_ * 112 + (i - 8) * 512 + lane * 8;
      step[j] = 64 * 112;
    }
  }
  const int g = lane >> 4, r16 = lane & 15;
  const int h = b & 15;
  char* dst0 = sm + lw * 1024;
#pragma unroll
  for (int s = 0; s < K3_NB; ++s) {
    const int ks = (s + rot) & 31;
#pragma unroll
    for (int j = 0; j < NI; ++j) glds16(src[j] + ks * step[j], dst0 + s * K3_STAGE + j * 4096);
  }
  asm volatile("s_waitcnt vmcnt(%0)" ::"n"(5 * NI) : "memory");
  __builtin_amdgcn_s_barrier();
  int nb = 0;
#pragma unroll 1
  for (int t = 0; t < 32; ++t) {
    const int infl = (t + 6 <= 32) ? 4 : (30 - t);
    k3_wait_inflight<NI>(infl);
    __builtin_amdgcn_s_barrier();
    STAMP(2, 5 + (t >> 1));
    if (t + 6 < 32) {
      char* dst = dst0 + nb * K3_STAGE;
      const int ks = (t + 6 + rot) & 31;
#pragma unroll
      for (int j = 0; j < NI; ++j) glds16(src[j] + ks * step[j], dst + j * 4096);
      nb = (nb == K3_NB - 1) ? 0 : nb + 1;
    }
    if (t == 2 || t == 6) {
      const int st = (t == 6) ? 1 : 0;
      const char* KT = sm + K3_AUX;
      const char* WP = KT + 32 * 144;
      bf16x8 af[2][2];
#pragma unroll
      for (int dt = 0; dt < 2; ++dt)
#pragma unroll
        for (int mf = 0; mf < 2; ++mf) af[dt][mf] = *(const bf16x8*)(KT + (16 * dt + r16) * 144 + (32 * mf + 8 * g) * 2);
      const char* wp = WP + (8 * g + ((lane >> 2) & 3)) * 56 + (lane & 3) * 8;
      bf16x8 wp0 = tr_frag(wp, wp + 4 * 56);
      bf16x8 wp1 = tr_frag(wp + 32, wp + 4 * 56 + 32);
      if (g == 3) {
#pragma unroll
        for (int j = 4; j < 8; ++j) { wp0[j] = 0; wp1[j] = 0; }
      }
      u32x4 q4 = *(const u32x4*)(WP + 2048 + (lw * 32 + st * 16 + r16) * 64 + g * 16);
      if (g == 3) { q4[2] = 0u; q4[3] = 0u; }
      unsigned pp[2][2], pm[2][2];
      phi_map(__builtin_bit_cast(bf16x8, q4), wp0, wp1, g, pp, pm);
      u32x4 b1 = {pp[0][0], pp[0][1], pp[1][0], pp[1][1]};
      u32x4 b2 = {pm[0][0], pm[0][1], pm[1][0], pm[1][1]};
      f32x4 a0 = {0.f, 0.f, 0.f, 0.f}, a1 = {0.f, 0.f, 0.f, 0.f};
      a0 = mfma16(af[0][0], __builtin_bit_cast(bf16x8, b1), a0);
      a1 = mfma16(af[1][0], __builtin_bit_cast(bf16x8, b1), a1);
      a0 = mfma16(af[0][1], __builtin_bit_cast(bf16x8, b2), a0);
      a1 = mfma16(af[1][1], __builtin_bit_cast(bf16x8, b2), a1);
      float den = __shfl(a1[0], 48 + r16, 64);
      den = rbf(rbf(den) + 1e-6f);
      const float inv = 1.f / den;
      u16* dst = oh + (size_t)((b >> 4) * 128 + lw * 32 + st * 16 + r16) * KO + h * 28 + 4 * g;
      *(u32x2*)(dst) = u32x2{pk2(rbf(a0[0]) * inv, rbf(a0[1]) * inv), pk2(rbf(a0[2]) * inv, rbf(a0[3]) * inv)};
      if (g != 3)
        *(u32x2*)(dst + 16) = u32x2{pk2(rbf(a1[0]) * inv, rbf(a1[1]) * inv), pk2(rbf(a1[2]) * inv, rbf(a1[3]) * inv)};
    }
  }
}

__global__ __launch_bounds__(768) void k3_kernel(const u16* __restrict__ wsp, const u16* __restrict__ bsp,
                                                  const u16* __restrict__ wphi, char* __restrict__ ws) {
  __shared__ __attribute__((aligned(16))) char sm[K3_NB * K3_STAGE + 32 * 144 + 2048 + 8192];
  const int tid = threadIdx.x, lane = tid & 63, wave = __builtin_amdgcn_readfirstlane(tid >> 6);
  const int b = blockIdx.x;
  const int xcd = b & 7, idx = b >> 3;
  const int mt_ = (xcd >> 1) * 8 + (idx & 7);
  const int nt_ = (xcd & 1) * 4 + (idx >> 3);
  const int rot = (8 * (idx >> 3) + 4 * (idx & 7)) & 31;
  const int t0 = mt_ * 64, f0 = nt_ * 112;
  const u16* z2n = (const u16*)(ws + WS_Z2N);
  const u16* Y = (const u16*)(ws + WS_Y);
  u16* oh = (u16*)(ws + WS_OH);
  constexpr int RS = 464;
  u32x4 zv[2];
  float bb[2];
  STAMP(2, 0);
  if (wave >= 8) {
    const int lw = wave - 8;
    if (lw < 2) k3_loader<6>(sm, ws, wsp, z2n, Y, oh, lw, lane, t0, nt_, rot, b);
    else k3_loader<5>(sm, ws, wsp, z2n, Y, oh, lw, lane, t0, nt_, rot, b);
    {
      const int t = tid / 14, c = tid % 14;
      zv[0] = *(const u32x4*)(Y + (size_t)(t0 + t) * YLD + 1344 + f0 + c * 8);
      bb[0] = bf2f(bsp[t0 + t]);
      zv[1] = zv[0]; bb[1] = bb[0];
    }
    __builtin_amdgcn_s_barrier();
  } else {
    const int g = lane >> 4, r16 = lane & 15;
    {
      const int h = b & 15;
      f32x4 part[8];
      {
        const int te = (tid < 448) ? tid : 0;
        const f32x4* kvp = (const f32x4*)(ws + WS_KVP) + (size_t)h * 448 + te;
#pragma unroll
        for (int p = 0; p < 8; ++p) part[p] = kvp[(size_t)p * 16 * 448];
      }
      u32x4 wraw = {0u, 0u, 0u, 0u};
      if (tid < 98) wraw = *(const u32x4*)(wphi + (size_t)h * 784 + tid * 8);
      u32x2 qv[2];
#pragma unroll
      for (int i = 0; i < 2; ++i) {
        const int id = tid + 512 * i;
        qv[i] = *(const u32x2*)(Y + (size_t)((b >> 4) * 128 + (id >> 3)) * YLD + h * 28 + (id & 7) * 4);
      }
#pragma unroll
      for (int i = 0; i < 2; ++i) {
        int id = tid + 768 * i;
        if (id >= 896) id = tid;
        const int t = id / 14, c = id % 14;
        zv[i] = *(const u32x4*)(Y + (size_t)(t0 + t) * YLD + 1344 + f0 + c * 8);
        bb[i] = bf2f(bsp[t0 + t]);
      }
      char* KT = sm + K3_AUX;
      char* WP = KT + 32 * 144;
      if (tid < 98) *(u32x4*)(WP + tid * 16) = wraw;
      *(u32x2*)(WP + 2048 + tid * 8) = qv[0];
      *(u32x2*)(WP + 2048 + 4096 + tid * 8) = qv[1];
      if (tid < 448) {
        const f32x4 s = ((part[0] + part[1]) + (part[2] + part[3])) + ((part[4] + part[5]) + (part[6] + part[7]));
        const int fp = tid >> 3, d0 = 4 * (tid & 7);
        const int mf = (fp >= 28) ? 1 : 0, f = fp - 28 * mf;
        const int pos = mf * 32 + ((f & 15) >> 2) * 8 + (f & 3) + 4 * (f >> 4);
        const unsigned a = pk2(s[0], s[1]), c = pk2(s[2], s[3]);
        char* d = KT + d0 * 144 + pos * 2;
        *(u16*)(d) = (u16)a; *(u16*)(d + 144) = (u16)(a >> 16);
        *(u16*)(d + 288) = (u16)c; *(u16*)(d + 432) = (u16)(c >> 16);
      } else {
        const int i = tid - 448, d = i >> 1, mf = i & 1;
        *(u32x2*)(KT + d * 144 + (mf * 32 + 28) * 2) = u32x2{0u, 0u};
      }
    }
    STAMP(2, 1);
    const int kg = wave >> 2, wm = (wave >> 1) & 1, wn = (wave & 1) ^ kg;
    f32x4 acc[4][2];
#pragma unroll
    for (int n = 0; n < 4; ++n) { acc[n][0] = f32x4{0.f, 0.f, 0.f, 0.f}; acc[n][1] = f32x4{0.f, 0.f, 0.f, 0.f}; }
    const int arow = (32 * wm + r16) * 128 + (((4 * kg + g) ^ ((r16 >> 1) & 7)) << 4);
    const int brow = 64 * 128 + (32 * kg + 4 * g + ((lane >> 2) & 3)) * 224 + (lane & 3) * 8 + 128 * wn;
#define K3_READ(AF, BF, BUF, NT)                                                               \
  do {                                                                                         \
    const char* sb_ = sm + (BUF)*K3_STAGE;                                                     \
    AF[0] = *(const bf16x8*)(sb_ + arow);                                                      \
    AF[1] = *(const bf16x8*)(sb_ + arow + 2048);                                               \
    _Pragma("unroll") for (int n = 0; n < NT; ++n) {                                           \
      const char* p_ = sb_ + brow + n * 32;                                                    \
      BF[n] = tr_frag(p_, p_ + 16 * 224);                                                      \
    }                                                                                          \
  } while (0)
#define K3_MMA(AF, BF, NT)                                                                     \
  do {                                                                                         \
    _Pragma("unroll") for (int n = 0; n < NT; ++n) acc[n][0] = mfma16(BF[n], AF[0], acc[n][0]); \
    _Pragma("unroll") for (int n = 0; n < NT; ++n) acc[n][1] = mfma16(BF[n], AF[1], acc[n][1]); \
  } while (0)
#define K3_MIX(NT)                                                                             \
  do {                                                                                         \
    SGB(0x100, 2);                                                                             \
    _Pragma("unroll") for (int i_ = 0; i_ < NT; ++i_) { SGB(0x008, 1); SGB(0x100, 2); }        \
  } while (0)
#define K3_LOOP(NT)                                                                            \
  do {                                                                                         \
    bf16x8 aA[2], bA[NT], aB[2], bB[NT];                                                       \
    BAR();                                                                                     \
    STAMP(2, 4);                                                                               \
    K3_READ(aA, bA, 0, NT);                                                                    \
    int rb = 1;                                                                                \
    _Pragma("unroll 1") for (int t = 0; t < 30; t += 2) {                                      \
      BAR();                                                                                   \
      STAMP(2, 5 + (t >> 1));                                                                  \
      K3_READ(aB, bB, rb, NT);                                                                 \
      rb = (rb == K3_NB - 1) ? 0 : rb + 1;                                                     \
      K3_MMA(aA, bA, NT);                                                                      \
      K3_MIX(NT);                                                                              \
      BAR();                                                                                   \
      K3_READ(aA, bA, rb, NT);                                                                 \
      rb = (rb == K3_NB - 1) ? 0 : rb + 1;                                                     \
      K3_MMA(aB, bB, NT);                                                                      \
      K3_MIX(NT);                                                                              \
    }                                                                                          \
    BAR();                                                                                     \
    K3_READ(aB, bB, 1, NT);                                                                    \
    K3_MMA(aA, bA, NT);                                                                        \
    K3_MIX(NT);                                                                                \
    BAR();                                                                                     \
    K3_MMA(aB, bB, NT);                                                                        \
  } while (0)
    if (wn == 0) K3_LOOP(4);
    else K3_LOOP(3);
    BAR();
    STAMP(2, 21);
    char* rd = sm + kg * (64 * RS);
#pragma unroll
    for (int m = 0; m < 2; ++m)
#pragma unroll
      for (int n = 0; n < 4; ++n)
        if (n < 3 || wn == 0) *(f32x4*)(rd + (32 * wm + 16 * m + r16) * RS + (64 * wn + 16 * n + 4 * g) * 4) = acc[n][m];
  }
  __syncthreads();
#pragma unroll
  for (int i = 0; i < 2; ++i) {
    const int id = tid + 768 * i;
    if (id < 896) {
      const int t = id / 14, c = id % 14;
      const char* p = sm + t * RS + c * 32;
      f32x4 v0 = *(const f32x4*)(p) + *(const f32x4*)(p + 64 * RS);
      f32x4 v1 = *(const f32x4*)(p + 16) + *(const f32x4*)(p + 64 * RS + 16);
      u32x4 z;
#pragma unroll
      for (int j = 0; j < 4; ++j) z[j] = gelu_pk(zv[i][j]);
      u32x4 o;
      o[0] = pk2(lo_f(z[0]) * rbf(rbf(v0[0]) + bb[i]), hi_f(z[0]) * rbf(rbf(v0[1]) + bb[i]));
      o[1] = pk2(lo_f(z[1]) * rbf(rbf(v0[2]) + bb[i]), hi_f(z[1]) * rbf(rbf(v0[3]) + bb[i]));
      o[2] = pk2(lo_f(z[2]) * rbf(rbf(v1[0]) + bb[i]), hi_f(z[2]) * rbf(rbf(v1[1]) + bb[i]));
      o[3] = pk2(lo_f(z[3]) * rbf(rbf(v1[2]) + bb[i]), hi_f(z[3]) * rbf(rbf(v1[3]) + bb[i]));
      *(u32x4*)(oh + (size_t)(t0 + t) * KO + 448 + f0 + c * 8) = o;
    }
  }
  STAMP(2, 31);
}

constexpr int K4_STAGE = 144 * 128;
constexpr int K4_NB = 8;
__global__ __launch_bounds__(448) void k4_kernel(const u16* __restrict__ x, const char* __restrict__ ws,
                                                  u16* __restrict__ out) {
  __shared__ __attribute__((aligned(16))) char sm[K4_NB * K4_STAGE];
  const int tid = threadIdx.x, lane = tid & 63, wave = __builtin_amdgcn_readfirstlane(tid >> 6);
  const int b = blockIdx.x;
  const int xcd = b & 7, idx = b >> 3;
  const int mt_ = xcd * 8 + (idx & 7), nt_ = idx >> 3;
  const int t0 = mt_ * 32, n0 = nt_ * 112;
  const u16* oh = (const u16*)(ws + WS_OH);
  const u16* wt = (const u16*)(ws + WS_WT);
  constexpr int RS = 464;
  const int et = tid / 14, ec = tid % 14;
  STAMP(3, 0);
  u32x4 xv = *(const u32x4*)(x + (size_t)(t0 + et) * D_ + n0 + ec * 8);

  if (wave >= 4) {
    const int lw = wave - 4;
    const u16* src[6];
#pragma unroll
    for (int j = 0; j < 6; ++j) {
      const int i = lw + 3 * j;
      const int row = 8 * i + (lane >> 3);
      const int c = (lane & 7) ^ ((row >> 1) & 7);
      const u16* base = (row < 32) ? (oh + (size_t)(t0 + row) * KO) : (wt + (size_t)(n0 + row - 32) * KO);
      src[j] = base + c * 8;
    }
    char* dst0 = sm + lw * 1024;
#pragma unroll
    for (int s = 0; s < K4_NB - 1; ++s)
#pragma unroll
      for (int j = 0; j < 6; ++j) glds16(src[j] + s * 64, dst0 + s * K4_STAGE + j * 3072);
    int nb = K4_NB - 1;
    for (int t = 0; t < 21; ++t) {
      if (t + K4_NB - 1 <= 21) asm volatile("s_waitcnt vmcnt(36)" ::: "memory");
      else {
        const int rem = 20 - t;
        if (rem >= 5) asm volatile("s_waitcnt vmcnt(30)" ::: "memory");
        else if (rem == 4) asm volatile("s_waitcnt vmcnt(24)" ::: "memory");
        else if (rem == 3) asm volatile("s_waitcnt vmcnt(18)" ::: "memory");
        else if (rem == 2) asm volatile("s_waitcnt vmcnt(12)" ::: "memory");
        else if (rem == 1) asm volatile("s_waitcnt vmcnt(6)" ::: "memory");
        else asm volatile("s_waitcnt vmcnt(0)" ::: "memory");
      }
      __builtin_amdgcn_s_barrier();
      if (t + K4_NB - 1 < 21) {
        char* dst = dst0 + nb * K4_STAGE;
#pragma unroll
        for (int j = 0; j < 6; ++j) glds16(src[j] + (t + K4_NB - 1) * 64, dst + j * 3072);
        nb = (nb == K4_NB - 1) ? 0 : nb + 1;
      }
    }
    __builtin_amdgcn_s_barrier();
  } else {
    const int g = lane >> 4, r16 = lane & 15;
    const int kg = wave >> 1, wn = wave & 1;
    f32x4 acc[4][2];
#pragma unroll
    for (int n = 0; n < 4; ++n) { acc[n][0] = f32x4{0.f, 0.f, 0.f, 0.f}; acc[n][1] = f32x4{0.f, 0.f, 0.f, 0.f}; }
    const int co = ((4 * kg + g) ^ ((r16 >> 1) & 7)) << 4;
    const int arow = r16 * 128 + co;
    const int brow = (32 + 64 * wn + r16) * 128 + co;
    int cb = 0;
    for (int t = 0; t < 21; ++t) {
      BAR();
      STAMP(3, 1 + t);
      const char* sb = sm + cb * K4_STAGE;
      cb = (cb == K4_NB - 1) ? 0 : cb + 1;
      bf16x8 a0 = *(const bf16x8*)(sb + arow);
      bf16x8 bf[4];
#pragma unroll
      for (int n = 0; n < 3; ++n) bf[n] = *(const bf16x8*)(sb + brow + n * 16 * 128);
      bf16x8 a1 = *(const bf16x8*)(sb + arow + 16 * 128);
      if (wn == 0) bf[3] = *(const bf16x8*)(sb + brow + 3 * 16 * 128);
#pragma unroll
      for (int n = 0; n < 3; ++n) acc[n][0] = mfma16(bf[n], a0, acc[n][0]);
#pragma unroll
      for (int n = 0; n < 3; ++n) acc[n][1] = mfma16(bf[n], a1, acc[n][1]);
      if (wn == 0) {
        acc[3][0] = mfma16(bf[3], a0, acc[3][0]);
        acc[3][1] = mfma16(bf[3], a1, acc[3][1]);
      }
    }
    BAR();
    char* rd = sm + kg * (32 * RS);
#pragma unroll
    for (int m = 0; m < 2; ++m)
#pragma unroll
      for (int n = 0; n < 4; ++n)
        if (n < 3 || wn == 0) *(f32x4*)(rd + (16 * m + r16) * RS + (64 * wn + 16 * n + 4 * g) * 4) = acc[n][m];
  }
  __syncthreads();
  {
    const char* p = sm + et * RS + ec * 32;
    f32x4 v0 = *(const f32x4*)(p) + *(const f32x4*)(p + 32 * RS);
    f32x4 v1 = *(const f32x4*)(p + 16) + *(const f32x4*)(p + 32 * RS + 16);
    u32x4 o;
    o[0] = pk2(lo_f(xv[0]) + v0[0], hi_f(xv[0]) + v0[1]);
    o[1] = pk2(lo_f(xv[1]) + v0[2], hi_f(xv[1]) + v0[3]);
    o[2] = pk2(lo_f(xv[2]) + v1[0], hi_f(xv[2]) + v1[1]);
    o[3] = pk2(lo_f(xv[3]) + v1[2], hi_f(xv[3]) + v1[3]);
    *(u32x4*)(out + (size_t)(t0 + et) * D_ + n0 + ec * 8) = o;
  }
  STAMP(3, 31);
}

extern "C" void kernel_launch(void* const* d_in, const int* in_sizes, int n_in, void* d_out, int out_size,
                              void* d_ws, size_t ws_size, hipStream_t stream) {
  const u16* x = (const u16*)d_in[0];
  const u16* wqkv = (const u16*)d_in[1];
  const u16* wphi = (const u16*)d_in[2];
  const u16* wo = (const u16*)d_in[3];
  const u16* win = (const u16*)d_in[5];
  const u16* wsp = (const u16*)d_in[6];
  const u16* bsp = (const u16*)d_in[7];
  const u16* wout = (const u16*)d_in[8];
  char* ws = (char*)d_ws;
  k1_kernel<<<256, 640, 0, stream>>>(x, wqkv, wo, win, wout, wsp, ws);
  k2_kernel<<<256, 512, 0, stream>>>(wphi, ws);
  k3_kernel<<<256, 768, 0, stream>>>(wsp, bsp, wphi, ws);
  k4_kernel<<<256, 448, 0, stream>>>(x, ws, (u16*)d_out);
}
```

```cpp
#include <hip/hip_runtime.h>
#include <hip/hip_bf16.h>
#include <stdint.h>

typedef __attribute__((ext_vector_type(8))) short bf16x8;
typedef __attribute__((ext_vector_type(4))) short s16x4;
typedef __attribute__((ext_vector_type(4))) float f32x4;
typedef __attribute__((ext_vector_type(2))) float f32x2;
typedef __attribute__((ext_vector_type(4))) unsigned u32x4;
typedef __attribute__((ext_vector_type(2))) unsigned u32x2;
typedef __attribute__((ext_vector_type(2))) __bf16 bf2_t;
typedef unsigned short u16;
#define LDS_AS __attribute__((address_space(3)))

constexpr int S_ = 2048, D_ = 448, NQKV = 1344, NIN = 1792, FH = 896, KO = 1344;

__device__ __forceinline__ unsigned pk2(float a, float b) {
  f32x2 v = {a, b};
  return __builtin_bit_cast(unsigned, __builtin_convertvector(v, bf2_t));
}
__device__ __forceinline__ float lo_f(unsigned u) { return __uint_as_float(u << 16); }
__device__ __forceinline__ float hi_f(unsigned u) { return __uint_as_float(u & 0xffff0000u); }
__device__ __forceinline__ float rbf(float a) { return lo_f(pk2(a, 0.f)); }
__device__ __forceinline__ float bf2f(u16 v) { return __uint_as_float(((unsigned)v) << 16); }
__device__ __forceinline__ float dot2(unsigned a, unsigned b, float c) {
  return __builtin_amdgcn_fdot2_f32_bf16(__builtin_bit_cast(bf2_t, a), __builtin_bit_cast(bf2_t, b), c, false);
}

__device__ __forceinline__ float x16_max(float v) {
  auto r = __builtin_amdgcn_permlane16_swap(__float_as_uint(v), __float_as_uint(v), false, false);
  return fmaxf(__uint_as_float(r[0]), __uint_as_float(r[1]));
}
__device__ __forceinline__ float x32_max(float v) {
  auto r = __builtin_amdgcn_permlane32_swap(__float_as_uint(v), __float_as_uint(v), false, false);
  return fmaxf(__uint_as_float(r[0]), __uint_as_float(r[1]));
}
__device__ __forceinline__ float x16_min(float v) {
  auto r = __builtin_amdgcn_permlane16_swap(__float_as_uint(v), __float_as_uint(v), false, false);
  return fminf(__uint_as_float(r[0]), __uint_as_float(r[1]));
}
__device__ __forceinline__ float x32_min(float v) {
  auto r = __builtin_amdgcn_permlane32_swap(__float_as_uint(v), __float_as_uint(v), false, false);
  return fminf(__uint_as_float(r[0]), __uint_as_float(r[1]));
}
__device__ __forceinline__ float x16_add(float v) {
  auto r = __builtin_amdgcn_permlane16_swap(__float_as_uint(v), __float_as_uint(v), false, false);
  return __uint_as_float(r[0]) + __uint_as_float(r[1]);
}
__device__ __forceinline__ float x32_add(float v) {
  auto r = __builtin_amdgcn_permlane32_swap(__float_as_uint(v), __float_as_uint(v), false, false);
  return __uint_as_float(r[0]) + __uint_as_float(r[1]);
}
__device__ __forceinline__ float red8_add(float v) {
  v += __uint_as_float(__builtin_amdgcn_mov_dpp(__float_as_uint(v), 0xB1, 0xf, 0xf, true));
  v += __uint_as_float(__builtin_amdgcn_mov_dpp(__float_as_uint(v), 0x4E, 0xf, 0xf, true));
  v += __uint_as_float(__builtin_amdgcn_mov_dpp(__float_as_uint(v), 0x141, 0xf, 0xf, true));
  return v;
}
__device__ __forceinline__ f32x4 mfma16(bf16x8 a, bf16x8 b, f32x4 c) {
  return __builtin_amdgcn_mfma_f32_16x16x32_bf16(a, b, c, 0, 0, 0);
}
__device__ __forceinline__ unsigned gelu_pk(unsigned p) {
  const f32x2 u = {lo_f(p), hi_f(p)};
  const f32x2 c1 = {-2.3022081986f, -2.3022081986f};
  const f32x2 c2 = {-0.1029432397f, -0.1029432397f};
  f32x2 t = u * u;
  f32x2 a = u * (t * c2 + c1);
  f32x2 d = {1.f + __builtin_amdgcn_exp2f(a[0]), 1.f + __builtin_amdgcn_exp2f(a[1])};
  f32x2 r = {__builtin_amdgcn_rcpf(d[0]), __builtin_amdgcn_rcpf(d[1])};
  f32x2 o = u * r;
  return pk2(o[0], o[1]);
}
#define SCHED() __builtin_amdgcn_sched_barrier(0)
#define BAR() do { SCHED(); asm volatile("" ::: "memory"); __builtin_amdgcn_s_waitcnt(0xC07F); __builtin_amdgcn_s_barrier(); asm volatile("" ::: "memory"); SCHED(); } while (0)
__device__ __forceinline__ void glds16(const void* g, char* l) {
  __builtin_amdgcn_global_load_lds((const unsigned*)g, (LDS_AS unsigned*)l, 16, 0, 0);
}
__device__ __forceinline__ bf16x8 tr_frag(const char* p0, const char* p1) {
  s16x4 a = __builtin_amdgcn_ds_read_tr16_b64_v4i16((LDS_AS s16x4*)p0);
  s16x4 c = __builtin_amdgcn_ds_read_tr16_b64_v4i16((LDS_AS s16x4*)p1);
  return __builtin_shufflevector(a, c, 0, 1, 2, 3, 4, 5, 6, 7);
}

constexpr int YLD = 3136;
constexpr size_t WS_Y = 0;
constexpr size_t WS_Z2N = WS_Y + (size_t)S_ * YLD * 2 + 256;
constexpr size_t WS_OH = WS_Z2N + (size_t)S_ * 896 * 2;
constexpr size_t WS_WT = WS_OH + (size_t)S_ * KO * 2;
constexpr size_t WS_KVP = WS_WT + (size_t)D_ * KO * 2;
constexpr size_t WS_END = WS_KVP + (size_t)8 * 16 * 56 * 32 * 4;

#define STAMP(K, I) do { } while (0)
#define SGB(mask, n) __builtin_amdgcn_sched_group_barrier(mask, n, 0)

__device__ __forceinline__ void phi_map(bf16x8 qf, bf16x8 wp0, bf16x8 wp1, int g, unsigned (&pp)[2][2],
                                        unsigned (&pm)[2][2]) {
  f32x4 z = {0.f, 0.f, 0.f, 0.f};
  f32x4 p0 = mfma16(wp0, qf, z);
  f32x4 p1 = mfma16(wp1, qf, z);
  float v[8];
  {
    unsigned a = pk2(p0[0], p0[1]), b = pk2(p0[2], p0[3]), c = pk2(p1[0], p1[1]), d = pk2(p1[2], p1[3]);
    v[0] = lo_f(a); v[1] = hi_f(a); v[2] = lo_f(b); v[3] = hi_f(b);
    v[4] = lo_f(c); v[5] = hi_f(c); v[6] = lo_f(d); v[7] = hi_f(d);
  }
  const bool valid1 = (g != 3);
  if (!valid1) { v[4] = v[0]; v[5] = v[0]; v[6] = v[0]; v[7] = v[0]; }
  float mx = fmaxf(fmaxf(fmaxf(v[0], v[1]), fmaxf(v[2], v[3])), fmaxf(fmaxf(v[4], v[5]), fmaxf(v[6], v[7])));
  float mn = fminf(fminf(fminf(v[0], v[1]), fminf(v[2], v[3])), fminf(fminf(v[4], v[5]), fminf(v[6], v[7])));
  mx = x32_max(x16_max(mx));
  mn = x32_min(x16_min(mn));
  const float L2E = 1.4426950408889634f;
  const float mxl = mx * L2E, mnl = mn * L2E;
  float ep[8], em[8];
  float sp = 0.f, sm = 0.f;
#pragma unroll
  for (int j = 0; j < 8; ++j) {
    ep[j] = __builtin_amdgcn_exp2f(__builtin_fmaf(v[j], L2E, -mxl));
    em[j] = __builtin_amdgcn_exp2f(__builtin_fmaf(v[j], -L2E, mnl));
    if (j >= 4) { ep[j] = valid1 ? ep[j] : 0.f; em[j] = valid1 ? em[j] : 0.f; }
    sp += ep[j];
    sm += em[j];
  }
  sp = x32_add(x16_add(sp));
  sm = x32_add(x16_add(sm));
  const float rp = __builtin_amdgcn_rcpf(sp), rm = __builtin_amdgcn_rcpf(sm);
  pp[0][0] = pk2(ep[0] * rp, ep[1] * rp); pp[0][1] = pk2(ep[2] * rp, ep[3] * rp);
  pp[1][0] = pk2(ep[4] * rp, ep[5] * rp); pp[1][1] = pk2(ep[6] * rp, ep[7] * rp);
  pm[0][0] = pk2(em[0] * rm, em[1] * rm); pm[0][1] = pk2(em[2] * rm, em[3] * rm);
  pm[1][0] = pk2(em[4] * rm, em[5] * rm); pm[1][1] = pk2(em[6] * rm, em[7] * rm);
}

constexpr int K1_XB = 128 * 896;
constexpr int K1_WST = 32 * 448;
constexpr int K1_LDS = K1_XB + 3 * K1_WST;

__global__ __launch_bounds__(640) void k1_kernel(const u16* __restrict__ x, const u16* __restrict__ wqkv,
                                                  const u16* __restrict__ wo, const u16* __restrict__ win,
                                                  const u16* __restrict__ wout, const u16* __restrict__ wsp,
                                                  char* __restrict__ ws) {
  __shared__ __attribute__((aligned(16))) char sm[K1_LDS];
  const int tid = threadIdx.x, lane = tid & 63, wave = __builtin_amdgcn_readfirstlane(tid >> 6);
  const int b = blockIdx.x;
  STAMP(0, 0);
  if (b >= 224) {
    const int kb = b - 224;
    const int k0 = kb * 64;
    if (kb < 21) {
    u16* T = (u16*)sm;
    constexpr int TS = 456;
    u32x4 v[6];
#pragma unroll
    for (int j = 0; j < 6; ++j) {
      int i = tid + 640 * j;
      if (i >= 64 * 56) i = tid;
      const int r = i / 56, c = i % 56;
      const int k = k0 + r;
      const u16* src = (k < 448) ? (wo + (size_t)k * 448) : (wout + (size_t)(k - 448) * 448);
      v[j] = *(const u32x4*)(src + c * 8);
    }
#pragma unroll
    for (int j = 0; j < 6; ++j) {
      const int i = tid + 640 * j;
      if (i < 64 * 56) *(u32x4*)(T + (i / 56) * TS + (i % 56) * 8) = v[j];
    }
    __syncthreads();
    u16* wT = (u16*)(ws + WS_WT);
    for (int i = tid; i < 448 * 8; i += 640) {
      const int n = i >> 3, c = i & 7;
      u16 t[8];
#pragma unroll
      for (int j = 0; j < 8; ++j) t[j] = T[(c * 8 + j) * TS + n];
      u32x4 o;
#pragma unroll
      for (int j = 0; j < 4; ++j) o[j] = (unsigned)t[2 * j] | ((unsigned)t[2 * j + 1] << 16);
      *(u32x4*)(wT + ((size_t)((n >> 4) * 42 + (k0 >> 5) + (c >> 2)) * 64 + (c & 3) * 16 + (n & 15)) * 8) = o;
    }
    }
    STAMP(0, 1);
    {
      const char* p = (const char*)wsp + (size_t)kb * 262144 + tid * 16;
      unsigned a = 0;
#pragma unroll
      for (int i = 0; i < 25; ++i) {
        u32x4 v = *(const u32x4*)(p + i * 10240);
        a |= v[0] & v[1] & v[2] & v[3];
      }
      asm volatile("" ::"v"(a));
    }
    STAMP(0, 31);
    return;
  }

  const int xcd = b & 7, idx = b >> 3;
  const int cb = (xcd & 1) * 7 + idx % 7;
  const int rt = (xcd >> 1) * 4 + idx / 7;
  const int row0 = rt * 128;
  char* wring = sm + K1_XB;
  const int g = lane >> 4, r16 = lane & 15;
  const int wm = (wave >> 1) & 3, wn = wave & 1;
  constexpr int RST = 464;

  if (wave >= 8) {
    const int lw = wave - 8;
    const u16* W = (cb < 6) ? (wqkv + 224 * cb) : (win + 224 * (cb - 6));
    const int ldw = (cb < 6) ? NQKV : NIN;
    const u16* src[7];
#pragma unroll
    for (int j = 0; j < 7; ++j) {
      const int o = (lw + 2 * j) * 1024 + lane * 16;
      const int k = o / 448, cp = (o % 448) >> 4;
      const int G = (cp >> 1) ^ ((k >> 3) & 1);
      const int c = 2 * G + (cp & 1);
      src[j] = W + (size_t)k * ldw + 8 * c;
    }
    const size_t wstep = (size_t)32 * ldw;
    char* dst0 = wring + lw * 1024;
#pragma unroll
    for (int s = 0; s < 3; ++s)
#pragma unroll
      for (int j = 0; j < 7; ++j) glds16(src[j] + s * wstep, dst0 + s * K1_WST + j * 2048);
    asm volatile("s_waitcnt vmcnt(14)" ::: "memory");
    __builtin_amdgcn_s_barrier();
    int nb = 0;
    for (int t = 0; t < 14; ++t) {
      if (t + 2 < 14) asm volatile("s_waitcnt vmcnt(7)" ::: "memory");
      else asm volatile("s_waitcnt vmcnt(0)" ::: "memory");
      __builtin_amdgcn_s_barrier();
      if (t + 3 < 14) {
        char* dst = dst0 + nb * K1_WST;
#pragma unroll
        for (int j = 0; j < 7; ++j) glds16(src[j] + (t + 3) * wstep, dst + j * 2048);
        nb = (nb == 2) ? 0 : nb + 1;
      }
    }
    __builtin_amdgcn_s_barrier();
  } else {
    u32x4 xv[2][7];
    const int rr = lane >> 3, cc = lane & 7;
#pragma unroll
    for (int p = 0; p < 2; ++p) {
      const u16* xp = x + (size_t)(row0 + 16 * wave + 8 * p + rr) * D_ + cc * 8;
#pragma unroll
      for (int i = 0; i < 7; ++i) xv[p][i] = *(const u32x4*)(xp + 64 * i);
    }
#pragma unroll
    for (int p = 0; p < 2; ++p) {
      float s0 = 0.f, s1 = 0.f;
#pragma unroll
      for (int i = 0; i < 7; ++i) {
        s0 = dot2(xv[p][i][0], 0x3F803F80u, s0); s1 = dot2(xv[p][i][1], 0x3F803F80u, s1);
        s0 = dot2(xv[p][i][2], 0x3F803F80u, s0); s1 = dot2(xv[p][i][3], 0x3F803F80u, s1);
      }
      const float mean = red8_add(s0 + s1) * (1.f / 448.f);
      const float mb = rbf(mean);
      float q0 = 0.f, q1 = 0.f;
#pragma unroll
      for (int i = 0; i < 7; ++i)
#pragma unroll
        for (int j = 0; j < 4; ++j) {
          const unsigned d = pk2(lo_f(xv[p][i][j]) - mb, hi_f(xv[p][i][j]) - mb);
          xv[p][i][j] = d;
          if (j & 1) q1 = dot2(d, d, q1); else q0 = dot2(d, d, q0);
        }
      const float q = red8_add(q0 + q1);
      const float dm = mean - mb;
      const float var = fmaxf(q * (1.f / 448.f) - dm * dm, 0.f);
      const float sd = rbf(sqrtf(rbf(rbf(var) + 1e-5f)));
      const float inv = 1.f / sd;
      const int row = 16 * wave + 8 * p + rr;
      char* dst = sm + row * 896 + ((cc ^ ((row >> 1) & 7)) << 4);
#pragma unroll
      for (int i = 0; i < 7; ++i) {
        u32x4 o;
#pragma unroll
        for (int j = 0; j < 4; ++j) o[j] = pk2(lo_f(xv[p][i][j]) * inv, hi_f(xv[p][i][j]) * inv);
        *(u32x4*)(dst + 128 * i) = o;
      }
    }
    STAMP(0, 1);
    f32x4 acc[7][2];
#pragma unroll
    for (int nt = 0; nt < 7; ++nt) { acc[nt][0] = f32x4{0.f, 0.f, 0.f, 0.f}; acc[nt][1] = f32x4{0.f, 0.f, 0.f, 0.f}; }
    const int sw = (r16 >> 1) & 7;
    const char* xbase = sm + (32 * wm + r16) * 896;
    const int xoE = ((g ^ sw) << 4), xoO = (((4 | g) ^ sw) << 4);
    const int wrow = (8 * g + ((lane >> 2) & 3)) * 448 + (lane & 3) * 8 + 7 * wn * 32;
    const int wE = wrow + ((wn & 1) ? -32 * (g & 1) : 32 * (g & 1));
    const int wO = wrow + ((wn & 1) ? 32 * (g & 1) : -32 * (g & 1));
    bf16x8 xa[2], wa[7], xb[2], wb[7];
#define K1_READ(XF, WF, T, BUF)                                                              \
  do {                                                                                       \
    const char* xp_ = xbase + ((T) >> 1) * 128 + (((T)&1) ? xoO : xoE);                      \
    const char* wb_ = wring + (BUF)*K1_WST;                                                  \
    XF[0] = *(const bf16x8*)(xp_);                                                           \
    XF[1] = *(const bf16x8*)(xp_ + 16 * 896);                                                \
    _Pragma("unroll") for (int nt = 0; nt < 7; ++nt) {                                       \
      const char* p_ = wb_ + ((nt & 1) ? wO : wE) + nt * 32;                                 \
      WF[nt] = tr_frag(p_, p_ + 4 * 448);                                                    \
    }                                                                                        \
  } while (0)
#define K1_MMA(XF, WF)                                                                       \
  do {                                                                                       \
    _Pragma("unroll") for (int nt = 0; nt < 7; ++nt) acc[nt][0] = mfma16(WF[nt], XF[0], acc[nt][0]); \
    _Pragma("unroll") for (int nt = 0; nt < 7; ++nt) acc[nt][1] = mfma16(WF[nt], XF[1], acc[nt][1]); \
  } while (0)
#define K1_MIX()                                                                             \
  do {                                                                                       \
    SGB(0x100, 2);                                                                           \
    _Pragma("unroll") for (int i_ = 0; i_ < 14; ++i_) { SGB(0x008, 1); SGB(0x100, 1); }      \
  } while (0)
    BAR();
    STAMP(0, 2);
    K1_READ(xa, wa, 0, 0);
    int rb = 1;
#pragma unroll 1
    for (int t = 0; t < 12; t += 2) {
      BAR();
      STAMP(0, 3 + t);
      K1_READ(xb, wb, t + 1, rb);
      rb = (rb == 2) ? 0 : rb + 1;
      K1_MMA(xa, wa);
      K1_MIX();
      BAR();
      STAMP(0, 4 + t);
      K1_READ(xa, wa, t + 2, rb);
      rb = (rb == 2) ? 0 : rb + 1;
      K1_MMA(xb, wb);
      K1_MIX();
    }
    BAR();
    K1_READ(xb, wb, 13, 1);
    K1_MMA(xa, wa);
    K1_MIX();
    BAR();
    K1_MMA(xb, wb);
    BAR();
    STAMP(0, 17);
#pragma unroll
    for (int mt = 0; mt < 2; ++mt) {
      char* dst = sm + (32 * wm + 16 * mt + r16) * RST + (112 * wn + 4 * g) * 2;
#pragma unroll
      for (int nt = 0; nt < 7; ++nt)
        *(u32x2*)(dst + 32 * nt) = u32x2{pk2(acc[nt][mt][0], acc[nt][mt][1]), pk2(acc[nt][mt][2], acc[nt][mt][3])};
    }
  }
  __syncthreads();
  u16* dstb = (u16*)(ws + WS_Y) + (size_t)row0 * YLD + 224 * cb;
#pragma unroll
  for (int i = 0; i < 6; ++i) {
    const int id = tid + 640 * i;
    if (id < 128 * 28) {
      const int s = id / 28, c = id % 28;
      *(u32x4*)(dstb + (size_t)s * YLD + c * 8) = *(const u32x4*)(sm + s * RST + c * 16);
    }
  }
  STAMP(0, 31);
}

__device__ __forceinline__ float red64_add(float v) {
  v = red8_add(v);
  v += __uint_as_float(__builtin_amdgcn_mov_dpp(__float_as_uint(v), 0x140, 0xf, 0xf, true));
  return x32_add(x16_add(v));
}

constexpr int K2_PW = 64 * 80 + 32 * 64;
__global__ __launch_bounds__(512) void k2_kernel(const u16* __restrict__ wphi, char* __restrict__ ws) {
  __shared__ __attribute__((aligned(16))) char sm[8 * 56 * 32 * 4 + 2048];
  const int tid = threadIdx.x, lane = tid & 63, wave = __builtin_amdgcn_readfirstlane(tid >> 6);
  const int b = blockIdx.x;
  const u16* Y = (const u16*)(ws + WS_Y);
  STAMP(1, 0);
  if (b < 128) {
    const int h = b & 15, rg = b >> 4;
    const int g = lane >> 4, r16 = lane & 15;
    const int rowb = rg * 256 + wave * 32;
    u32x2 kv_[2][2];
#pragma unroll
    for (int st = 0; st < 2; ++st) {
      const u16* kp = Y + (size_t)(rowb + st * 16 + r16) * YLD + 448 + h * 28 + 8 * g;
      kv_[st][0] = *(const u32x2*)(kp);
      kv_[st][1] = *(const u32x2*)(kp + 4);
    }
    u32x2 vv[4];
#pragma unroll
    for (int i = 0; i < 4; ++i) {
      const int row = (lane >> 3) + 8 * i, pc = lane & 7;
      vv[i] = *(const u32x2*)(Y + (size_t)(rowb + row) * YLD + 896 + h * 28 + 4 * (pc < 7 ? pc : 0));
      if (pc == 7) vv[i] = u32x2{0x00003F80u, 0u};
    }
    u32x4 wraw = {0u, 0u, 0u, 0u};
    if (tid < 98) wraw = *(const u32x4*)(wphi + (size_t)h * 784 + tid * 8);
    char* WP = sm + 8 * 56 * 32 * 4;
    if (tid < 98) *(u32x4*)(WP + tid * 16) = wraw;
    char* PK = sm + wave * K2_PW;
    char* VS = PK + 64 * 80;
    __syncthreads();
    STAMP(1, 1);
    const char* wp = WP + (8 * g + ((lane >> 2) & 3)) * 56 + (lane & 3) * 8;
    bf16x8 wp0 = tr_frag(wp, wp + 4 * 56);
    bf16x8 wp1 = tr_frag(wp + 32, wp + 4 * 56 + 32);
    if (g == 3) {
#pragma unroll
      for (int j = 4; j < 8; ++j) { wp0[j] = 0; wp1[j] = 0; }
    }
    f32x4 kacc[4][2];
#pragma unroll
    for (int a = 0; a < 4; ++a) { kacc[a][0] = f32x4{0.f, 0.f, 0.f, 0.f}; kacc[a][1] = f32x4{0.f, 0.f, 0.f, 0.f}; }
#pragma unroll
    for (int pr = 0; pr < 1; ++pr) {
#pragma unroll
      for (int i = 0; i < 4; ++i) {
        const int row = (lane >> 3) + 8 * i;
        const int slot = 2 * (row & 15) + (row >> 4);
        *(u32x2*)(VS + slot * 64 + (lane & 7) * 8) = vv[4 * pr + i];
      }
      unsigned pp[2][2][2], pm[2][2][2];
#pragma unroll
      for (int mt = 0; mt < 2; ++mt) {
        u32x4 q4 = {kv_[2 * pr + mt][0][0], kv_[2 * pr + mt][0][1], kv_[2 * pr + mt][1][0], kv_[2 * pr + mt][1][1]};
        if (g == 3) { q4[2] = 0u; q4[3] = 0u; }
        phi_map(__builtin_bit_cast(bf16x8, q4), wp0, wp1, g, pp[mt], pm[mt]);
      }
#pragma unroll
      for (int ft = 0; ft < 2; ++ft) {
        if (ft == 0 || g != 3) {
          char* d0 = PK + (16 * ft + 4 * g) * 80 + r16 * 4;
#pragma unroll
          for (int j = 0; j < 2; ++j) {
            *(unsigned*)(d0 + (2 * j) * 80) = (pp[0][ft][j] & 0xffffu) | (pp[1][ft][j] << 16);
            *(unsigned*)(d0 + (2 * j + 1) * 80) = (pp[0][ft][j] >> 16) | (pp[1][ft][j] & 0xffff0000u);
            *(unsigned*)(d0 + (28 + 2 * j) * 80) = (pm[0][ft][j] & 0xffffu) | (pm[1][ft][j] << 16);
            *(unsigned*)(d0 + (28 + 2 * j + 1) * 80) = (pm[0][ft][j] >> 16) | (pm[1][ft][j] & 0xffff0000u);
          }
        }
      }
      __builtin_amdgcn_s_waitcnt(0xC07F);
      __builtin_amdgcn_wave_barrier();
      bf16x8 bfr[2];
      {
        const char* vp = VS + (8 * g + ((lane >> 2) & 3)) * 64 + (lane & 3) * 8;
        bfr[0] = tr_frag(vp, vp + 4 * 64);
        bfr[1] = tr_frag(vp + 32, vp + 4 * 64 + 32);
      }
#pragma unroll
      for (int a = 0; a < 4; ++a) {
        bf16x8 af = *(const bf16x8*)(PK + (16 * a + r16) * 80 + g * 16);
        kacc[a][0] = mfma16(af, bfr[0], kacc[a][0]);
        kacc[a][1] = mfma16(af, bfr[1], kacc[a][1]);
      }
      __builtin_amdgcn_s_waitcnt(0xC07F);
      __builtin_amdgcn_wave_barrier();
    }
    STAMP(1, 2);
    __syncthreads();
    float* red = (float*)sm + wave * (56 * 32);
#pragma unroll
    for (int a = 0; a < 4; ++a)
#pragma unroll
      for (int c = 0; c < 2; ++c)
#pragma unroll
        for (int j = 0; j < 4; ++j) {
          const int f = 16 * a + 4 * g + j;
          if (f < 56) red[f * 32 + 16 * c + r16] = kacc[a][c][j];
        }
    __syncthreads();
    if (tid < 448) {
      const f32x4* rp = (const f32x4*)sm + tid;
      f32x4 s = (rp[0] + rp[448]) + (rp[2 * 448] + rp[3 * 448]);
      s += (rp[4 * 448] + rp[5 * 448]) + (rp[6 * 448] + rp[7 * 448]);
      *((f32x4*)(ws + WS_KVP) + (size_t)(rg * 16 + h) * 448 + tid) = s;
    }
  } else {
    const int row0 = (b - 128) * 16 + wave * 2;
    u16* z2n = (u16*)(ws + WS_Z2N);
    const int le = (lane < 56) ? lane : 0;
    u32x4 v[2][2];
#pragma unroll
    for (int r = 0; r < 2; ++r)
#pragma unroll
      for (int c = 0; c < 2; ++c) v[r][c] = *(const u32x4*)(Y + (size_t)(row0 + r) * YLD + 2240 + (le + 56 * c) * 8);
#pragma unroll
    for (int r = 0; r < 2; ++r) {
      float s = 0.f;
#pragma unroll
      for (int c = 0; c < 2; ++c)
#pragma unroll
        for (int j = 0; j < 4; ++j) {
          v[r][c][j] = gelu_pk(v[r][c][j]);
          s = dot2(v[r][c][j], 0x3F803F80u, s);
        }
      if (lane >= 56) s = 0.f;
      const float mean = red64_add(s) * (1.f / 896.f);
      const float mb = rbf(mean);
      float q = 0.f;
#pragma unroll
      for (int c = 0; c < 2; ++c)
#pragma unroll
        for (int j = 0; j < 4; ++j) {
          const float a = lo_f(v[r][c][j]) - mean, e = hi_f(v[r][c][j]) - mean;
          q += a * a + e * e;
          v[r][c][j] = pk2(lo_f(v[r][c][j]) - mb, hi_f(v[r][c][j]) - mb);
        }
      if (lane >= 56) q = 0.f;
      const float var = red64_add(q) * (1.f / 896.f);
      const float sd = rbf(sqrtf(rbf(rbf(var) + 1e-5f)));
      const float inv = 1.f / sd;
      if (lane < 56) {
#pragma unroll
        for (int c = 0; c < 2; ++c) {
          u32x4 o;
#pragma unroll
          for (int j = 0; j < 4; ++j) o[j] = pk2(lo_f(v[r][c][j]) * inv, hi_f(v[r][c][j]) * inv);
          *(u32x4*)(sm + (wave * 2 + r) * 1808 + (lane + 56 * c) * 16) = o;
        }
      }
    }
    __syncthreads();
    {
      const int rb0 = (b - 128) * 16;
      const int kb = rb0 >> 5, half = (rb0 >> 4) & 1;
      const int g = lane >> 4;
#pragma unroll
      for (int i = 0; i < 4; ++i) {
        const int u = wave + 8 * i;
        if (u < 28) {
          const int T = 2 * u + (g >> 1), gp = g & 1;
          const char* p = sm + (8 * gp + ((lane >> 2) & 3)) * 1808 + T * 32 + (lane & 3) * 8;
          bf16x8 fr = tr_frag(p, p + 4 * 1808);
          const int pn = T / 7, n = T % 7;
          *(bf16x8*)(z2n + ((size_t)((pn * 64 + kb) * 7 + n) * 64 + (2 * half + gp) * 16 + (lane & 15)) * 8) = fr;
        }
      }
    }
  }
  STAMP(1, 31);
}

constexpr int K3_RS = 464;
__global__ __launch_bounds__(512) void k3_kernel(const u16* __restrict__ wsp, const u16* __restrict__ bsp,
                                                  const u16* __restrict__ wphi, char* __restrict__ ws) {
  __shared__ __attribute__((aligned(16))) char sm[4 * 64 * K3_RS];
  const int tid = threadIdx.x, lane = tid & 63, wave = __builtin_amdgcn_readfirstlane(tid >> 6);
  const int b = blockIdx.x;
  const int xcd = b & 7, idx = b >> 3;
  const int mt_ = (xcd >> 1) * 8 + (idx & 7);
  const int nt_ = (xcd & 1) * 4 + (idx >> 3);
  const int t0 = mt_ * 64, f0 = nt_ * 112;
  const u16* z2n = (const u16*)(ws + WS_Z2N);
  const u16* Y = (const u16*)(ws + WS_Y);
  u16* oh = (u16*)(ws + WS_OH);
  const int g = lane >> 4, r16 = lane & 15;
  STAMP(2, 0);
  const int h = b & 15, srow = (b >> 4) * 128 + wave * 16 + r16;
  f32x4 part[8];
  {
    const int te = (tid < 448) ? tid : 0;
    const f32x4* kvp = (const f32x4*)(ws + WS_KVP) + (size_t)h * 448 + te;
#pragma unroll
    for (int p = 0; p < 8; ++p) part[p] = kvp[(size_t)p * 16 * 448];
  }
  u32x4 wraw = {0u, 0u, 0u, 0u};
  if (tid < 98) wraw = *(const u32x4*)(wphi + (size_t)h * 784 + tid * 8);
  const u16* qp = Y + (size_t)srow * YLD + h * 28 + 8 * g;
  const u32x2 q0 = *(const u32x2*)(qp), q1 = *(const u32x2*)(qp + 4);
  SCHED();
  const u16* ap = wsp + (size_t)(t0 + r16) * S_ + wave * 256 + 8 * g;
  const u16* bp = z2n + ((size_t)(nt_ * 64 + wave * 8) * 7 * 64 + lane) * 8;
  bf16x8 a0[4], b0[7], a1[4], b1[7], a2[4], b2[7];
#define K3_LOAD(AF, BF, KS)                                                                     \
  do {                                                                                          \
    _Pragma("unroll") for (int m = 0; m < 4; ++m) AF[m] = *(const bf16x8*)(ap + (size_t)m * 16 * S_ + (KS)*32); \
    _Pragma("unroll") for (int n = 0; n < 7; ++n) BF[n] = *(const bf16x8*)(bp + ((KS)*7 + n) * 512); \
  } while (0)
  K3_LOAD(a0, b0, 0);
  K3_LOAD(a1, b1, 1);
  SCHED();
  {
    char* KT = sm;
    char* WP = KT + 32 * 144;
    if (tid < 98) *(u32x4*)(WP + tid * 16) = wraw;
    if (tid < 448) {
      const f32x4 s = ((part[0] + part[1]) + (part[2] + part[3])) + ((part[4] + part[5]) + (part[6] + part[7]));
      const int fp = tid >> 3, d0 = 4 * (tid & 7);
      const int mf = (fp >= 28) ? 1 : 0, f = fp - 28 * mf;
      const int pos = mf * 32 + ((f & 15) >> 2) * 8 + (f & 3) + 4 * (f >> 4);
      const unsigned a = pk2(s[0], s[1]), c = pk2(s[2], s[3]);
      char* d = KT + d0 * 144 + pos * 2;
      *(u16*)(d) = (u16)a; *(u16*)(d + 144) = (u16)(a >> 16);
      *(u16*)(d + 288) = (u16)c; *(u16*)(d + 432) = (u16)(c >> 16);
    } else {
      const int i = tid - 448, d = i >> 1, mf = i & 1;
      *(u32x2*)(KT + d * 144 + (mf * 32 + 28) * 2) = u32x2{0u, 0u};
    }
    STAMP(2, 1);
    BAR();
    bf16x8 af[2][2];
#pragma unroll
    for (int dt = 0; dt < 2; ++dt)
#pragma unroll
      for (int mf = 0; mf < 2; ++mf) af[dt][mf] = *(const bf16x8*)(KT + (16 * dt + r16) * 144 + (32 * mf + 8 * g) * 2);
    const char* wp = WP + (8 * g + ((lane >> 2) & 3)) * 56 + (lane & 3) * 8;
    bf16x8 wp0 = tr_frag(wp, wp + 4 * 56);
    bf16x8 wp1 = tr_frag(wp + 32, wp + 4 * 56 + 32);
    if (g == 3) {
#pragma unroll
      for (int j = 4; j < 8; ++j) { wp0[j] = 0; wp1[j] = 0; }
    }
    u32x4 q4 = {q0[0], q0[1], q1[0], q1[1]};
    if (g == 3) { q4[2] = 0u; q4[3] = 0u; }
    unsigned pp[2][2], pm[2][2];
    phi_map(__builtin_bit_cast(bf16x8, q4), wp0, wp1, g, pp, pm);
    u32x4 c1 = {pp[0][0], pp[0][1], pp[1][0], pp[1][1]};
    u32x4 c2 = {pm[0][0], pm[0][1], pm[1][0], pm[1][1]};
    f32x4 o0 = {0.f, 0.f, 0.f, 0.f}, o1 = {0.f, 0.f, 0.f, 0.f};
    o0 = mfma16(af[0][0], __builtin_bit_cast(bf16x8, c1), o0);
    o1 = mfma16(af[1][0], __builtin_bit_cast(bf16x8, c1), o1);
    o0 = mfma16(af[0][1], __builtin_bit_cast(bf16x8, c2), o0);
    o1 = mfma16(af[1][1], __builtin_bit_cast(bf16x8, c2), o1);
    float den = __shfl(o1[0], 48 + r16, 64);
    den = rbf(rbf(den) + 1e-6f);
    const float inv = 1.f / den;
    {
      const int k = h * 28 + 4 * g;
      u16* dst = oh + ((size_t)((srow >> 4) * 42 + (k >> 5)) * 64 + ((k >> 3) & 3) * 16 + r16) * 8 + (k & 7);
      *(u32x2*)(dst) = u32x2{pk2(rbf(o0[0]) * inv, rbf(o0[1]) * inv), pk2(rbf(o0[2]) * inv, rbf(o0[3]) * inv)};
    }
    if (g != 3) {
      const int k = h * 28 + 16 + 4 * g;
      u16* dst = oh + ((size_t)((srow >> 4) * 42 + (k >> 5)) * 64 + ((k >> 3) & 3) * 16 + r16) * 8 + (k & 7);
      *(u32x2*)(dst) = u32x2{pk2(rbf(o1[0]) * inv, rbf(o1[1]) * inv), pk2(rbf(o1[2]) * inv, rbf(o1[3]) * inv)};
    }
  }
  STAMP(2, 2);
  SCHED();
  f32x4 acc[7][4];
#pragma unroll
  for (int n = 0; n < 7; ++n)
#pragma unroll
    for (int m = 0; m < 4; ++m) acc[n][m] = f32x4{0.f, 0.f, 0.f, 0.f};
#define K3_MMA(AF, BF)                                                                          \
  do {                                                                                          \
    _Pragma("unroll") for (int n = 0; n < 7; ++n)                                               \
      _Pragma("unroll") for (int m = 0; m < 4; ++m) acc[n][m] = mfma16(BF[n], AF[m], acc[n][m]); \
  } while (0)
#define K3_MIX()                                                                                \
  do {                                                                                          \
    _Pragma("unroll") for (int i_ = 0; i_ < 11; ++i_) { SGB(0x020, 1); SGB(0x008, 2); }         \
    SCHED();                                                                                    \
  } while (0)
  K3_LOAD(a2, b2, 2); K3_MMA(a0, b0); K3_MIX();
  K3_LOAD(a0, b0, 3); K3_MMA(a1, b1); K3_MIX();
  K3_LOAD(a1, b1, 4); K3_MMA(a2, b2); K3_MIX();
  STAMP(2, 3);
  K3_LOAD(a2, b2, 5); K3_MMA(a0, b0); K3_MIX();
  K3_LOAD(a0, b0, 6); K3_MMA(a1, b1); K3_MIX();
  K3_LOAD(a1, b1, 7); K3_MMA(a2, b2); K3_MIX();
  u32x4 zv[2];
  float bb[2];
#pragma unroll
  for (int i = 0; i < 2; ++i) {
    int id = tid + 512 * i;
    if (id >= 896) id = tid;
    const int t = id / 14, c = id % 14;
    zv[i] = *(const u32x4*)(Y + (size_t)(t0 + t) * YLD + 1344 + f0 + c * 8);
    bb[i] = bf2f(bsp[t0 + t]);
  }
  SCHED();
  K3_MMA(a0, b0);
  K3_MMA(a1, b1);
  STAMP(2, 4);
  char* rd = sm + (wave & 3) * (64 * K3_RS) + r16 * K3_RS + 16 * g;
  BAR();
  if (wave >= 4) {
#pragma unroll
    for (int n = 0; n < 7; ++n)
#pragma unroll
      for (int m = 0; m < 4; ++m) *(f32x4*)(rd + 16 * m * K3_RS + 64 * n) = acc[n][m];
  }
  BAR();
  if (wave < 4) {
#pragma unroll
    for (int n = 0; n < 7; ++n)
#pragma unroll
      for (int m = 0; m < 4; ++m) {
        f32x4* p = (f32x4*)(rd + 16 * m * K3_RS + 64 * n);
        *p = *p + acc[n][m];
      }
  }
  BAR();
  STAMP(2, 5);
#pragma unroll
  for (int i = 0; i < 2; ++i) {
    const int id = tid + 512 * i;
    if (id < 896) {
      const int t = id / 14, c = id % 14;
      const char* p = sm + t * K3_RS + c * 32;
      f32x4 v0 = (*(const f32x4*)(p) + *(const f32x4*)(p + 64 * K3_RS)) +
                 (*(const f32x4*)(p + 128 * K3_RS) + *(const f32x4*)(p + 192 * K3_RS));
      f32x4 v1 = (*(const f32x4*)(p + 16) + *(const f32x4*)(p + 64 * K3_RS + 16)) +
                 (*(const f32x4*)(p + 128 * K3_RS + 16) + *(const f32x4*)(p + 192 * K3_RS + 16));
      u32x4 z;
#pragma unroll
      for (int j = 0; j < 4; ++j) z[j] = gelu_pk(zv[i][j]);
      u32x4 o;
      o[0] = pk2(lo_f(z[0]) * rbf(rbf(v0[0]) + bb[i]), hi_f(z[0]) * rbf(rbf(v0[1]) + bb[i]));
      o[1] = pk2(lo_f(z[1]) * rbf(rbf(v0[2]) + bb[i]), hi_f(z[1]) * rbf(rbf(v0[3]) + bb[i]));
      o[2] = pk2(lo_f(z[2]) * rbf(rbf(v1[0]) + bb[i]), hi_f(z[2]) * rbf(rbf(v1[1]) + bb[i]));
      o[3] = pk2(lo_f(z[3]) * rbf(rbf(v1[2]) + bb[i]), hi_f(z[3]) * rbf(rbf(v1[3]) + bb[i]));
      const int k = 448 + f0 + c * 8, tt = t0 + t;
      *(u32x4*)(oh + ((size_t)((tt >> 4) * 42 + (k >> 5)) * 64 + ((k >> 3) & 3) * 16 + (tt & 15)) * 8) = o;
    }
  }
  STAMP(2, 31);
}

__global__ __launch_bounds__(384) void k4_kernel(const u16* __restrict__ x, const char* __restrict__ ws,
                                                  u16* __restrict__ out) {
  __shared__ __attribute__((aligned(16))) char sm[6 * 32 * K3_RS];
  const int tid = threadIdx.x, lane = tid & 63, wave = __builtin_amdgcn_readfirstlane(tid >> 6);
  const int b = blockIdx.x;
  const int xcd = b & 7, idx = b >> 3;
  const int mt_ = xcd * 8 + (idx & 7), nt_ = idx >> 3;
  const int t0 = mt_ * 32, n0 = nt_ * 112;
  const int g = lane >> 4, r16 = lane & 15;
  STAMP(3, 0);
  const u16* ap = (const u16*)(ws + WS_OH) + ((size_t)(mt_ * 2 * 42 + wave * 7) * 64 + lane) * 8;
  const u16* bp = (const u16*)(ws + WS_WT) + ((size_t)(nt_ * 7 * 42 + wave * 7) * 64 + lane) * 8;
  bf16x8 a0[2], b0[7], a1[2], b1[7], a2[2], b2[7], a3[2], b3[7];
#define K4_LOAD(AF, BF, KS)                                                                     \
  do {                                                                                          \
    _Pragma("unroll") for (int m = 0; m < 2; ++m) AF[m] = *(const bf16x8*)(ap + (m * 42 + (KS)) * 512); \
    _Pragma("unroll") for (int n = 0; n < 7; ++n) BF[n] = *(const bf16x8*)(bp + (n * 42 + (KS)) * 512); \
  } while (0)
  K4_LOAD(a0, b0, 0);
  K4_LOAD(a1, b1, 1);
  K4_LOAD(a2, b2, 2);
  K4_LOAD(a3, b3, 3);
  u32x4 xv[2];
#pragma unroll
  for (int i = 0; i < 2; ++i) {
    int id = tid + 384 * i;
    if (id >= 448) id = tid;
    xv[i] = *(const u32x4*)(x + (size_t)(t0 + id / 14) * D_ + n0 + (id % 14) * 8);
  }
  SCHED();
  f32x4 acc[7][2];
#pragma unroll
  for (int n = 0; n < 7; ++n) { acc[n][0] = f32x4{0.f, 0.f, 0.f, 0.f}; acc[n][1] = f32x4{0.f, 0.f, 0.f, 0.f}; }
#define K4_MMA(AF, BF)                                                                          \
  do {                                                                                          \
    _Pragma("unroll") for (int n = 0; n < 7; ++n) {                                             \
      acc[n][0] = mfma16(BF[n], AF[0], acc[n][0]);                                              \
      acc[n][1] = mfma16(BF[n], AF[1], acc[n][1]);                                              \
    }                                                                                           \
    SCHED();                                                                                    \
  } while (0)
  K4_MMA(a0, b0); K4_LOAD(a0, b0, 4); SCHED();
  K4_MMA(a1, b1); K4_LOAD(a1, b1, 5); SCHED();
  K4_MMA(a2, b2); K4_LOAD(a2, b2, 6); SCHED();
  K4_MMA(a3, b3);
  K4_MMA(a0, b0);
  K4_MMA(a1, b1);
  K4_MMA(a2, b2);
  STAMP(3, 1);
  char* rd = sm + wave * (32 * K3_RS) + r16 * K3_RS + 16 * g;
#pragma unroll
  for (int n = 0; n < 7; ++n)
#pragma unroll
    for (int m = 0; m < 2; ++m) *(f32x4*)(rd + 16 * m * K3_RS + 64 * n) = acc[n][m];
  __syncthreads();
#pragma unroll
  for (int i = 0; i < 2; ++i) {
    const int id = tid + 384 * i;
    if (id < 448) {
      const int et = id / 14, ec = id % 14;
      const char* p = sm + et * K3_RS + ec * 32;
      f32x4 v0 = {0.f, 0.f, 0.f, 0.f}, v1 = {0.f, 0.f, 0.f, 0.f};
#pragma unroll
      for (int w = 0; w < 6; ++w) {
        v0 += *(const f32x4*)(p + w * 32 * K3_RS);
        v1 += *(const f32x4*)(p + w * 32 * K3_RS + 16);
      }
      u32x4 o;
      o[0] = pk2(lo_f(xv[i][0]) + v0[0], hi_f(xv[i][0]) + v0[1]);
      o[1] = pk2(lo_f(xv[i][1]) + v0[2], hi_f(xv[i][1]) + v0[3]);
      o[2] = pk2(lo_f(xv[i][2]) + v1[0], hi_f(xv[i][2]) + v1[1]);
      o[3] = pk2(lo_f(xv[i][3]) + v1[2], hi_f(xv[i][3]) + v1[3]);
      *(u32x4*)(out + (size_t)(t0 + et) * D_ + n0 + ec * 8) = o;
    }
  }
  STAMP(3, 31);
}

extern "C" void kernel_launch(void* const* d_in, const int* in_sizes, int n_in, void* d_out, int out_size,
                              void* d_ws, size_t ws_size, hipStream_t stream) {
  const u16* x = (const u16*)d_in[0];
  const u16* wqkv = (const u16*)d_in[1];
  const u16* wphi = (const u16*)d_in[2];
  const u16* wo = (const u16*)d_in[3];
  const u16* win = (const u16*)d_in[5];
  const u16* wsp = (const u16*)d_in[6];
  const u16* bsp = (const u16*)d_in[7];
  const u16* wout = (const u16*)d_in[8];
  char* ws = (char*)d_ws;
  k1_kernel<<<256, 640, 0, stream>>>(x, wqkv, wo, win, wout, wsp, ws);
  k2_kernel<<<256, 512, 0, stream>>>(wphi, ws);
  k3_kernel<<<256, 512, 0, stream>>>(wsp, bsp, wphi, ws);
  k4_kernel<<<256, 384, 0, stream>>>(x, ws, (u16*)d_out);
}
```

```cpp
#include <hip/hip_runtime.h>
#include <hip/hip_bf16.h>
#include <stdint.h>

typedef __attribute__((ext_vector_type(8))) short bf16x8;
typedef __attribute__((ext_vector_type(4))) short s16x4;
typedef __attribute__((ext_vector_type(4))) float f32x4;
typedef __attribute__((ext_vector_type(2))) float f32x2;
typedef __attribute__((ext_vector_type(4))) unsigned u32x4;
typedef __attribute__((ext_vector_type(2))) unsigned u32x2;
typedef __attribute__((ext_vector_type(2))) __bf16 bf2_t;
typedef unsigned short u16;
#define LDS_AS __attribute__((address_space(3)))

constexpr int S_ = 2048, D_ = 448, NQKV = 1344, NIN = 1792, FH = 896, KO = 1344;

__device__ __forceinline__ unsigned pk2(float a, float b) {
  f32x2 v = {a, b};
  return __builtin_bit_cast(unsigned, __builtin_convertvector(v, bf2_t));
}
__device__ __forceinline__ float lo_f(unsigned u) { return __uint_as_float(u << 16); }
__device__ __forceinline__ float hi_f(unsigned u) { return __uint_as_float(u & 0xffff0000u); }
__device__ __forceinline__ float rbf(float a) { return lo_f(pk2(a, 0.f)); }
__device__ __forceinline__ float bf2f(u16 v) { return __uint_as_float(((unsigned)v) << 16); }
__device__ __forceinline__ float dot2(unsigned a, unsigned b, float c) {
  return __builtin_amdgcn_fdot2_f32_bf16(__builtin_bit_cast(bf2_t, a), __builtin_bit_cast(bf2_t, b), c, false);
}

__device__ __forceinline__ float x16_max(float v) {
  auto r = __builtin_amdgcn_permlane16_swap(__float_as_uint(v), __float_as_uint(v), false, false);
  return fmaxf(__uint_as_float(r[0]), __uint_as_float(r[1]));
}
__device__ __forceinline__ float x32_max(float v) {
  auto r = __builtin_amdgcn_permlane32_swap(__float_as_uint(v), __float_as_uint(v), false, false);
  return fmaxf(__uint_as_float(r[0]), __uint_as_float(r[1]));
}
__device__ __forceinline__ float x16_min(float v) {
  auto r = __builtin_amdgcn_permlane16_swap(__float_as_uint(v), __float_as_uint(v), false, false);
  return fminf(__uint_as_float(r[0]), __uint_as_float(r[1]));
}
__device__ __forceinline__ float x32_min(float v) {
  auto r = __builtin_amdgcn_permlane32_swap(__float_as_uint(v), __float_as_uint(v), false, false);
  return fminf(__uint_as_float(r[0]), __uint_as_float(r[1]));
}
__device__ __forceinline__ float x16_add(float v) {
  auto r = __builtin_amdgcn_permlane16_swap(__float_as_uint(v), __float_as_uint(v), false, false);
  return __uint_as_float(r[0]) + __uint_as_float(r[1]);
}
__device__ __forceinline__ float x32_add(float v) {
  auto r = __builtin_amdgcn_permlane32_swap(__float_as_uint(v), __float_as_uint(v), false, false);
  return __uint_as_float(r[0]) + __uint_as_float(r[1]);
}
__device__ __forceinline__ float red8_add(float v) {
  v += __uint_as_float(__builtin_amdgcn_mov_dpp(__float_as_uint(v), 0xB1, 0xf, 0xf, true));
  v += __uint_as_float(__builtin_amdgcn_mov_dpp(__float_as_uint(v), 0x4E, 0xf, 0xf, true));
  v += __uint_as_float(__builtin_amdgcn_mov_dpp(__float_as_uint(v), 0x141, 0xf, 0xf, true));
  return v;
}
__device__ __forceinline__ f32x4 mfma16(bf16x8 a, bf16x8 b, f32x4 c) {
  return __builtin_amdgcn_mfma_f32_16x16x32_bf16(a, b, c, 0, 0, 0);
}
__device__ __forceinline__ unsigned gelu_pk(unsigned p) {
  const f32x2 u = {lo_f(p), hi_f(p)};
  const f32x2 c1 = {-2.3022081986f, -2.3022081986f};
  const f32x2 c2 = {-0.1029432397f, -0.1029432397f};
  f32x2 t = u * u;
  f32x2 a = u * (t * c2 + c1);
  f32x2 d = {1.f + __builtin_amdgcn_exp2f(a[0]), 1.f + __builtin_amdgcn_exp2f(a[1])};
  f32x2 r = {__builtin_amdgcn_rcpf(d[0]), __builtin_amdgcn_rcpf(d[1])};
  f32x2 o = u * r;
  return pk2(o[0], o[1]);
}
#define SCHED() __builtin_amdgcn_sched_barrier(0)
#define BAR() do { SCHED(); asm volatile("" ::: "memory"); __builtin_amdgcn_s_waitcnt(0xC07F); __builtin_amdgcn_s_barrier(); asm volatile("" ::: "memory"); SCHED(); } while (0)
__device__ __forceinline__ void glds16(const void* g, char* l) {
  __builtin_amdgcn_global_load_lds((const unsigned*)g, (LDS_AS unsigned*)l, 16, 0, 0);
}
__device__ __forceinline__ bf16x8 tr_frag(const char* p0, const char* p1) {
  s16x4 a = __builtin_amdgcn_ds_read_tr16_b64_v4i16((LDS_AS s16x4*)p0);
  s16x4 c = __builtin_amdgcn_ds_read_tr16_b64_v4i16((LDS_AS s16x4*)p1);
  return __builtin_shufflevector(a, c, 0, 1, 2, 3, 4, 5, 6, 7);
}

constexpr int YLD = 3136;
constexpr size_t WS_Y = 0;
constexpr size_t WS_Z2N = WS_Y + (size_t)S_ * YLD * 2 + 256;
constexpr size_t WS_OH = WS_Z2N + (size_t)S_ * 896 * 2;
constexpr size_t WS_WT = WS_OH + (size_t)S_ * KO * 2;
constexpr size_t WS_KVP = WS_WT + (size_t)D_ * KO * 2;
constexpr size_t WS_WSPF = WS_KVP + (size_t)8 * 16 * 56 * 32 * 4;
constexpr size_t WS_END = WS_WSPF + (size_t)S_ * S_ * 2;

#define STAMP(K, I) do { } while (0)
#define SGB(mask, n) __builtin_amdgcn_sched_group_barrier(mask, n, 0)

__device__ __forceinline__ void phi_map(bf16x8 qf, bf16x8 wp0, bf16x8 wp1, int g, unsigned (&pp)[2][2],
                                        unsigned (&pm)[2][2]) {
  f32x4 z = {0.f, 0.f, 0.f, 0.f};
  f32x4 p0 = mfma16(wp0, qf, z);
  f32x4 p1 = mfma16(wp1, qf, z);
  float v[8];
  {
    unsigned a = pk2(p0[0], p0[1]), b = pk2(p0[2], p0[3]), c = pk2(p1[0], p1[1]), d = pk2(p1[2], p1[3]);
    v[0] = lo_f(a); v[1] = hi_f(a); v[2] = lo_f(b); v[3] = hi_f(b);
    v[4] = lo_f(c); v[5] = hi_f(c); v[6] = lo_f(d); v[7] = hi_f(d);
  }
  const bool valid1 = (g != 3);
  if (!valid1) { v[4] = v[0]; v[5] = v[0]; v[6] = v[0]; v[7] = v[0]; }
  float mx = fmaxf(fmaxf(fmaxf(v[0], v[1]), fmaxf(v[2], v[3])), fmaxf(fmaxf(v[4], v[5]), fmaxf(v[6], v[7])));
  float mn = fminf(fminf(fminf(v[0], v[1]), fminf(v[2], v[3])), fminf(fminf(v[4], v[5]), fminf(v[6], v[7])));
  mx = x32_max(x16_max(mx));
  mn = x32_min(x16_min(mn));
  const float L2E = 1.4426950408889634f;
  const float mxl = mx * L2E, mnl = mn * L2E;
  float ep[8], em[8];
  float sp = 0.f, sm = 0.f;
#pragma unroll
  for (int j = 0; j < 8; ++j) {
    ep[j] = __builtin_amdgcn_exp2f(__builtin_fmaf(v[j], L2E, -mxl));
    em[j] = __builtin_amdgcn_exp2f(__builtin_fmaf(v[j], -L2E, mnl));
    if (j >= 4) { ep[j] = valid1 ? ep[j] : 0.f; em[j] = valid1 ? em[j] : 0.f; }
    sp += ep[j];
    sm += em[j];
  }
  sp = x32_add(x16_add(sp));
  sm = x32_add(x16_add(sm));
  const float rp = __builtin_amdgcn_rcpf(sp), rm = __builtin_amdgcn_rcpf(sm);
  pp[0][0] = pk2(ep[0] * rp, ep[1] * rp); pp[0][1] = pk2(ep[2] * rp, ep[3] * rp);
  pp[1][0] = pk2(ep[4] * rp, ep[5] * rp); pp[1][1] = pk2(ep[6] * rp, ep[7] * rp);
  pm[0][0] = pk2(em[0] * rm, em[1] * rm); pm[0][1] = pk2(em[2] * rm, em[3] * rm);
  pm[1][0] = pk2(em[4] * rm, em[5] * rm); pm[1][1] = pk2(em[6] * rm, em[7] * rm);
}

constexpr int K1_XB = 128 * 896;
constexpr int K1_WST = 32 * 448;
constexpr int K1_LDS = K1_XB + 3 * K1_WST;

__global__ __launch_bounds__(640) void k1_kernel(const u16* __restrict__ x, const u16* __restrict__ wqkv,
                                                  const u16* __restrict__ wo, const u16* __restrict__ win,
                                                  const u16* __restrict__ wout, const u16* __restrict__ wsp,
                                                  char* __restrict__ ws) {
  __shared__ __attribute__((aligned(16))) char sm[K1_LDS];
  const int tid = threadIdx.x, lane = tid & 63, wave = __builtin_amdgcn_readfirstlane(tid >> 6);
  const int b = blockIdx.x;
  STAMP(0, 0);
  if (b >= 224) {
    const int kb = b - 224;
    const int k0 = kb * 64;
    if (kb < 21) {
    u16* T = (u16*)sm;
    constexpr int TS = 456;
    u32x4 v[6];
#pragma unroll
    for (int j = 0; j < 6; ++j) {
      int i = tid + 640 * j;
      if (i >= 64 * 56) i = tid;
      const int r = i / 56, c = i % 56;
      const int k = k0 + r;
      const u16* src = (k < 448) ? (wo + (size_t)k * 448) : (wout + (size_t)(k - 448) * 448);
      v[j] = *(const u32x4*)(src + c * 8);
    }
#pragma unroll
    for (int j = 0; j < 6; ++j) {
      const int i = tid + 640 * j;
      if (i < 64 * 56) *(u32x4*)(T + (i / 56) * TS + (i % 56) * 8) = v[j];
    }
    __syncthreads();
    u16* wT = (u16*)(ws + WS_WT);
    for (int i = tid; i < 448 * 8; i += 640) {
      const int n = i >> 3, c = i & 7;
      u16 t[8];
#pragma unroll
      for (int j = 0; j < 8; ++j) t[j] = T[(c * 8 + j) * TS + n];
      u32x4 o;
#pragma unroll
      for (int j = 0; j < 4; ++j) o[j] = (unsigned)t[2 * j] | ((unsigned)t[2 * j + 1] << 16);
      *(u32x4*)(wT + ((size_t)((n >> 4) * 42 + (k0 >> 5) + (c >> 2)) * 64 + (c & 3) * 16 + (n & 15)) * 8) = o;
    }
    }
    STAMP(0, 1);
    {
      u16* wf = (u16*)(ws + WS_WSPF);
      const int r = lane >> 3, cc = lane & 7;
#pragma unroll 1
      for (int u0 = wave; u0 < 256; u0 += 80) {
        u32x4 v[8];
#pragma unroll
        for (int j = 0; j < 8; ++j) {
          int u = u0 + 10 * j;
          if (u >= 256) u = u0;
          v[j] = *(const u32x4*)(wsp + (size_t)(kb * 64 + (u & 7) * 8 + r) * S_ + ((u >> 3) * 8 + cc) * 8);
        }
#pragma unroll
        for (int j = 0; j < 8; ++j) {
          const int u = u0 + 10 * j;
          if (u < 256) {
            const int row = kb * 64 + (u & 7) * 8 + r, c = (u >> 3) * 8 + cc;
            *(u32x4*)(wf + ((size_t)((row >> 4) * 64 + (c >> 2)) * 64 + (c & 3) * 16 + (row & 15)) * 8) = v[j];
          }
        }
      }
    }
    STAMP(0, 31);
    return;
  }

  const int xcd = b & 7, idx = b >> 3;
  const int cb = (xcd & 1) * 7 + idx % 7;
  const int rt = (xcd >> 1) * 4 + idx / 7;
  const int row0 = rt * 128;
  char* wring = sm + K1_XB;
  const int g = lane >> 4, r16 = lane & 15;
  const int wm = (wave >> 1) & 3, wn = wave & 1;
  constexpr int RST = 464;

  if (wave >= 8) {
    const int lw = wave - 8;
    const u16* W = (cb < 6) ? (wqkv + 224 * cb) : (win + 224 * (cb - 6));
    const int ldw = (cb < 6) ? NQKV : NIN;
    const u16* src[7];
#pragma unroll
    for (int j = 0; j < 7; ++j) {
      const int o = (lw + 2 * j) * 1024 + lane * 16;
      const int k = o / 448, cp = (o % 448) >> 4;
      const int G = (cp >> 1) ^ ((k >> 3) & 1);
      const int c = 2 * G + (cp & 1);
      src[j] = W + (size_t)k * ldw + 8 * c;
    }
    const size_t wstep = (size_t)32 * ldw;
    char* dst0 = wring + lw * 1024;
#pragma unroll
    for (int s = 0; s < 3; ++s)
#pragma unroll
      for (int j = 0; j < 7; ++j) glds16(src[j] + s * wstep, dst0 + s * K1_WST + j * 2048);
    asm volatile("s_waitcnt vmcnt(14)" ::: "memory");
    __builtin_amdgcn_s_barrier();
    int nb = 0;
    for (int t = 0; t < 14; ++t) {
      if (t + 2 < 14) asm volatile("s_waitcnt vmcnt(7)" ::: "memory");
      else asm volatile("s_waitcnt vmcnt(0)" ::: "memory");
      __builtin_amdgcn_s_barrier();
      if (t + 3 < 14) {
        char* dst = dst0 + nb * K1_WST;
#pragma unroll
        for (int j = 0; j < 7; ++j) glds16(src[j] + (t + 3) * wstep, dst + j * 2048);
        nb = (nb == 2) ? 0 : nb + 1;
      }
    }
    __builtin_amdgcn_s_barrier();
  } else {
    u32x4 xv[2][7];
    const int rr = lane >> 3, cc = lane & 7;
#pragma unroll
    for (int p = 0; p < 2; ++p) {
      const u16* xp = x + (size_t)(row0 + 16 * wave + 8 * p + rr) * D_ + cc * 8;
#pragma unroll
      for (int i = 0; i < 7; ++i) xv[p][i] = *(const u32x4*)(xp + 64 * i);
    }
#pragma unroll
    for (int p = 0; p < 2; ++p) {
      float s0 = 0.f, s1 = 0.f;
#pragma unroll
      for (int i = 0; i < 7; ++i) {
        s0 = dot2(xv[p][i][0], 0x3F803F80u, s0); s1 = dot2(xv[p][i][1], 0x3F803F80u, s1);
        s0 = dot2(xv[p][i][2], 0x3F803F80u, s0); s1 = dot2(xv[p][i][3], 0x3F803F80u, s1);
      }
      const float mean = red8_add(s0 + s1) * (1.f / 448.f);
      const float mb = rbf(mean);
      float q0 = 0.f, q1 = 0.f;
#pragma unroll
      for (int i = 0; i < 7; ++i)
#pragma unroll
        for (int j = 0; j < 4; ++j) {
          const unsigned d = pk2(lo_f(xv[p][i][j]) - mb, hi_f(xv[p][i][j]) - mb);
          xv[p][i][j] = d;
          if (j & 1) q1 = dot2(d, d, q1); else q0 = dot2(d, d, q0);
        }
      const float q = red8_add(q0 + q1);
      const float dm = mean - mb;
      const float var = fmaxf(q * (1.f / 448.f) - dm * dm, 0.f);
      const float sd = rbf(sqrtf(rbf(rbf(var) + 1e-5f)));
      const float inv = 1.f / sd;
      const int row = 16 * wave + 8 * p + rr;
      char* dst = sm + row * 896 + ((cc ^ ((row >> 1) & 7)) << 4);
#pragma unroll
      for (int i = 0; i < 7; ++i) {
        u32x4 o;
#pragma unroll
        for (int j = 0; j < 4; ++j) o[j] = pk2(lo_f(xv[p][i][j]) * inv, hi_f(xv[p][i][j]) * inv);
        *(u32x4*)(dst + 128 * i) = o;
      }
    }
    STAMP(0, 1);
    f32x4 acc[7][2];
#pragma unroll
    for (int nt = 0; nt < 7; ++nt) { acc[nt][0] = f32x4{0.f, 0.f, 0.f, 0.f}; acc[nt][1] = f32x4{0.f, 0.f, 0.f, 0.f}; }
    const int sw = (r16 >> 1) & 7;
    const char* xbase = sm + (32 * wm + r16) * 896;
    const int xoE = ((g ^ sw) << 4), xoO = (((4 | g) ^ sw) << 4);
    const int wrow = (8 * g + ((lane >> 2) & 3)) * 448 + (lane & 3) * 8 + 7 * wn * 32;
    const int wE = wrow + ((wn & 1) ? -32 * (g & 1) : 32 * (g & 1));
    const int wO = wrow + ((wn & 1) ? 32 * (g & 1) : -32 * (g & 1));
    bf16x8 xa[2], wa[7], xb[2], wb[7];
#define K1_READ(XF, WF, T, BUF)                                                              \
  do {                                                                                       \
    const char* xp_ = xbase + ((T) >> 1) * 128 + (((T)&1) ? xoO : xoE);                      \
    const char* wb_ = wring + (BUF)*K1_WST;                                                  \
    XF[0] = *(const bf16x8*)(xp_);                                                           \
    XF[1] = *(const bf16x8*)(xp_ + 16 * 896);                                                \
    _Pragma("unroll") for (int nt = 0; nt < 7; ++nt) {                                       \
      const char* p_ = wb_ + ((nt & 1) ? wO : wE) + nt * 32;                                 \
      WF[nt] = tr_frag(p_, p_ + 4 * 448);                                                    \
    }                                                                                        \
  } while (0)
#define K1_MMA(XF, WF)                                                                       \
  do {                                                                                       \
    _Pragma("unroll") for (int nt = 0; nt < 7; ++nt) acc[nt][0] = mfma16(WF[nt], XF[0], acc[nt][0]); \
    _Pragma("unroll") for (int nt = 0; nt < 7; ++nt) acc[nt][1] = mfma16(WF[nt], XF[1], acc[nt][1]); \
  } while (0)
#define K1_MIX()                                                                             \
  do {                                                                                       \
    SGB(0x100, 2);                                                                           \
    _Pragma("unroll") for (int i_ = 0; i_ < 14; ++i_) { SGB(0x008, 1); SGB(0x100, 1); }      \
  } while (0)
    BAR();
    STAMP(0, 2);
    K1_READ(xa, wa, 0, 0);
    int rb = 1;
#pragma unroll 1
    for (int t = 0; t < 12; t += 2) {
      BAR();
      STAMP(0, 3 + t);
      K1_READ(xb, wb, t + 1, rb);
      rb = (rb == 2) ? 0 : rb + 1;
      K1_MMA(xa, wa);
      K1_MIX();
      BAR();
      STAMP(0, 4 + t);
      K1_READ(xa, wa, t + 2, rb);
      rb = (rb == 2) ? 0 : rb + 1;
      K1_MMA(xb, wb);
      K1_MIX();
    }
    BAR();
    K1_READ(xb, wb, 13, 1);
    K1_MMA(xa, wa);
    K1_MIX();
    BAR();
    K1_MMA(xb, wb);
    BAR();
    STAMP(0, 17);
#pragma unroll
    for (int mt = 0; mt < 2; ++mt) {
      char* dst = sm + (32 * wm + 16 * mt + r16) * RST + (112 * wn + 4 * g) * 2;
#pragma unroll
      for (int nt = 0; nt < 7; ++nt)
        *(u32x2*)(dst + 32 * nt) = u32x2{pk2(acc[nt][mt][0], acc[nt][mt][1]), pk2(acc[nt][mt][2], acc[nt][mt][3])};
    }
  }
  __syncthreads();
  u16* dstb = (u16*)(ws + WS_Y) + (size_t)row0 * YLD + 224 * cb;
#pragma unroll
  for (int i = 0; i < 6; ++i) {
    const int id = tid + 640 * i;
    if (id < 128 * 28) {
      const int s = id / 28, c = id % 28;
      *(u32x4*)(dstb + (size_t)s * YLD + c * 8) = *(const u32x4*)(sm + s * RST + c * 16);
    }
  }
  STAMP(0, 31);
}

__device__ __forceinline__ float red64_add(float v) {
  v = red8_add(v);
  v += __uint_as_float(__builtin_amdgcn_mov_dpp(__float_as_uint(v), 0x140, 0xf, 0xf, true));
  return x32_add(x16_add(v));
}

constexpr int K2_PW = 64 * 80 + 32 * 64;
__global__ __launch_bounds__(512) void k2_kernel(const u16* __restrict__ wphi, char* __restrict__ ws) {
  __shared__ __attribute__((aligned(16))) char sm[8 * 56 * 32 * 4 + 2048];
  const int tid = threadIdx.x, lane = tid & 63, wave = __builtin_amdgcn_readfirstlane(tid >> 6);
  const int b = blockIdx.x;
  const u16* Y = (const u16*)(ws + WS_Y);
  STAMP(1, 0);
  if (b < 128) {
    const int h = b & 15, rg = b >> 4;
    const int g = lane >> 4, r16 = lane & 15;
    const int rowb = rg * 256 + wave * 32;
    u32x2 kv_[2][2];
#pragma unroll
    for (int st = 0; st < 2; ++st) {
      const u16* kp = Y + (size_t)(rowb + st * 16 + r16) * YLD + 448 + h * 28 + 8 * g;
      kv_[st][0] = *(const u32x2*)(kp);
      kv_[st][1] = *(const u32x2*)(kp + 4);
    }
    u32x2 vv[4];
#pragma unroll
    for (int i = 0; i < 4; ++i) {
      const int row = (lane >> 3) + 8 * i, pc = lane & 7;
      vv[i] = *(const u32x2*)(Y + (size_t)(rowb + row) * YLD + 896 + h * 28 + 4 * (pc < 7 ? pc : 0));
      if (pc == 7) vv[i] = u32x2{0x00003F80u, 0u};
    }
    u32x4 wraw = {0u, 0u, 0u, 0u};
    if (tid < 98) wraw = *(const u32x4*)(wphi + (size_t)h * 784 + tid * 8);
    char* WP = sm + 8 * 56 * 32 * 4;
    if (tid < 98) *(u32x4*)(WP + tid * 16) = wraw;
    char* PK = sm + wave * K2_PW;
    char* VS = PK + 64 * 80;
    __syncthreads();
    STAMP(1, 1);
    const char* wp = WP + (8 * g + ((lane >> 2) & 3)) * 56 + (lane & 3) * 8;
    bf16x8 wp0 = tr_frag(wp, wp + 4 * 56);
    bf16x8 wp1 = tr_frag(wp + 32, wp + 4 * 56 + 32);
    if (g == 3) {
#pragma unroll
      for (int j = 4; j < 8; ++j) { wp0[j] = 0; wp1[j] = 0; }
    }
    f32x4 kacc[4][2];
#pragma unroll
    for (int a = 0; a < 4; ++a) { kacc[a][0] = f32x4{0.f, 0.f, 0.f, 0.f}; kacc[a][1] = f32x4{0.f, 0.f, 0.f, 0.f}; }
#pragma unroll
    for (int pr = 0; pr < 1; ++pr) {
#pragma unroll
      for (int i = 0; i < 4; ++i) {
        const int row = (lane >> 3) + 8 * i;
        const int slot = 2 * (row & 15) + (row >> 4);
        *(u32x2*)(VS + slot * 64 + (lane & 7) * 8) = vv[4 * pr + i];
      }
      unsigned pp[2][2][2], pm[2][2][2];
#pragma unroll
      for (int mt = 0; mt < 2; ++mt) {
        u32x4 q4 = {kv_[2 * pr + mt][0][0], kv_[2 * pr + mt][0][1], kv_[2 * pr + mt][1][0], kv_[2 * pr + mt][1][1]};
        if (g == 3) { q4[2] = 0u; q4[3] = 0u; }
        phi_map(__builtin_bit_cast(bf16x8, q4), wp0, wp1, g, pp[mt], pm[mt]);
      }
#pragma unroll
      for (int ft = 0; ft < 2; ++ft) {
        if (ft == 0 || g != 3) {
          char* d0 = PK + (16 * ft + 4 * g) * 80 + r16 * 4;
#pragma unroll
          for (int j = 0; j < 2; ++j) {
            *(unsigned*)(d0 + (2 * j) * 80) = (pp[0][ft][j] & 0xffffu) | (pp[1][ft][j] << 16);
            *(unsigned*)(d0 + (2 * j + 1) * 80) = (pp[0][ft][j] >> 16) | (pp[1][ft][j] & 0xffff0000u);
            *(unsigned*)(d0 + (28 + 2 * j) * 80) = (pm[0][ft][j] & 0xffffu) | (pm[1][ft][j] << 16);
            *(unsigned*)(d0 + (28 + 2 * j + 1) * 80) = (pm[0][ft][j] >> 16) | (pm[1][ft][j] & 0xffff0000u);
          }
        }
      }
      __builtin_amdgcn_s_waitcnt(0xC07F);
      __builtin_amdgcn_wave_barrier();
      bf16x8 bfr[2];
      {
        const char* vp = VS + (8 * g + ((lane >> 2) & 3)) * 64 + (lane & 3) * 8;
        bfr[0] = tr_frag(vp, vp + 4 * 64);
        bfr[1] = tr_frag(vp + 32, vp + 4 * 64 + 32);
      }
#pragma unroll
      for (int a = 0; a < 4; ++a) {
        bf16x8 af = *(const bf16x8*)(PK + (16 * a + r16) * 80 + g * 16);
        kacc[a][0] = mfma16(af, bfr[0], kacc[a][0]);
        kacc[a][1] = mfma16(af, bfr[1], kacc[a][1]);
      }
      __builtin_amdgcn_s_waitcnt(0xC07F);
      __builtin_amdgcn_wave_barrier();
    }
    STAMP(1, 2);
    __syncthreads();
    float* red = (float*)sm + wave * (56 * 32);
#pragma unroll
    for (int a = 0; a < 4; ++a)
#pragma unroll
      for (int c = 0; c < 2; ++c)
#pragma unroll
        for (int j = 0; j < 4; ++j) {
          const int f = 16 * a + 4 * g + j;
          if (f < 56) red[f * 32 + 16 * c + r16] = kacc[a][c][j];
        }
    __syncthreads();
    if (tid < 448) {
      const f32x4* rp = (const f32x4*)sm + tid;
      f32x4 s = (rp[0] + rp[448]) + (rp[2 * 448] + rp[3 * 448]);
      s += (rp[4 * 448] + rp[5 * 448]) + (rp[6 * 448] + rp[7 * 448]);
      *((f32x4*)(ws + WS_KVP) + (size_t)(rg * 16 + h) * 448 + tid) = s;
    }
  } else {
    const int row0 = (b - 128) * 16 + wave * 2;
    u16* z2n = (u16*)(ws + WS_Z2N);
    const int le = (lane < 56) ? lane : 0;
    u32x4 v[2][2];
#pragma unroll
    for (int r = 0; r < 2; ++r)
#pragma unroll
      for (int c = 0; c < 2; ++c) v[r][c] = *(const u32x4*)(Y + (size_t)(row0 + r) * YLD + 2240 + (le + 56 * c) * 8);
#pragma unroll
    for (int r = 0; r < 2; ++r) {
      float s = 0.f;
#pragma unroll
      for (int c = 0; c < 2; ++c)
#pragma unroll
        for (int j = 0; j < 4; ++j) {
          v[r][c][j] = gelu_pk(v[r][c][j]);
          s = dot2(v[r][c][j], 0x3F803F80u, s);
        }
      if (lane >= 56) s = 0.f;
      const float mean = red64_add(s) * (1.f / 896.f);
      const float mb = rbf(mean);
      float q = 0.f;
#pragma unroll
      for (int c = 0; c < 2; ++c)
#pragma unroll
        for (int j = 0; j < 4; ++j) {
          const float a = lo_f(v[r][c][j]) - mean, e = hi_f(v[r][c][j]) - mean;
          q += a * a + e * e;
          v[r][c][j] = pk2(lo_f(v[r][c][j]) - mb, hi_f(v[r][c][j]) - mb);
        }
      if (lane >= 56) q = 0.f;
      const float var = red64_add(q) * (1.f / 896.f);
      const float sd = rbf(sqrtf(rbf(rbf(var) + 1e-5f)));
      const float inv = 1.f / sd;
      if (lane < 56) {
#pragma unroll
        for (int c = 0; c < 2; ++c) {
          u32x4 o;
#pragma unroll
          for (int j = 0; j < 4; ++j) o[j] = pk2(lo_f(v[r][c][j]) * inv, hi_f(v[r][c][j]) * inv);
          *(u32x4*)(sm + (wave * 2 + r) * 1808 + (lane + 56 * c) * 16) = o;
        }
      }
    }
    __syncthreads();
    {
      const int rb0 = (b - 128) * 16;
      const int kb = rb0 >> 5, half = (rb0 >> 4) & 1;
      const int g = lane >> 4;
#pragma unroll
      for (int i = 0; i < 4; ++i) {
        const int u = wave + 8 * i;
        if (u < 28) {
          const int T = 2 * u + (g >> 1), gp = g & 1;
          const char* p = sm + (8 * gp + ((lane >> 2) & 3)) * 1808 + T * 32 + (lane & 3) * 8;
          bf16x8 fr = tr_frag(p, p + 4 * 1808);
          const int pn = T / 7, n = T % 7;
          *(bf16x8*)(z2n + ((size_t)((pn * 64 + kb) * 7 + n) * 64 + (2 * half + gp) * 16 + (lane & 15)) * 8) = fr;
        }
      }
    }
  }
  STAMP(1, 31);
}

constexpr int K3_RS = 464;
__global__ __launch_bounds__(512) void k3_kernel(const u16* __restrict__ wsp, const u16* __restrict__ bsp,
                                                  const u16* __restrict__ wphi, char* __restrict__ ws) {
  __shared__ __attribute__((aligned(16))) char sm[4 * 64 * K3_RS];
  const int tid = threadIdx.x, lane = tid & 63, wave = __builtin_amdgcn_readfirstlane(tid >> 6);
  const int b = blockIdx.x;
  const int xcd = b & 7, idx = b >> 3;
  const int mt_ = (xcd >> 1) * 8 + (idx & 7);
  const int nt_ = (xcd & 1) * 4 + (idx >> 3);
  const int t0 = mt_ * 64, f0 = nt_ * 112;
  const u16* z2n = (const u16*)(ws + WS_Z2N);
  const u16* Y = (const u16*)(ws + WS_Y);
  u16* oh = (u16*)(ws + WS_OH);
  const int g = lane >> 4, r16 = lane & 15;
  STAMP(2, 0);
  const int h = b & 15, srow = (b >> 4) * 128 + wave * 16 + r16;
  f32x4 part[8];
  {
    const int te = (tid < 448) ? tid : 0;
    const f32x4* kvp = (const f32x4*)(ws + WS_KVP) + (size_t)h * 448 + te;
#pragma unroll
    for (int p = 0; p < 8; ++p) part[p] = kvp[(size_t)p * 16 * 448];
  }
  u32x4 wraw = {0u, 0u, 0u, 0u};
  if (tid < 98) wraw = *(const u32x4*)(wphi + (size_t)h * 784 + tid * 8);
  const u16* qp = Y + (size_t)srow * YLD + h * 28 + 8 * g;
  const u32x2 q0 = *(const u32x2*)(qp), q1 = *(const u32x2*)(qp + 4);
  SCHED();
  const u16* ap = (const u16*)(ws + WS_WSPF) + ((size_t)(mt_ * 4 * 64 + wave * 8) * 64 + lane) * 8;
  const u16* bp = z2n + ((size_t)(nt_ * 64 + wave * 8) * 7 * 64 + lane) * 8;
  bf16x8 a0[4], b0[7], a1[4], b1[7], a2[4], b2[7];
#define K3_LOAD(AF, BF, KS)                                                                     \
  do {                                                                                          \
    _Pragma("unroll") for (int m = 0; m < 4; ++m) AF[m] = *(const bf16x8*)(ap + (m * 64 + (KS)) * 512); \
    _Pragma("unroll") for (int n = 0; n < 7; ++n) BF[n] = *(const bf16x8*)(bp + ((KS)*7 + n) * 512); \
  } while (0)
  K3_LOAD(a0, b0, 0);
  K3_LOAD(a1, b1, 1);
  SCHED();
  {
    char* KT = sm;
    char* WP = KT + 32 * 144;
    if (tid < 98) *(u32x4*)(WP + tid * 16) = wraw;
    if (tid < 448) {
      const f32x4 s = ((part[0] + part[1]) + (part[2] + part[3])) + ((part[4] + part[5]) + (part[6] + part[7]));
      const int fp = tid >> 3, d0 = 4 * (tid & 7);
      const int mf = (fp >= 28) ? 1 : 0, f = fp - 28 * mf;
      const int pos = mf * 32 + ((f & 15) >> 2) * 8 + (f & 3) + 4 * (f >> 4);
      const unsigned a = pk2(s[0], s[1]), c = pk2(s[2], s[3]);
      char* d = KT + d0 * 144 + pos * 2;
      *(u16*)(d) = (u16)a; *(u16*)(d + 144) = (u16)(a >> 16);
      *(u16*)(d + 288) = (u16)c; *(u16*)(d + 432) = (u16)(c >> 16);
    } else {
      const int i = tid - 448, d = i >> 1, mf = i & 1;
      *(u32x2*)(KT + d * 144 + (mf * 32 + 28) * 2) = u32x2{0u, 0u};
    }
    STAMP(2, 1);
    BAR();
    bf16x8 af[2][2];
#pragma unroll
    for (int dt = 0; dt < 2; ++dt)
#pragma unroll
      for (int mf = 0; mf < 2; ++mf) af[dt][mf] = *(const bf16x8*)(KT + (16 * dt + r16) * 144 + (32 * mf + 8 * g) * 2);
    const char* wp = WP + (8 * g + ((lane >> 2) & 3)) * 56 + (lane & 3) * 8;
    bf16x8 wp0 = tr_frag(wp, wp + 4 * 56);
    bf16x8 wp1 = tr_frag(wp + 32, wp + 4 * 56 + 32);
    if (g == 3) {
#pragma unroll
      for (int j = 4; j < 8; ++j) { wp0[j] = 0; wp1[j] = 0; }
    }
    u32x4 q4 = {q0[0], q0[1], q1[0], q1[1]};
    if (g == 3) { q4[2] = 0u; q4[3] = 0u; }
    unsigned pp[2][2], pm[2][2];
    phi_map(__builtin_bit_cast(bf16x8, q4), wp0, wp1, g, pp, pm);
    u32x4 c1 = {pp[0][0], pp[0][1], pp[1][0], pp[1][1]};
    u32x4 c2 = {pm[0][0], pm[0][1], pm[1][0], pm[1][1]};
    f32x4 o0 = {0.f, 0.f, 0.f, 0.f}, o1 = {0.f, 0.f, 0.f, 0.f};
    o0 = mfma16(af[0][0], __builtin_bit_cast(bf16x8, c1), o0);
    o1 = mfma16(af[1][0], __builtin_bit_cast(bf16x8, c1), o1);
    o0 = mfma16(af[0][1], __builtin_bit_cast(bf16x8, c2), o0);
    o1 = mfma16(af[1][1], __builtin_bit_cast(bf16x8, c2), o1);
    float den = __shfl(o1[0], 48 + r16, 64);
    den = rbf(rbf(den) + 1e-6f);
    const float inv = 1.f / den;
    {
      const int k = h * 28 + 4 * g;
      u16* dst = oh + ((size_t)((srow >> 4) * 42 + (k >> 5)) * 64 + ((k >> 3) & 3) * 16 + r16) * 8 + (k & 7);
      *(u32x2*)(dst) = u32x2{pk2(rbf(o0[0]) * inv, rbf(o0[1]) * inv), pk2(rbf(o0[2]) * inv, rbf(o0[3]) * inv)};
    }
    if (g != 3) {
      const int k = h * 28 + 16 + 4 * g;
      u16* dst = oh + ((size_t)((srow >> 4) * 42 + (k >> 5)) * 64 + ((k >> 3) & 3) * 16 + r16) * 8 + (k & 7);
      *(u32x2*)(dst) = u32x2{pk2(rbf(o1[0]) * inv, rbf(o1[1]) * inv), pk2(rbf(o1[2]) * inv, rbf(o1[3]) * inv)};
    }
  }
  STAMP(2, 2);
  SCHED();
  f32x4 acc[7][4];
#pragma unroll
  for (int n = 0; n < 7; ++n)
#pragma unroll
    for (int m = 0; m < 4; ++m) acc[n][m] = f32x4{0.f, 0.f, 0.f, 0.f};
#define K3_MMA(AF, BF)                                                                          \
  do {                                                                                          \
    _Pragma("unroll") for (int n = 0; n < 7; ++n)                                               \
      _Pragma("unroll") for (int m = 0; m < 4; ++m) acc[n][m] = mfma16(BF[n], AF[m], acc[n][m]); \
  } while (0)
#define K3_MIX()                                                                                \
  do {                                                                                          \
    _Pragma("unroll") for (int i_ = 0; i_ < 11; ++i_) { SGB(0x020, 1); SGB(0x008, 2); }         \
    SCHED();                                                                                    \
  } while (0)
  K3_LOAD(a2, b2, 2); K3_MMA(a0, b0); K3_MIX();
  K3_LOAD(a0, b0, 3); K3_MMA(a1, b1); K3_MIX();
  K3_LOAD(a1, b1, 4); K3_MMA(a2, b2); K3_MIX();
  STAMP(2, 3);
  K3_LOAD(a2, b2, 5); K3_MMA(a0, b0); K3_MIX();
  K3_LOAD(a0, b0, 6); K3_MMA(a1, b1); K3_MIX();
  K3_LOAD(a1, b1, 7); K3_MMA(a2, b2); K3_MIX();
  u32x4 zv[2];
  float bb[2];
#pragma unroll
  for (int i = 0; i < 2; ++i) {
    int id = tid + 512 * i;
    if (id >= 896) id = tid;
    const int t = id / 14, c = id % 14;
    zv[i] = *(const u32x4*)(Y + (size_t)(t0 + t) * YLD + 1344 + f0 + c * 8);
    bb[i] = bf2f(bsp[t0 + t]);
  }
  SCHED();
  K3_MMA(a0, b0);
  K3_MMA(a1, b1);
  STAMP(2, 4);
  char* rd = sm + (wave & 3) * (64 * K3_RS) + r16 * K3_RS + 16 * g;
  BAR();
  if (wave >= 4) {
#pragma unroll
    for (int n = 0; n < 7; ++n)
#pragma unroll
      for (int m = 0; m < 4; ++m) *(f32x4*)(rd + 16 * m * K3_RS + 64 * n) = acc[n][m];
  }
  BAR();
  if (wave < 4) {
#pragma unroll
    for (int n = 0; n < 7; ++n)
#pragma unroll
      for (int m = 0; m < 4; ++m) {
        f32x4* p = (f32x4*)(rd + 16 * m * K3_RS + 64 * n);
        *p = *p + acc[n][m];
      }
  }
  BAR();
  STAMP(2, 5);
#pragma unroll
  for (int i = 0; i < 2; ++i) {
    const int id = tid + 512 * i;
    if (id < 896) {
      const int t = id / 14, c = id % 14;
      const char* p = sm + t * K3_RS + c * 32;
      f32x4 v0 = (*(const f32x4*)(p) + *(const f32x4*)(p + 64 * K3_RS)) +
                 (*(const f32x4*)(p + 128 * K3_RS) + *(const f32x4*)(p + 192 * K3_RS));
      f32x4 v1 = (*(const f32x4*)(p + 16) + *(const f32x4*)(p + 64 * K3_RS + 16)) +
                 (*(const f32x4*)(p + 128 * K3_RS + 16) + *(const f32x4*)(p + 192 * K3_RS + 16));
      u32x4 z;
#pragma unroll
      for (int j = 0; j < 4; ++j) z[j] = gelu_pk(zv[i][j]);
      u32x4 o;
      o[0] = pk2(lo_f(z[0]) * rbf(rbf(v0[0]) + bb[i]), hi_f(z[0]) * rbf(rbf(v0[1]) + bb[i]));
      o[1] = pk2(lo_f(z[1]) * rbf(rbf(v0[2]) + bb[i]), hi_f(z[1]) * rbf(rbf(v0[3]) + bb[i]));
      o[2] = pk2(lo_f(z[2]) * rbf(rbf(v1[0]) + bb[i]), hi_f(z[2]) * rbf(rbf(v1[1]) + bb[i]));
      o[3] = pk2(lo_f(z[3]) * rbf(rbf(v1[2]) + bb[i]), hi_f(z[3]) * rbf(rbf(v1[3]) + bb[i]));
      const int k = 448 + f0 + c * 8, tt = t0 + t;
      *(u32x4*)(oh + ((size_t)((tt >> 4) * 42 + (k >> 5)) * 64 + ((k >> 3) & 3) * 16 + (tt & 15)) * 8) = o;
    }
  }
  STAMP(2, 31);
}

__global__ __launch_bounds__(384) void k4_kernel(const u16* __restrict__ x, const char* __restrict__ ws,
                                                  u16* __restrict__ out) {
  __shared__ __attribute__((aligned(16))) char sm[6 * 32 * K3_RS];
  const int tid = threadIdx.x, lane = tid & 63, wave = __builtin_amdgcn_readfirstlane(tid >> 6);
  const int b = blockIdx.x;
  const int xcd = b & 7, idx = b >> 3;
  const int mt_ = xcd * 8 + (idx & 7), nt_ = idx >> 3;
  const int t0 = mt_ * 32, n0 = nt_ * 112;
  const int g = lane >> 4, r16 = lane & 15;
  STAMP(3, 0);
  const u16* ap = (const u16*)(ws + WS_OH) + ((size_t)(mt_ * 2 * 42 + wave * 7) * 64 + lane) * 8;
  const u16* bp = (const u16*)(ws + WS_WT) + ((size_t)(nt_ * 7 * 42 + wave * 7) * 64 + lane) * 8;
  bf16x8 a0[2], b0[7], a1[2], b1[7], a2[2], b2[7], a3[2], b3[7];
#define K4_LOAD(AF, BF, KS)                                                                     \
  do {                                                                                          \
    _Pragma("unroll") for (int m = 0; m < 2; ++m) AF[m] = *(const bf16x8*)(ap + (m * 42 + (KS)) * 512); \
    _Pragma("unroll") for (int n = 0; n < 7; ++n) BF[n] = *(const bf16x8*)(bp + (n * 42 + (KS)) * 512); \
  } while (0)
  K4_LOAD(a0, b0, 0);
  K4_LOAD(a1, b1, 1);
  K4_LOAD(a2, b2, 2);
  K4_LOAD(a3, b3, 3);
  u32x4 xv[2];
#pragma unroll
  for (int i = 0; i < 2; ++i) {
    int id = tid + 384 * i;
    if (id >= 448) id = tid;
    xv[i] = *(const u32x4*)(x + (size_t)(t0 + id / 14) * D_ + n0 + (id % 14) * 8);
  }
  SCHED();
  f32x4 acc[7][2];
#pragma unroll
  for (int n = 0; n < 7; ++n) { acc[n][0] = f32x4{0.f, 0.f, 0.f, 0.f}; acc[n][1] = f32x4{0.f, 0.f, 0.f, 0.f}; }
#define K4_MMA(AF, BF)                                                                          \
  do {                                                                                          \
    _Pragma("unroll") for (int n = 0; n < 7; ++n) {                                             \
      acc[n][0] = mfma16(BF[n], AF[0], acc[n][0]);                                              \
      acc[n][1] = mfma16(BF[n], AF[1], acc[n][1]);                                              \
    }                                                                                           \
    SCHED();                                                                                    \
  } while (0)
  K4_MMA(a0, b0); K4_LOAD(a0, b0, 4); SCHED();
  K4_MMA(a1, b1); K4_LOAD(a1, b1, 5); SCHED();
  K4_MMA(a2, b2); K4_LOAD(a2, b2, 6); SCHED();
  K4_MMA(a3, b3);
  K4_MMA(a0, b0);
  K4_MMA(a1, b1);
  K4_MMA(a2, b2);
  STAMP(3, 1);
  char* rd = sm + wave * (32 * K3_RS) + r16 * K3_RS + 16 * g;
#pragma unroll
  for (int n = 0; n < 7; ++n)
#pragma unroll
    for (int m = 0; m < 2; ++m) *(f32x4*)(rd + 16 * m * K3_RS + 64 * n) = acc[n][m];
  __syncthreads();
#pragma unroll
  for (int i = 0; i < 2; ++i) {
    const int id = tid + 384 * i;
    if (id < 448) {
      const int et = id / 14, ec = id % 14;
      const char* p = sm + et * K3_RS + ec * 32;
      f32x4 v0 = {0.f, 0.f, 0.f, 0.f}, v1 = {0.f, 0.f, 0.f, 0.f};
#pragma unroll
      for (int w = 0; w < 6; ++w) {
        v0 += *(const f32x4*)(p + w * 32 * K3_RS);
        v1 += *(const f32x4*)(p + w * 32 * K3_RS + 16);
      }
      u32x4 o;
      o[0] = pk2(lo_f(xv[i][0]) + v0[0], hi_f(xv[i][0]) + v0[1]);
      o[1] = pk2(lo_f(xv[i][1]) + v0[2], hi_f(xv[i][1]) + v0[3]);
      o[2] = pk2(lo_f(xv[i][2]) + v1[0], hi_f(xv[i][2]) + v1[1]);
      o[3] = pk2(lo_f(xv[i][3]) + v1[2], hi_f(xv[i][3]) + v1[3]);
      *(u32x4*)(out + (size_t)(t0 + et) * D_ + n0 + ec * 8) = o;
    }
  }
  STAMP(3, 31);
}

extern "C" void kernel_launch(void* const* d_in, const int* in_sizes, int n_in, void* d_out, int out_size,
                              void* d_ws, size_t ws_size, hipStream_t stream) {
  const u16* x = (const u16*)d_in[0];
  const u16* wqkv = (const u16*)d_in[1];
  const u16* wphi = (const u16*)d_in[2];
  const u16* wo = (const u16*)d_in[3];
  const u16* win = (const u16*)d_in[5];
  const u16* wsp = (const u16*)d_in[6];
  const u16* bsp = (const u16*)d_in[7];
  const u16* wout = (const u16*)d_in[8];
  char* ws = (char*)d_ws;
  k1_kernel<<<256, 640, 0, stream>>>(x, wqkv, wo, win, wout, wsp, ws);
  k2_kernel<<<256, 512, 0, stream>>>(wphi, ws);
  k3_kernel<<<256, 512, 0, stream>>>(wsp, bsp, wphi, ws);
  k4_kernel<<<256, 384, 0, stream>>>(x, ws, (u16*)d_out);
}
```

```cpp
#include <hip/hip_runtime.h>
#include <hip/hip_bf16.h>
#include <stdint.h>

typedef __attribute__((ext_vector_type(8))) short bf16x8;
typedef __attribute__((ext_vector_type(4))) short s16x4;
typedef __attribute__((ext_vector_type(4))) float f32x4;
typedef __attribute__((ext_vector_type(2))) float f32x2;
typedef __attribute__((ext_vector_type(4))) unsigned u32x4;
typedef __attribute__((ext_vector_type(2))) unsigned u32x2;
typedef __attribute__((ext_vector_type(2))) __bf16 bf2_t;
typedef unsigned short u16;
#define LDS_AS __attribute__((address_space(3)))

constexpr int S_ = 2048, D_ = 448, NQKV = 1344, NIN = 1792, FH = 896, KO = 1344;

__device__ __forceinline__ unsigned pk2(float a, float b) {
  f32x2 v = {a, b};
  return __builtin_bit_cast(unsigned, __builtin_convertvector(v, bf2_t));
}
__device__ __forceinline__ float lo_f(unsigned u) { return __uint_as_float(u << 16); }
__device__ __forceinline__ float hi_f(unsigned u) { return __uint_as_float(u & 0xffff0000u); }
__device__ __forceinline__ float rbf(float a) { return lo_f(pk2(a, 0.f)); }
__device__ __forceinline__ float bf2f(u16 v) { return __uint_as_float(((unsigned)v) << 16); }
__device__ __forceinline__ float dot2(unsigned a, unsigned b, float c) {
  return __builtin_amdgcn_fdot2_f32_bf16(__builtin_bit_cast(bf2_t, a), __builtin_bit_cast(bf2_t, b), c, false);
}

__device__ __forceinline__ float x16_max(float v) {
  auto r = __builtin_amdgcn_permlane16_swap(__float_as_uint(v), __float_as_uint(v), false, false);
  return fmaxf(__uint_as_float(r[0]), __uint_as_float(r[1]));
}
__device__ __forceinline__ float x32_max(float v) {
  auto r = __builtin_amdgcn_permlane32_swap(__float_as_uint(v), __float_as_uint(v), false, false);
  return fmaxf(__uint_as_float(r[0]), __uint_as_float(r[1]));
}
__device__ __forceinline__ float x16_min(float v) {
  auto r = __builtin_amdgcn_permlane16_swap(__float_as_uint(v), __float_as_uint(v), false, false);
  return fminf(__uint_as_float(r[0]), __uint_as_float(r[1]));
}
__device__ __forceinline__ float x32_min(float v) {
  auto r = __builtin_amdgcn_permlane32_swap(__float_as_uint(v), __float_as_uint(v), false, false);
  return fminf(__uint_as_float(r[0]), __uint_as_float(r[1]));
}
__device__ __forceinline__ float x16_add(float v) {
  auto r = __builtin_amdgcn_permlane16_swap(__float_as_uint(v), __float_as_uint(v), false, false);
  return __uint_as_float(r[0]) + __uint_as_float(r[1]);
}
__device__ __forceinline__ float x32_add(float v) {
  auto r = __builtin_amdgcn_permlane32_swap(__float_as_uint(v), __float_as_uint(v), false, false);
  return __uint_as_float(r[0]) + __uint_as_float(r[1]);
}
__device__ __forceinline__ float red8_add(float v) {
  v += __uint_as_float(__builtin_amdgcn_mov_dpp(__float_as_uint(v), 0xB1, 0xf, 0xf, true));
  v += __uint_as_float(__builtin_amdgcn_mov_dpp(__float_as_uint(v), 0x4E, 0xf, 0xf, true));
  v += __uint_as_float(__builtin_amdgcn_mov_dpp(__float_as_uint(v), 0x141, 0xf, 0xf, true));
  return v;
}
__device__ __forceinline__ f32x4 mfma16(bf16x8 a, bf16x8 b, f32x4 c) {
  return __builtin_amdgcn_mfma_f32_16x16x32_bf16(a, b, c, 0, 0, 0);
}
__device__ __forceinline__ unsigned gelu_pk(unsigned p) {
  const f32x2 u = {lo_f(p), hi_f(p)};
  const f32x2 c1 = {-2.3022081986f, -2.3022081986f};
  const f32x2 c2 = {-0.1029432397f, -0.1029432397f};
  f32x2 t = u * u;
  f32x2 a = u * (t * c2 + c1);
  f32x2 d = {1.f + __builtin_amdgcn_exp2f(a[0]), 1.f + __builtin_amdgcn_exp2f(a[1])};
  f32x2 r = {__builtin_amdgcn_rcpf(d[0]), __builtin_amdgcn_rcpf(d[1])};
  f32x2 o = u * r;
  return pk2(o[0], o[1]);
}
#define SCHED() __builtin_amdgcn_sched_barrier(0)
#define BAR() do { SCHED(); asm volatile("" ::: "memory"); __builtin_amdgcn_s_waitcnt(0xC07F); __builtin_amdgcn_s_barrier(); asm volatile("" ::: "memory"); SCHED(); } while (0)
__device__ __forceinline__ void glds16(const void* g, char* l) {
  __builtin_amdgcn_global_load_lds((const unsigned*)g, (LDS_AS unsigned*)l, 16, 0, 0);
}
__device__ __forceinline__ bf16x8 tr_frag(const char* p0, const char* p1) {
  s16x4 a = __builtin_amdgcn_ds_read_tr16_b64_v4i16((LDS_AS s16x4*)p0);
  s16x4 c = __builtin_amdgcn_ds_read_tr16_b64_v4i16((LDS_AS s16x4*)p1);
  return __builtin_shufflevector(a, c, 0, 1, 2, 3, 4, 5, 6, 7);
}

constexpr int YLD = 3136;
constexpr size_t WS_Y = 0;
constexpr size_t WS_Z2N = WS_Y + (size_t)S_ * YLD * 2 + 256;
constexpr size_t WS_OH = WS_Z2N + (size_t)S_ * 896 * 2;
constexpr size_t WS_WT = WS_OH + (size_t)S_ * KO * 2;
constexpr size_t WS_KVP = WS_WT + (size_t)D_ * KO * 2;
constexpr size_t WS_END = WS_KVP + (size_t)8 * 16 * 56 * 32 * 4;

#define STAMP(K, I) do { } while (0)
#define SGB(mask, n) __builtin_amdgcn_sched_group_barrier(mask, n, 0)

__device__ __forceinline__ void phi_map(bf16x8 qf, bf16x8 wp0, bf16x8 wp1, int g, unsigned (&pp)[2][2],
                                        unsigned (&pm)[2][2]) {
  f32x4 z = {0.f, 0.f, 0.f, 0.f};
  f32x4 p0 = mfma16(wp0, qf, z);
  f32x4 p1 = mfma16(wp1, qf, z);
  float v[8];
  {
    unsigned a = pk2(p0[0], p0[1]), b = pk2(p0[2], p0[3]), c = pk2(p1[0], p1[1]), d = pk2(p1[2], p1[3]);
    v[0] = lo_f(a); v[1] = hi_f(a); v[2] = lo_f(b); v[3] = hi_f(b);
    v[4] = lo_f(c); v[5] = hi_f(c); v[6] = lo_f(d); v[7] = hi_f(d);
  }
  const bool valid1 = (g != 3);
  if (!valid1) { v[4] = v[0]; v[5] = v[0]; v[6] = v[0]; v[7] = v[0]; }
  float mx = fmaxf(fmaxf(fmaxf(v[0], v[1]), fmaxf(v[2], v[3])), fmaxf(fmaxf(v[4], v[5]), fmaxf(v[6], v[7])));
  float mn = fminf(fminf(fminf(v[0], v[1]), fminf(v[2], v[3])), fminf(fminf(v[4], v[5]), fminf(v[6], v[7])));
  mx = x32_max(x16_max(mx));
  mn = x32_min(x16_min(mn));
  const float L2E = 1.4426950408889634f;
  const float mxl = mx * L2E, mnl = mn * L2E;
  float ep[8], em[8];
  float sp = 0.f, sm = 0.f;
#pragma unroll
  for (int j = 0; j < 8; ++j) {
    ep[j] = __builtin_amdgcn_exp2f(__builtin_fmaf(v[j], L2E, -mxl));
    em[j] = __builtin_amdgcn_exp2f(__builtin_fmaf(v[j], -L2E, mnl));
    if (j >= 4) { ep[j] = valid1 ? ep[j] : 0.f; em[j] = valid1 ? em[j] : 0.f; }
    sp += ep[j];
    sm += em[j];
  }
  sp = x32_add(x16_add(sp));
  sm = x32_add(x16_add(sm));
  const float rp = __builtin_amdgcn_rcpf(sp), rm = __builtin_amdgcn_rcpf(sm);
  pp[0][0] = pk2(ep[0] * rp, ep[1] * rp); pp[0][1] = pk2(ep[2] * rp, ep[3] * rp);
  pp[1][0] = pk2(ep[4] * rp, ep[5] * rp); pp[1][1] = pk2(ep[6] * rp, ep[7] * rp);
  pm[0][0] = pk2(em[0] * rm, em[1] * rm); pm[0][1] = pk2(em[2] * rm, em[3] * rm);
  pm[1][0] = pk2(em[4] * rm, em[5] * rm); pm[1][1] = pk2(em[6] * rm, em[7] * rm);
}

constexpr int K1_XB = 128 * 896;
constexpr int K1_WST = 32 * 448;
constexpr int K1_LDS = K1_XB + 3 * K1_WST;

__global__ __launch_bounds__(640) void k1_kernel(const u16* __restrict__ x, const u16* __restrict__ wqkv,
                                                  const u16* __restrict__ wo, const u16* __restrict__ win,
                                                  const u16* __restrict__ wout, const u16* __restrict__ wsp,
                                                  char* __restrict__ ws) {
  __shared__ __attribute__((aligned(16))) char sm[K1_LDS];
  const int tid = threadIdx.x, lane = tid & 63, wave = __builtin_amdgcn_readfirstlane(tid >> 6);
  const int b = blockIdx.x;
  STAMP(0, 0);
  if (b >= 224) {
    const int kb = b - 224;
    const int k0 = kb * 64;
    if (kb < 21) {
    u16* T = (u16*)sm;
    constexpr int TS = 456;
    u32x4 v[6];
#pragma unroll
    for (int j = 0; j < 6; ++j) {
      int i = tid + 640 * j;
      if (i >= 64 * 56) i = tid;
      const int r = i / 56, c = i % 56;
      const int k = k0 + r;
      const u16* src = (k < 448) ? (wo + (size_t)k * 448) : (wout + (size_t)(k - 448) * 448);
      v[j] = *(const u32x4*)(src + c * 8);
    }
#pragma unroll
    for (int j = 0; j < 6; ++j) {
      const int i = tid + 640 * j;
      if (i < 64 * 56) *(u32x4*)(T + (i / 56) * TS + (i % 56) * 8) = v[j];
    }
    __syncthreads();
    u16* wT = (u16*)(ws + WS_WT);
    for (int i = tid; i < 448 * 8; i += 640) {
      const int n = i >> 3, c = i & 7;
      u16 t[8];
#pragma unroll
      for (int j = 0; j < 8; ++j) t[j] = T[(c * 8 + j) * TS + n];
      u32x4 o;
#pragma unroll
      for (int j = 0; j < 4; ++j) o[j] = (unsigned)t[2 * j] | ((unsigned)t[2 * j + 1] << 16);
      *(u32x4*)(wT + ((size_t)((n >> 4) * 42 + (k0 >> 5) + (c >> 2)) * 64 + (c & 3) * 16 + (n & 15)) * 8) = o;
    }
    }
    STAMP(0, 1);
    {
      const char* p = (const char*)wsp + (size_t)kb * 262144 + tid * 16;
      unsigned a = 0;
#pragma unroll
      for (int i = 0; i < 25; ++i) {
        u32x4 v = *(const u32x4*)(p + i * 10240);
        a |= v[0] & v[1] & v[2] & v[3];
      }
      asm volatile("" ::"v"(a));
    }
    STAMP(0, 31);
    return;
  }

  const int xcd = b & 7, idx = b >> 3;
  const int cb = (xcd & 1) * 7 + idx % 7;
  const int rt = (xcd >> 1) * 4 + idx / 7;
  const int row0 = rt * 128;
  char* wring = sm + K1_XB;
  const int g = lane >> 4, r16 = lane & 15;
  const int wm = (wave >> 1) & 3, wn = wave & 1;
  constexpr int RST = 464;

  if (wave >= 8) {
    const int lw = wave - 8;
    const u16* W = (cb < 6) ? (wqkv + 224 * cb) : (win + 224 * (cb - 6));
    const int ldw = (cb < 6) ? NQKV : NIN;
    const u16* src[7];
#pragma unroll
    for (int j = 0; j < 7; ++j) {
      const int o = (lw + 2 * j) * 1024 + lane * 16;
      const int k = o / 448, cp = (o % 448) >> 4;
      const int G = (cp >> 1) ^ ((k >> 3) & 1);
      const int c = 2 * G + (cp & 1);
      src[j] = W + (size_t)k * ldw + 8 * c;
    }
    const size_t wstep = (size_t)32 * ldw;
    char* dst0 = wring + lw * 1024;
#pragma unroll
    for (int s = 0; s < 3; ++s)
#pragma unroll
      for (int j = 0; j < 7; ++j) glds16(src[j] + s * wstep, dst0 + s * K1_WST + j * 2048);
    asm volatile("s_waitcnt vmcnt(14)" ::: "memory");
    __builtin_amdgcn_s_barrier();
    int nb = 0;
    for (int t = 0; t < 14; ++t) {
      if (t + 2 < 14) asm volatile("s_waitcnt vmcnt(7)" ::: "memory");
      else asm volatile("s_waitcnt vmcnt(0)" ::: "memory");
      __builtin_amdgcn_s_barrier();
      if (t + 3 < 14) {
        char* dst = dst0 + nb * K1_WST;
#pragma unroll
        for (int j = 0; j < 7; ++j) glds16(src[j] + (t + 3) * wstep, dst + j * 2048);
        nb = (nb == 2) ? 0 : nb + 1;
      }
    }
    __builtin_amdgcn_s_barrier();
  } else {
    u32x4 xv[2][7];
    const int rr = lane >> 3, cc = lane & 7;
#pragma unroll
    for (int p = 0; p < 2; ++p) {
      const u16* xp = x + (size_t)(row0 + 16 * wave + 8 * p + rr) * D_ + cc * 8;
#pragma unroll
      for (int i = 0; i < 7; ++i) xv[p][i] = *(const u32x4*)(xp + 64 * i);
    }
#pragma unroll
    for (int p = 0; p < 2; ++p) {
      float s0 = 0.f, s1 = 0.f;
#pragma unroll
      for (int i = 0; i < 7; ++i) {
        s0 = dot2(xv[p][i][0], 0x3F803F80u, s0); s1 = dot2(xv[p][i][1], 0x3F803F80u, s1);
        s0 = dot2(xv[p][i][2], 0x3F803F80u, s0); s1 = dot2(xv[p][i][3], 0x3F803F80u, s1);
      }
      const float mean = red8_add(s0 + s1) * (1.f / 448.f);
      const float mb = rbf(mean);
      float q0 = 0.f, q1 = 0.f;
#pragma unroll
      for (int i = 0; i < 7; ++i)
#pragma unroll
        for (int j = 0; j < 4; ++j) {
          const unsigned d = pk2(lo_f(xv[p][i][j]) - mb, hi_f(xv[p][i][j]) - mb);
          xv[p][i][j] = d;
          if (j & 1) q1 = dot2(d, d, q1); else q0 = dot2(d, d, q0);
        }
      const float q = red8_add(q0 + q1);
      const float dm = mean - mb;
      const float var = fmaxf(q * (1.f / 448.f) - dm * dm, 0.f);
      const float sd = rbf(sqrtf(rbf(rbf(var) + 1e-5f)));
      const float inv = 1.f / sd;
      const int row = 16 * wave + 8 * p + rr;
      char* dst = sm + row * 896 + ((cc ^ ((row >> 1) & 7)) << 4);
#pragma unroll
      for (int i = 0; i < 7; ++i) {
        u32x4 o;
#pragma unroll
        for (int j = 0; j < 4; ++j) o[j] = pk2(lo_f(xv[p][i][j]) * inv, hi_f(xv[p][i][j]) * inv);
        *(u32x4*)(dst + 128 * i) = o;
      }
    }
    STAMP(0, 1);
    f32x4 acc[7][2];
#pragma unroll
    for (int nt = 0; nt < 7; ++nt) { acc[nt][0] = f32x4{0.f, 0.f, 0.f, 0.f}; acc[nt][1] = f32x4{0.f, 0.f, 0.f, 0.f}; }
    const int sw = (r16 >> 1) & 7;
    const char* xbase = sm + (32 * wm + r16) * 896;
    const int xoE = ((g ^ sw) << 4), xoO = (((4 | g) ^ sw) << 4);
    const int wrow = (8 * g + ((lane >> 2) & 3)) * 448 + (lane & 3) * 8 + 7 * wn * 32;
    const int wE = wrow + ((wn & 1) ? -32 * (g & 1) : 32 * (g & 1));
    const int wO = wrow + ((wn & 1) ? 32 * (g & 1) : -32 * (g & 1));
    bf16x8 xa[2], wa[7], xb[2], wb[7];
#define K1_READ(XF, WF, T, BUF)                                                              \
  do {                                                                                       \
    const char* xp_ = xbase + ((T) >> 1) * 128 + (((T)&1) ? xoO : xoE);                      \
    const char* wb_ = wring + (BUF)*K1_WST;                                                  \
    XF[0] = *(const bf16x8*)(xp_);                                                           \
    XF[1] = *(const bf16x8*)(xp_ + 16 * 896);                                                \
    _Pragma("unroll") for (int nt = 0; nt < 7; ++nt) {                                       \
      const char* p_ = wb_ + ((nt & 1) ? wO : wE) + nt * 32;                                 \
      WF[nt] = tr_frag(p_, p_ + 4 * 448);                                                    \
    }                                                                                        \
  } while (0)
#define K1_MMA(XF, WF)                                                                       \
  do {                                                                                       \
    _Pragma("unroll") for (int nt = 0; nt < 7; ++nt) acc[nt][0] = mfma16(WF[nt], XF[0], acc[nt][0]); \
    _Pragma("unroll") for (int nt = 0; nt < 7; ++nt) acc[nt][1] = mfma16(WF[nt], XF[1], acc[nt][1]); \
  } while (0)
#define K1_MIX()                                                                             \
  do {                                                                                       \
    SGB(0x100, 2);                                                                           \
    _Pragma("unroll") for (int i_ = 0; i_ < 14; ++i_) { SGB(0x008, 1); SGB(0x100, 1); }      \
  } while (0)
    BAR();
    STAMP(0, 2);
    K1_READ(xa, wa, 0, 0);
    int rb = 1;
#pragma unroll 1
    for (int t = 0; t < 12; t += 2) {
      BAR();
      STAMP(0, 3 + t);
      K1_READ(xb, wb, t + 1, rb);
      rb = (rb == 2) ? 0 : rb + 1;
      K1_MMA(xa, wa);
      K1_MIX();
      BAR();
      STAMP(0, 4 + t);
      K1_READ(xa, wa, t + 2, rb);
      rb = (rb == 2) ? 0 : rb + 1;
      K1_MMA(xb, wb);
      K1_MIX();
    }
    BAR();
    K1_READ(xb, wb, 13, 1);
    K1_MMA(xa, wa);
    K1_MIX();
    BAR();
    K1_MMA(xb, wb);
    BAR();
    STAMP(0, 17);
#pragma unroll
    for (int mt = 0; mt < 2; ++mt) {
      char* dst = sm + (32 * wm + 16 * mt + r16) * RST + (112 * wn + 4 * g) * 2;
#pragma unroll
      for (int nt = 0; nt < 7; ++nt)
        *(u32x2*)(dst + 32 * nt) = u32x2{pk2(acc[nt][mt][0], acc[nt][mt][1]), pk2(acc[nt][mt][2], acc[nt][mt][3])};
    }
  }
  __syncthreads();
  u16* dstb = (u16*)(ws + WS_Y) + (size_t)row0 * YLD + 224 * cb;
#pragma unroll
  for (int i = 0; i < 6; ++i) {
    const int id = tid + 640 * i;
    if (id < 128 * 28) {
      const int s = id / 28, c = id % 28;
      *(u32x4*)(dstb + (size_t)s * YLD + c * 8) = *(const u32x4*)(sm + s * RST + c * 16);
    }
  }
  STAMP(0, 31);
}

__device__ __forceinline__ float red64_add(float v) {
  v = red8_add(v);
  v += __uint_as_float(__builtin_amdgcn_mov_dpp(__float_as_uint(v), 0x140, 0xf, 0xf, true));
  return x32_add(x16_add(v));
}

constexpr int K2_PW = 64 * 80 + 32 * 64;
__global__ __launch_bounds__(512) void k2_kernel(const u16* __restrict__ wphi, char* __restrict__ ws) {
  __shared__ __attribute__((aligned(16))) char sm[8 * 56 * 32 * 4 + 2048];
  const int tid = threadIdx.x, lane = tid & 63, wave = __builtin_amdgcn_readfirstlane(tid >> 6);
  const int b = blockIdx.x;
  const u16* Y = (const u16*)(ws + WS_Y);
  STAMP(1, 0);
  if (b < 128) {
    const int h = b & 15, rg = b >> 4;
    const int g = lane >> 4, r16 = lane & 15;
    const int rowb = rg * 256 + wave * 32;
    u32x2 kv_[2][2];
#pragma unroll
    for (int st = 0; st < 2; ++st) {
      const u16* kp = Y + (size_t)(rowb + st * 16 + r16) * YLD + 448 + h * 28 + 8 * g;
      kv_[st][0] = *(const u32x2*)(kp);
      kv_[st][1] = *(const u32x2*)(kp + 4);
    }
    u32x2 vv[4];
#pragma unroll
    for (int i = 0; i < 4; ++i) {
      const int row = (lane >> 3) + 8 * i, pc = lane & 7;
      vv[i] = *(const u32x2*)(Y + (size_t)(rowb + row) * YLD + 896 + h * 28 + 4 * (pc < 7 ? pc : 0));
      if (pc == 7) vv[i] = u32x2{0x00003F80u, 0u};
    }
    u32x4 wraw = {0u, 0u, 0u, 0u};
    if (tid < 98) wraw = *(const u32x4*)(wphi + (size_t)h * 784 + tid * 8);
    char* WP = sm + 8 * 56 * 32 * 4;
    if (tid < 98) *(u32x4*)(WP + tid * 16) = wraw;
    char* PK = sm + wave * K2_PW;
    char* VS = PK + 64 * 80;
    __syncthreads();
    STAMP(1, 1);
    const char* wp = WP + (8 * g + ((lane >> 2) & 3)) * 56 + (lane & 3) * 8;
    bf16x8 wp0 = tr_frag(wp, wp + 4 * 56);
    bf16x8 wp1 = tr_frag(wp + 32, wp + 4 * 56 + 32);
    if (g == 3) {
#pragma unroll
      for (int j = 4; j < 8; ++j) { wp0[j] = 0; wp1[j] = 0; }
    }
    f32x4 kacc[4][2];
#pragma unroll
    for (int a = 0; a < 4; ++a) { kacc[a][0] = f32x4{0.f, 0.f, 0.f, 0.f}; kacc[a][1] = f32x4{0.f, 0.f, 0.f, 0.f}; }
#pragma unroll
    for (int pr = 0; pr < 1; ++pr) {
#pragma unroll
      for (int i = 0; i < 4; ++i) {
        const int row = (lane >> 3) + 8 * i;
        const int slot = 2 * (row & 15) + (row >> 4);
        *(u32x2*)(VS + slot * 64 + (lane & 7) * 8) = vv[4 * pr + i];
      }
      unsigned pp[2][2][2], pm[2][2][2];
#pragma unroll
      for (int mt = 0; mt < 2; ++mt) {
        u32x4 q4 = {kv_[2 * pr + mt][0][0], kv_[2 * pr + mt][0][1], kv_[2 * pr + mt][1][0], kv_[2 * pr + mt][1][1]};
        if (g == 3) { q4[2] = 0u; q4[3] = 0u; }
        phi_map(__builtin_bit_cast(bf16x8, q4), wp0, wp1, g, pp[mt], pm[mt]);
      }
#pragma unroll
      for (int ft = 0; ft < 2; ++ft) {
        if (ft == 0 || g != 3) {
          char* d0 = PK + (16 * ft + 4 * g) * 80 + r16 * 4;
#pragma unroll
          for (int j = 0; j < 2; ++j) {
            *(unsigned*)(d0 + (2 * j) * 80) = (pp[0][ft][j] & 0xffffu) | (pp[1][ft][j] << 16);
            *(unsigned*)(d0 + (2 * j + 1) * 80) = (pp[0][ft][j] >> 16) | (pp[1][ft][j] & 0xffff0000u);
            *(unsigned*)(d0 + (28 + 2 * j) * 80) = (pm[0][ft][j] & 0xffffu) | (pm[1][ft][j] << 16);
            *(unsigned*)(d0 + (28 + 2 * j + 1) * 80) = (pm[0][ft][j] >> 16) | (pm[1][ft][j] & 0xffff0000u);
          }
        }
      }
      __builtin_amdgcn_s_waitcnt(0xC07F);
      __builtin_amdgcn_wave_barrier();
      bf16x8 bfr[2];
      {
        const char* vp = VS + (8 * g + ((lane >> 2) & 3)) * 64 + (lane & 3) * 8;
        bfr[0] = tr_frag(vp, vp + 4 * 64);
        bfr[1] = tr_frag(vp + 32, vp + 4 * 64 + 32);
      }
#pragma unroll
      for (int a = 0; a < 4; ++a) {
        bf16x8 af = *(const bf16x8*)(PK + (16 * a + r16) * 80 + g * 16);
        kacc[a][0] = mfma16(af, bfr[0], kacc[a][0]);
        kacc[a][1] = mfma16(af, bfr[1], kacc[a][1]);
      }
      __builtin_amdgcn_s_waitcnt(0xC07F);
      __builtin_amdgcn_wave_barrier();
    }
    STAMP(1, 2);
    __syncthreads();
    float* red = (float*)sm + wave * (56 * 32);
#pragma unroll
    for (int a = 0; a < 4; ++a)
#pragma unroll
      for (int c = 0; c < 2; ++c)
#pragma unroll
        for (int j = 0; j < 4; ++j) {
          const int f = 16 * a + 4 * g + j;
          if (f < 56) red[f * 32 + 16 * c + r16] = kacc[a][c][j];
        }
    __syncthreads();
    if (tid < 448) {
      const f32x4* rp = (const f32x4*)sm + tid;
      f32x4 s = (rp[0] + rp[448]) + (rp[2 * 448] + rp[3 * 448]);
      s += (rp[4 * 448] + rp[5 * 448]) + (rp[6 * 448] + rp[7 * 448]);
      *((f32x4*)(ws + WS_KVP) + (size_t)(rg * 16 + h) * 448 + tid) = s;
    }
  } else {
    const int row0 = (b - 128) * 16 + wave * 2;
    u16* z2n = (u16*)(ws + WS_Z2N);
    const int le = (lane < 56) ? lane : 0;
    u32x4 v[2][2];
#pragma unroll
    for (int r = 0; r < 2; ++r)
#pragma unroll
      for (int c = 0; c < 2; ++c) v[r][c] = *(const u32x4*)(Y + (size_t)(row0 + r) * YLD + 2240 + (le + 56 * c) * 8);
#pragma unroll
    for (int r = 0; r < 2; ++r) {
      float s = 0.f;
#pragma unroll
      for (int c = 0; c < 2; ++c)
#pragma unroll
        for (int j = 0; j < 4; ++j) {
          v[r][c][j] = gelu_pk(v[r][c][j]);
          s = dot2(v[r][c][j], 0x3F803F80u, s);
        }
      if (lane >= 56) s = 0.f;
      const float mean = red64_add(s) * (1.f / 896.f);
      const float mb = rbf(mean);
      float q = 0.f;
#pragma unroll
      for (int c = 0; c < 2; ++c)
#pragma unroll
        for (int j = 0; j < 4; ++j) {
          const float a = lo_f(v[r][c][j]) - mean, e = hi_f(v[r][c][j]) - mean;
          q += a * a + e * e;
          v[r][c][j] = pk2(lo_f(v[r][c][j]) - mb, hi_f(v[r][c][j]) - mb);
        }
      if (lane >= 56) q = 0.f;
      const float var = red64_add(q) * (1.f / 896.f);
      const float sd = rbf(sqrtf(rbf(rbf(var) + 1e-5f)));
      const float inv = 1.f / sd;
      if (lane < 56) {
#pragma unroll
        for (int c = 0; c < 2; ++c) {
          u32x4 o;
#pragma unroll
          for (int j = 0; j < 4; ++j) o[j] = pk2(lo_f(v[r][c][j]) * inv, hi_f(v[r][c][j]) * inv);
          *(u32x4*)(sm + (wave * 2 + r) * 1808 + (lane + 56 * c) * 16) = o;
        }
      }
    }
    __syncthreads();
    {
      const int rb0 = (b - 128) * 16;
      const int kb = rb0 >> 5, half = (rb0 >> 4) & 1;
      const int g = lane >> 4;
#pragma unroll
      for (int i = 0; i < 4; ++i) {
        const int u = wave + 8 * i;
        if (u < 28) {
          const int T = 2 * u + (g >> 1), gp = g & 1;
          const char* p = sm + (8 * gp + ((lane >> 2) & 3)) * 1808 + T * 32 + (lane & 3) * 8;
          bf16x8 fr = tr_frag(p, p + 4 * 1808);
          const int pn = T / 7, n = T % 7;
          *(bf16x8*)(z2n + ((size_t)((pn * 64 + kb) * 7 + n) * 64 + (2 * half + gp) * 16 + (lane & 15)) * 8) = fr;
        }
      }
    }
  }
  STAMP(1, 31);
}

constexpr int K3_RS = 464;
constexpr int K3_STAGE = 64 * 256;
constexpr int K3_NB = 8;
constexpr int K3_AUX = K3_NB * K3_STAGE;
__global__ __launch_bounds__(768) void k3_kernel(const u16* __restrict__ wsp, const u16* __restrict__ bsp,
                                                  const u16* __restrict__ wphi, char* __restrict__ ws) {
  __shared__ __attribute__((aligned(16))) char sm[K3_NB * K3_STAGE + 32 * 144 + 2048 + 8192];
  const int tid = threadIdx.x, lane = tid & 63, wave = __builtin_amdgcn_readfirstlane(tid >> 6);
  const int b = blockIdx.x;
  const int xcd = b & 7, idx = b >> 3;
  const int mt_ = (xcd >> 1) * 8 + (idx & 7);
  const int nt_ = (xcd & 1) * 4 + (idx >> 3);
  const int t0 = mt_ * 64, f0 = nt_ * 112;
  const u16* z2n = (const u16*)(ws + WS_Z2N);
  const u16* Y = (const u16*)(ws + WS_Y);
  u16* oh = (u16*)(ws + WS_OH);
  const int g = lane >> 4, r16 = lane & 15;
  const int h = b & 15;
  u32x4 zv[2];
  float bb[2];
  STAMP(2, 0);
  if (wave >= 8) {
    const int lw = wave - 8;
    const u16* src[4];
#pragma unroll
    for (int j = 0; j < 4; ++j) {
      const int row = 4 * (lw + 4 * j) + (lane >> 4);
      src[j] = wsp + (size_t)(t0 + row) * S_ + (((lane & 15) ^ (row & 15)) << 3);
    }
    char* dst0 = sm + lw * 1024;
#pragma unroll
    for (int s = 0; s < K3_NB; ++s)
#pragma unroll
      for (int j = 0; j < 4; ++j) glds16(src[j] + s * 128, dst0 + s * K3_STAGE + j * 4096);
    asm volatile("s_waitcnt vmcnt(28)" ::: "memory");
    __builtin_amdgcn_s_barrier();
#pragma unroll 1
    for (int t = 0; t < 16; ++t) {
      if (t <= 8) asm volatile("s_waitcnt vmcnt(24)" ::: "memory");
      else if (t == 9) asm volatile("s_waitcnt vmcnt(20)" ::: "memory");
      else if (t == 10) asm volatile("s_waitcnt vmcnt(16)" ::: "memory");
      else if (t == 11) asm volatile("s_waitcnt vmcnt(12)" ::: "memory");
      else if (t == 12) asm volatile("s_waitcnt vmcnt(8)" ::: "memory");
      else if (t == 13) asm volatile("s_waitcnt vmcnt(4)" ::: "memory");
      else asm volatile("s_waitcnt vmcnt(0)" ::: "memory");
      __builtin_amdgcn_s_barrier();
      STAMP(2, 5 + t);
      if (t + 8 < 16) {
        char* dst = dst0 + (t & 7) * K3_STAGE;
#pragma unroll
        for (int j = 0; j < 4; ++j) glds16(src[j] + (t + 8) * 128, dst + j * 4096);
      }
      if (t == 1 || t == 4) {
        const int st = (t == 4) ? 1 : 0;
        const char* KT = sm + K3_AUX;
        const char* WP = KT + 32 * 144;
        bf16x8 af[2][2];
#pragma unroll
        for (int dt = 0; dt < 2; ++dt)
#pragma unroll
          for (int mf = 0; mf < 2; ++mf) af[dt][mf] = *(const bf16x8*)(KT + (16 * dt + r16) * 144 + (32 * mf + 8 * g) * 2);
        const char* wp = WP + (8 * g + ((lane >> 2) & 3)) * 56 + (lane & 3) * 8;
        bf16x8 wp0 = tr_frag(wp, wp + 4 * 56);
        bf16x8 wp1 = tr_frag(wp + 32, wp + 4 * 56 + 32);
        if (g == 3) {
#pragma unroll
          for (int j = 4; j < 8; ++j) { wp0[j] = 0; wp1[j] = 0; }
        }
        u32x4 q4 = *(const u32x4*)(WP + 2048 + (lw * 32 + st * 16 + r16) * 64 + g * 16);
        if (g == 3) { q4[2] = 0u; q4[3] = 0u; }
        unsigned pp[2][2], pm[2][2];
        phi_map(__builtin_bit_cast(bf16x8, q4), wp0, wp1, g, pp, pm);
        u32x4 c1 = {pp[0][0], pp[0][1], pp[1][0], pp[1][1]};
        u32x4 c2 = {pm[0][0], pm[0][1], pm[1][0], pm[1][1]};
        f32x4 o0 = {0.f, 0.f, 0.f, 0.f}, o1 = {0.f, 0.f, 0.f, 0.f};
        o0 = mfma16(af[0][0], __builtin_bit_cast(bf16x8, c1), o0);
        o1 = mfma16(af[1][0], __builtin_bit_cast(bf16x8, c1), o1);
        o0 = mfma16(af[0][1], __builtin_bit_cast(bf16x8, c2), o0);
        o1 = mfma16(af[1][1], __builtin_bit_cast(bf16x8, c2), o1);
        float den = __shfl(o1[0], 48 + r16, 64);
        den = rbf(rbf(den) + 1e-6f);
        const float inv = 1.f / den;
        const int mtile = (b >> 4) * 8 + lw * 2 + st;
        {
          const int k = h * 28 + 4 * g;
          u16* dst = oh + ((size_t)(mtile * 42 + (k >> 5)) * 64 + ((k >> 3) & 3) * 16 + r16) * 8 + (k & 7);
          *(u32x2*)(dst) = u32x2{pk2(rbf(o0[0]) * inv, rbf(o0[1]) * inv), pk2(rbf(o0[2]) * inv, rbf(o0[3]) * inv)};
        }
        if (g != 3) {
          const int k = h * 28 + 16 + 4 * g;
          u16* dst = oh + ((size_t)(mtile * 42 + (k >> 5)) * 64 + ((k >> 3) & 3) * 16 + r16) * 8 + (k & 7);
          *(u32x2*)(dst) = u32x2{pk2(rbf(o1[0]) * inv, rbf(o1[1]) * inv), pk2(rbf(o1[2]) * inv, rbf(o1[3]) * inv)};
        }
      }
    }
    {
      const int t = tid / 14, c = tid % 14;
      zv[0] = *(const u32x4*)(Y + (size_t)(t0 + t) * YLD + 1344 + f0 + c * 8);
      bb[0] = bf2f(bsp[t0 + t]);
      zv[1] = zv[0]; bb[1] = bb[0];
    }
    __builtin_amdgcn_s_barrier();
  } else {
    const int kq = wave >> 1, wn = (wave & 1) ^ (wave >> 2);
    const u16* bp = z2n + ((size_t)(nt_ * 64 + kq) * 7 + 4 * wn) * 512 + lane * 8;
    const int arow = r16 * 256 + (((4 * kq + g) ^ r16) << 4);
    f32x4 acc[4][4];
    bf16x8 aA[4], aB[4], b0[4], b1[4], b2[4];
#define K3_BLOAD(BF, T, NT)                                                                     \
  do { _Pragma("unroll") for (int n = 0; n < NT; ++n) BF[n] = *(const bf16x8*)(bp + ((T)*28 + n) * 512); } while (0)
#define K3_AREAD(AF, T)                                                                         \
  do { _Pragma("unroll") for (int m = 0; m < 4; ++m) AF[m] = *(const bf16x8*)(sm + ((T)&7) * K3_STAGE + arow + m * 4096); } while (0)
#define K3_MMA(AF, BF, NT)                                                                      \
  do {                                                                                          \
    _Pragma("unroll") for (int n = 0; n < NT; ++n)                                              \
      _Pragma("unroll") for (int m = 0; m < 4; ++m) acc[n][m] = mfma16(BF[n], AF[m], acc[n][m]); \
  } while (0)
#define K3_STEP(T, AC, AN, BC, BL, NT)                                                          \
  do {                                                                                          \
    BAR();                                                                                      \
    STAMP(2, 5 + (T));                                                                          \
    if ((T) + 1 < 16) K3_AREAD(AN, (T) + 1);                                                    \
    if ((T) + 2 < 16) K3_BLOAD(BL, (T) + 2, NT);                                                \
    K3_MMA(AC, BC, NT);                                                                         \
    SGB(0x100, 4);                                                                              \
    _Pragma("unroll") for (int i_ = 0; i_ < NT; ++i_) { SGB(0x008, 2); SGB(0x020, 1); }         \
  } while (0)
#define K3_LOOP(NT) \
  do { \
    BAR(); \
    STAMP(2, 4); \
    K3_AREAD(aA, 0); \
    K3_STEP(0, aA, aB, b0, b2, NT); \
    K3_STEP(1, aB, aA, b1, b0, NT); \
    K3_STEP(2, aA, aB, b2, b1, NT); \
    K3_STEP(3, aB, aA, b0, b2, NT); \
    K3_STEP(4, aA, aB, b1, b0, NT); \
    K3_STEP(5, aB, aA, b2, b1, NT); \
    K3_STEP(6, aA, aB, b0, b2, NT); \
    K3_STEP(7, aB, aA, b1, b0, NT); \
    K3_STEP(8, aA, aB, b2, b1, NT); \
    K3_STEP(9, aB, aA, b0, b2, NT); \
    K3_STEP(10, aA, aB, b1, b0, NT); \
    K3_STEP(11, aB, aA, b2, b1, NT); \
    K3_STEP(12, aA, aB, b0, b2, NT); \
    K3_STEP(13, aB, aA, b1, b0, NT); \
    _Pragma("unroll") for (int i = 0; i < 2; ++i) {                                             \
      int id = tid + 768 * i;                                                                   \
      if (id >= 896) id = tid;                                                                  \
      const int t = id / 14, c = id % 14;                                                       \
      zv[i] = *(const u32x4*)(Y + (size_t)(t0 + t) * YLD + 1344 + f0 + c * 8);                  \
      bb[i] = bf2f(bsp[t0 + t]);                                                                \
    }                                                                                           \
    K3_STEP(14, aA, aB, b2, b1, NT); \
    K3_STEP(15, aB, aA, b0, b2, NT); \
  } while (0)
    if (wn == 0) { K3_BLOAD(b0, 0, 4); K3_BLOAD(b1, 1, 4); }
    else { K3_BLOAD(b0, 0, 3); K3_BLOAD(b1, 1, 3); }
    SCHED();
    {
      f32x4 part[8];
      {
        const int te = (tid < 448) ? tid : 0;
        const f32x4* kvp = (const f32x4*)(ws + WS_KVP) + (size_t)h * 448 + te;
#pragma unroll
        for (int p = 0; p < 8; ++p) part[p] = kvp[(size_t)p * 16 * 448];
      }
      u32x4 wraw = {0u, 0u, 0u, 0u};
      if (tid < 98) wraw = *(const u32x4*)(wphi + (size_t)h * 784 + tid * 8);
      u32x2 qv[2];
#pragma unroll
      for (int i = 0; i < 2; ++i) {
        const int id = tid + 512 * i;
        qv[i] = *(const u32x2*)(Y + (size_t)((b >> 4) * 128 + (id >> 3)) * YLD + h * 28 + (id & 7) * 4);
      }
      char* KT = sm + K3_AUX;
      char* WP = KT + 32 * 144;
      if (tid < 98) *(u32x4*)(WP + tid * 16) = wraw;
      *(u32x2*)(WP + 2048 + tid * 8) = qv[0];
      *(u32x2*)(WP + 2048 + 4096 + tid * 8) = qv[1];
      if (tid < 448) {
        const f32x4 s = ((part[0] + part[1]) + (part[2] + part[3])) + ((part[4] + part[5]) + (part[6] + part[7]));
        const int fp = tid >> 3, d0 = 4 * (tid & 7);
        const int mf = (fp >= 28) ? 1 : 0, f = fp - 28 * mf;
        const int pos = mf * 32 + ((f & 15) >> 2) * 8 + (f & 3) + 4 * (f >> 4);
        const unsigned a = pk2(s[0], s[1]), c = pk2(s[2], s[3]);
        char* d = KT + d0 * 144 + pos * 2;
        *(u16*)(d) = (u16)a; *(u16*)(d + 144) = (u16)(a >> 16);
        *(u16*)(d + 288) = (u16)c; *(u16*)(d + 432) = (u16)(c >> 16);
      } else {
        const int i = tid - 448, d = i >> 1, mf = i & 1;
        *(u32x2*)(KT + d * 144 + (mf * 32 + 28) * 2) = u32x2{0u, 0u};
      }
    }
    STAMP(2, 1);
    SCHED();
#pragma unroll
    for (int n = 0; n < 4; ++n)
#pragma unroll
      for (int m = 0; m < 4; ++m) acc[n][m] = f32x4{0.f, 0.f, 0.f, 0.f};
    if (wn == 0) K3_LOOP(4);
    else K3_LOOP(3);
    BAR();
    STAMP(2, 21);
    char* rd = sm + kq * (64 * K3_RS) + r16 * K3_RS + (64 * wn + 4 * g) * 4;
#pragma unroll
    for (int n = 0; n < 4; ++n)
      if (n < 3 || wn == 0) {
#pragma unroll
        for (int m = 0; m < 4; ++m) *(f32x4*)(rd + 16 * m * K3_RS + 64 * n) = acc[n][m];
      }
  }
  __syncthreads();
#pragma unroll
  for (int i = 0; i < 2; ++i) {
    const int id = tid + 768 * i;
    if (id < 896) {
      const int t = id / 14, c = id % 14;
      const char* p = sm + t * K3_RS + c * 32;
      f32x4 v0 = (*(const f32x4*)(p) + *(const f32x4*)(p + 64 * K3_RS)) +
                 (*(const f32x4*)(p + 128 * K3_RS) + *(const f32x4*)(p + 192 * K3_RS));
      f32x4 v1 = (*(const f32x4*)(p + 16) + *(const f32x4*)(p + 64 * K3_RS + 16)) +
                 (*(const f32x4*)(p + 128 * K3_RS + 16) + *(const f32x4*)(p + 192 * K3_RS + 16));
      u32x4 z;
#pragma unroll
      for (int j = 0; j < 4; ++j) z[j] = gelu_pk(zv[i][j]);
      u32x4 o;
      o[0] = pk2(lo_f(z[0]) * rbf(rbf(v0[0]) + bb[i]), hi_f(z[0]) * rbf(rbf(v0[1]) + bb[i]));
      o[1] = pk2(lo_f(z[1]) * rbf(rbf(v0[2]) + bb[i]), hi_f(z[1]) * rbf(rbf(v0[3]) + bb[i]));
      o[2] = pk2(lo_f(z[2]) * rbf(rbf(v1[0]) + bb[i]), hi_f(z[2]) * rbf(rbf(v1[1]) + bb[i]));
      o[3] = pk2(lo_f(z[3]) * rbf(rbf(v1[2]) + bb[i]), hi_f(z[3]) * rbf(rbf(v1[3]) + bb[i]));
      const int k = 448 + f0 + c * 8, tt = t0 + t;
      *(u32x4*)(oh + ((size_t)((tt >> 4) * 42 + (k >> 5)) * 64 + ((k >> 3) & 3) * 16 + (tt & 15)) * 8) = o;
    }
  }
  STAMP(2, 31);
}

__global__ __launch_bounds__(384) void k4_kernel(const u16* __restrict__ x, const char* __restrict__ ws,
                                                  u16* __restrict__ out) {
  __shared__ __attribute__((aligned(16))) char sm[6 * 32 * K3_RS];
  const int tid = threadIdx.x, lane = tid & 63, wave = __builtin_amdgcn_readfirstlane(tid >> 6);
  const int b = blockIdx.x;
  const int xcd = b & 7, idx = b >> 3;
  const int mt_ = xcd * 8 + (idx & 7), nt_ = idx >> 3;
  const int t0 = mt_ * 32, n0 = nt_ * 112;
  const int g = lane >> 4, r16 = lane & 15;
  STAMP(3, 0);
  const u16* ap = (const u16*)(ws + WS_OH) + ((size_t)(mt_ * 2 * 42 + wave * 7) * 64 + lane) * 8;
  const u16* bp = (const u16*)(ws + WS_WT) + ((size_t)(nt_ * 7 * 42 + wave * 7) * 64 + lane) * 8;
  bf16x8 a0[2], b0[7], a1[2], b1[7], a2[2], b2[7], a3[2], b3[7];
#define K4_LOAD(AF, BF, KS)                                                                     \
  do {                                                                                          \
    _Pragma("unroll") for (int m = 0; m < 2; ++m) AF[m] = *(const bf16x8*)(ap + (m * 42 + (KS)) * 512); \
    _Pragma("unroll") for (int n = 0; n < 7; ++n) BF[n] = *(const bf16x8*)(bp + (n * 42 + (KS)) * 512); \
  } while (0)
  K4_LOAD(a0, b0, 0);
  K4_LOAD(a1, b1, 1);
  K4_LOAD(a2, b2, 2);
  K4_LOAD(a3, b3, 3);
  u32x4 xv[2];
#pragma unroll
  for (int i = 0; i < 2; ++i) {
    int id = tid + 384 * i;
    if (id >= 448) id = tid;
    xv[i] = *(const u32x4*)(x + (size_t)(t0 + id / 14) * D_ + n0 + (id % 14) * 8);
  }
  SCHED();
  f32x4 acc[7][2];
#pragma unroll
  for (int n = 0; n < 7; ++n) { acc[n][0] = f32x4{0.f, 0.f, 0.f, 0.f}; acc[n][1] = f32x4{0.f, 0.f, 0.f, 0.f}; }
#define K4_MMA(AF, BF)                                                                          \
  do {                                                                                          \
    _Pragma("unroll") for (int n = 0; n < 7; ++n) {                                             \
      acc[n][0] = mfma16(BF[n], AF[0], acc[n][0]);                                              \
      acc[n][1] = mfma16(BF[n], AF[1], acc[n][1]);                                              \
    }                                                                                           \
    SCHED();                                                                                    \
  } while (0)
  K4_MMA(a0, b0); K4_LOAD(a0, b0, 4); SCHED();
  K4_MMA(a1, b1); K4_LOAD(a1, b1, 5); SCHED();
  K4_MMA(a2, b2); K4_LOAD(a2, b2, 6); SCHED();
  K4_MMA(a3, b3);
  K4_MMA(a0, b0);
  K4_MMA(a1, b1);
  K4_MMA(a2, b2);
  STAMP(3, 1);
  char* rd = sm + wave * (32 * K3_RS) + r16 * K3_RS + 16 * g;
#pragma unroll
  for (int n = 0; n < 7; ++n)
#pragma unroll
    for (int m = 0; m < 2; ++m) *(f32x4*)(rd + 16 * m * K3_RS + 64 * n) = acc[n][m];
  __syncthreads();
#pragma unroll
  for (int i = 0; i < 2; ++i) {
    const int id = tid + 384 * i;
    if (id < 448) {
      const int et = id / 14, ec = id % 14;
      const char* p = sm + et * K3_RS + ec * 32;
      f32x4 v0 = {0.f, 0.f, 0.f, 0.f}, v1 = {0.f, 0.f, 0.f, 0.f};
#pragma unroll
      for (int w = 0; w < 6; ++w) {
        v0 += *(const f32x4*)(p + w * 32 * K3_RS);
        v1 += *(const f32x4*)(p + w * 32 * K3_RS + 16);
      }
      u32x4 o;
      o[0] = pk2(lo_f(xv[i][0]) + v0[0], hi_f(xv[i][0]) + v0[1]);
      o[1] = pk2(lo_f(xv[i][1]) + v0[2], hi_f(xv[i][1]) + v0[3]);
      o[2] = pk2(lo_f(xv[i][2]) + v1[0], hi_f(xv[i][2]) + v1[1]);
      o[3] = pk2(lo_f(xv[i][3]) + v1[2], hi_f(xv[i][3]) + v1[3]);
      *(u32x4*)(out + (size_t)(t0 + et) * D_ + n0 + ec * 8) = o;
    }
  }
  STAMP(3, 31);
}

extern "C" void kernel_launch(void* const* d_in, const int* in_sizes, int n_in, void* d_out, int out_size,
                              void* d_ws, size_t ws_size, hipStream_t stream) {
  const u16* x = (const u16*)d_in[0];
  const u16* wqkv = (const u16*)d_in[1];
  const u16* wphi = (const u16*)d_in[2];
  const u16* wo = (const u16*)d_in[3];
  const u16* win = (const u16*)d_in[5];
  const u16* wsp = (const u16*)d_in[6];
  const u16* bsp = (const u16*)d_in[7];
  const u16* wout = (const u16*)d_in[8];
  char* ws = (char*)d_ws;
  k1_kernel<<<256, 640, 0, stream>>>(x, wqkv, wo, win, wout, wsp, ws);
  k2_kernel<<<256, 512, 0, stream>>>(wphi, ws);
  k3_kernel<<<256, 768, 0, stream>>>(wsp, bsp, wphi, ws);
  k4_kernel<<<256, 384, 0, stream>>>(x, ws, (u16*)d_out);
}
```

```cpp
#include <hip/hip_runtime.h>
#include <hip/hip_bf16.h>
#include <stdint.h>

typedef __attribute__((ext_vector_type(8))) short bf16x8;
typedef __attribute__((ext_vector_type(4))) short s16x4;
typedef __attribute__((ext_vector_type(4))) float f32x4;
typedef __attribute__((ext_vector_type(2))) float f32x2;
typedef __attribute__((ext_vector_type(4))) unsigned u32x4;
typedef __attribute__((ext_vector_type(2))) unsigned u32x2;
typedef __attribute__((ext_vector_type(2))) __bf16 bf2_t;
typedef unsigned short u16;
#define LDS_AS __attribute__((address_space(3)))

constexpr int S_ = 2048, D_ = 448, NQKV = 1344, NIN = 1792, FH = 896, KO = 1344;

__device__ __forceinline__ unsigned pk2(float a, float b) {
  f32x2 v = {a, b};
  return __builtin_bit_cast(unsigned, __builtin_convertvector(v, bf2_t));
}
__device__ __forceinline__ float lo_f(unsigned u) { return __uint_as_float(u << 16); }
__device__ __forceinline__ float hi_f(unsigned u) { return __uint_as_float(u & 0xffff0000u); }
__device__ __forceinline__ float rbf(float a) { return lo_f(pk2(a, 0.f)); }
__device__ __forceinline__ float bf2f(u16 v) { return __uint_as_float(((unsigned)v) << 16); }
__device__ __forceinline__ float dot2(unsigned a, unsigned b, float c) {
  return __builtin_amdgcn_fdot2_f32_bf16(__builtin_bit_cast(bf2_t, a), __builtin_bit_cast(bf2_t, b), c, false);
}

__device__ __forceinline__ float x16_max(float v) {
  auto r = __builtin_amdgcn_permlane16_swap(__float_as_uint(v), __float_as_uint(v), false, false);
  return fmaxf(__uint_as_float(r[0]), __uint_as_float(r[1]));
}
__device__ __forceinline__ float x32_max(float v) {
  auto r = __builtin_amdgcn_permlane32_swap(__float_as_uint(v), __float_as_uint(v), false, false);
  return fmaxf(__uint_as_float(r[0]), __uint_as_float(r[1]));
}
__device__ __forceinline__ float x16_min(float v) {
  auto r = __builtin_amdgcn_permlane16_swap(__float_as_uint(v), __float_as_uint(v), false, false);
  return fminf(__uint_as_float(r[0]), __uint_as_float(r[1]));
}
__device__ __forceinline__ float x32_min(float v) {
  auto r = __builtin_amdgcn_permlane32_swap(__float_as_uint(v), __float_as_uint(v), false, false);
  return fminf(__uint_as_float(r[0]), __uint_as_float(r[1]));
}
__device__ __forceinline__ float x16_add(float v) {
  auto r = __builtin_amdgcn_permlane16_swap(__float_as_uint(v), __float_as_uint(v), false, false);
  return __uint_as_float(r[0]) + __uint_as_float(r[1]);
}
__device__ __forceinline__ float x32_add(float v) {
  auto r = __builtin_amdgcn_permlane32_swap(__float_as_uint(v), __float_as_uint(v), false, false);
  return __uint_as_float(r[0]) + __uint_as_float(r[1]);
}
__device__ __forceinline__ float red8_add(float v) {
  v += __uint_as_float(__builtin_amdgcn_mov_dpp(__float_as_uint(v), 0xB1, 0xf, 0xf, true));
  v += __uint_as_float(__builtin_amdgcn_mov_dpp(__float_as_uint(v), 0x4E, 0xf, 0xf, true));
  v += __uint_as_float(__builtin_amdgcn_mov_dpp(__float_as_uint(v), 0x141, 0xf, 0xf, true));
  return v;
}
__device__ __forceinline__ f32x4 mfma16(bf16x8 a, bf16x8 b, f32x4 c) {
  return __builtin_amdgcn_mfma_f32_16x16x32_bf16(a, b, c, 0, 0, 0);
}
__device__ __forceinline__ unsigned gelu_pk(unsigned p) {
  const f32x2 u = {lo_f(p), hi_f(p)};
  const f32x2 c1 = {-2.3022081986f, -2.3022081986f};
  const f32x2 c2 = {-0.1029432397f, -0.1029432397f};
  f32x2 t = u * u;
  f32x2 a = u * (t * c2 + c1);
  f32x2 d = {1.f + __builtin_amdgcn_exp2f(a[0]), 1.f + __builtin_amdgcn_exp2f(a[1])};
  f32x2 r = {__builtin_amdgcn_rcpf(d[0]), __builtin_amdgcn_rcpf(d[1])};
  f32x2 o = u * r;
  return pk2(o[0], o[1]);
}
#define SCHED() __builtin_amdgcn_sched_barrier(0)
#define BAR() do { SCHED(); asm volatile("" ::: "memory"); __builtin_amdgcn_s_waitcnt(0xC07F); __builtin_amdgcn_s_barrier(); asm volatile("" ::: "memory"); SCHED(); } while (0)
__device__ __forceinline__ void glds16(const void* g, char* l) {
  __builtin_amdgcn_global_load_lds((const unsigned*)g, (LDS_AS unsigned*)l, 16, 0, 0);
}
__device__ __forceinline__ bf16x8 tr_frag(const char* p0, const char* p1) {
  s16x4 a = __builtin_amdgcn_ds_read_tr16_b64_v4i16((LDS_AS s16x4*)p0);
  s16x4 c = __builtin_amdgcn_ds_read_tr16_b64_v4i16((LDS_AS s16x4*)p1);
  return __builtin_shufflevector(a, c, 0, 1, 2, 3, 4, 5, 6, 7);
}

constexpr int YLD = 3136;
constexpr size_t WS_Y = 0;
constexpr size_t WS_Z2N = WS_Y + (size_t)S_ * YLD * 2 + 256;
constexpr size_t WS_OH = WS_Z2N + (size_t)S_ * 896 * 2;
constexpr size_t WS_WT = WS_OH + (size_t)S_ * KO * 2;
constexpr size_t WS_KVP = WS_WT + (size_t)D_ * KO * 2;
constexpr size_t WS_PHQ = WS_KVP + (size_t)8 * 16 * 56 * 32 * 4;
constexpr size_t WS_END = WS_PHQ + (size_t)16 * 128 * 2048;

#define STAMP(K, I) do { } while (0)
#define SGB(mask, n) __builtin_amdgcn_sched_group_barrier(mask, n, 0)

__device__ __forceinline__ void phi_map(bf16x8 qf, bf16x8 wp0, bf16x8 wp1, int g, unsigned (&pp)[2][2],
                                        unsigned (&pm)[2][2]) {
  f32x4 z = {0.f, 0.f, 0.f, 0.f};
  f32x4 p0 = mfma16(wp0, qf, z);
  f32x4 p1 = mfma16(wp1, qf, z);
  float v[8];
  {
    unsigned a = pk2(p0[0], p0[1]), b = pk2(p0[2], p0[3]), c = pk2(p1[0], p1[1]), d = pk2(p1[2], p1[3]);
    v[0] = lo_f(a); v[1] = hi_f(a); v[2] = lo_f(b); v[3] = hi_f(b);
    v[4] = lo_f(c); v[5] = hi_f(c); v[6] = lo_f(d); v[7] = hi_f(d);
  }
  const bool valid1 = (g != 3);
  if (!valid1) { v[4] = v[0]; v[5] = v[0]; v[6] = v[0]; v[7] = v[0]; }
  float mx = fmaxf(fmaxf(fmaxf(v[0], v[1]), fmaxf(v[2], v[3])), fmaxf(fmaxf(v[4], v[5]), fmaxf(v[6], v[7])));
  float mn = fminf(fminf(fminf(v[0], v[1]), fminf(v[2], v[3])), fminf(fminf(v[4], v[5]), fminf(v[6], v[7])));
  mx = x32_max(x16_max(mx));
  mn = x32_min(x16_min(mn));
  const float L2E = 1.4426950408889634f;
  const float mxl = mx * L2E, mnl = mn * L2E;
  float ep[8], em[8];
  float sp = 0.f, sm = 0.f;
#pragma unroll
  for (int j = 0; j < 8; ++j) {
    ep[j] = __builtin_amdgcn_exp2f(__builtin_fmaf(v[j], L2E, -mxl));
    em[j] = __builtin_amdgcn_exp2f(__builtin_fmaf(v[j], -L2E, mnl));
    if (j >= 4) { ep[j] = valid1 ? ep[j] : 0.f; em[j] = valid1 ? em[j] : 0.f; }
    sp += ep[j];
    sm += em[j];
  }
  sp = x32_add(x16_add(sp));
  sm = x32_add(x16_add(sm));
  const float rp = __builtin_amdgcn_rcpf(sp), rm = __builtin_amdgcn_rcpf(sm);
  pp[0][0] = pk2(ep[0] * rp, ep[1] * rp); pp[0][1] = pk2(ep[2] * rp, ep[3] * rp);
  pp[1][0] = pk2(ep[4] * rp, ep[5] * rp); pp[1][1] = pk2(ep[6] * rp, ep[7] * rp);
  pm[0][0] = pk2(em[0] * rm, em[1] * rm); pm[0][1] = pk2(em[2] * rm, em[3] * rm);
  pm[1][0] = pk2(em[4] * rm, em[5] * rm); pm[1][1] = pk2(em[6] * rm, em[7] * rm);
}

constexpr int K1_XB = 128 * 896;
constexpr int K1_WST = 32 * 448;
constexpr int K1_LDS = K1_XB + 3 * K1_WST;

__global__ __launch_bounds__(640) void k1_kernel(const u16* __restrict__ x, const u16* __restrict__ wqkv,
                                                  const u16* __restrict__ wo, const u16* __restrict__ win,
                                                  const u16* __restrict__ wout, const u16* __restrict__ wsp,
                                                  char* __restrict__ ws) {
  __shared__ __attribute__((aligned(16))) char sm[K1_LDS];
  const int tid = threadIdx.x, lane = tid & 63, wave = __builtin_amdgcn_readfirstlane(tid >> 6);
  const int b = blockIdx.x;
  STAMP(0, 0);
  if (b >= 224) {
    const int kb = b - 224;
    const int k0 = kb * 64;
    if (kb < 21) {
    u16* T = (u16*)sm;
    constexpr int TS = 456;
    u32x4 v[6];
#pragma unroll
    for (int j = 0; j < 6; ++j) {
      int i = tid + 640 * j;
      if (i >= 64 * 56) i = tid;
      const int r = i / 56, c = i % 56;
      const int k = k0 + r;
      const u16* src = (k < 448) ? (wo + (size_t)k * 448) : (wout + (size_t)(k - 448) * 448);
      v[j] = *(const u32x4*)(src + c * 8);
    }
#pragma unroll
    for (int j = 0; j < 6; ++j) {
      const int i = tid + 640 * j;
      if (i < 64 * 56) *(u32x4*)(T + (i / 56) * TS + (i % 56) * 8) = v[j];
    }
    __syncthreads();
    u16* wT = (u16*)(ws + WS_WT);
    for (int i = tid; i < 448 * 8; i += 640) {
      const int n = i >> 3, c = i & 7;
      u16 t[8];
#pragma unroll
      for (int j = 0; j < 8; ++j) t[j] = T[(c * 8 + j) * TS + n];
      u32x4 o;
#pragma unroll
      for (int j = 0; j < 4; ++j) o[j] = (unsigned)t[2 * j] | ((unsigned)t[2 * j + 1] << 16);
      *(u32x4*)(wT + ((size_t)((n >> 4) * 42 + (k0 >> 5) + (c >> 2)) * 64 + (c & 3) * 16 + (n & 15)) * 8) = o;
    }
    }
    STAMP(0, 1);
    {
      const char* p = (const char*)wsp + (size_t)kb * 262144 + tid * 16;
      unsigned a = 0;
#pragma unroll
      for (int i = 0; i < 25; ++i) {
        u32x4 v = *(const u32x4*)(p + i * 10240);
        a |= v[0] & v[1] & v[2] & v[3];
      }
      asm volatile("" ::"v"(a));
    }
    STAMP(0, 31);
    return;
  }

  const int xcd = b & 7, idx = b >> 3;
  const int cb = (xcd & 1) * 7 + idx % 7;
  const int rt = (xcd >> 1) * 4 + idx / 7;
  const int row0 = rt * 128;
  char* wring = sm + K1_XB;
  const int g = lane >> 4, r16 = lane & 15;
  const int wm = (wave >> 1) & 3, wn = wave & 1;
  constexpr int RST = 464;

  if (wave >= 8) {
    const int lw = wave - 8;
    const u16* W = (cb < 6) ? (wqkv + 224 * cb) : (win + 224 * (cb - 6));
    const int ldw = (cb < 6) ? NQKV : NIN;
    const u16* src[7];
#pragma unroll
    for (int j = 0; j < 7; ++j) {
      const int o = (lw + 2 * j) * 1024 + lane * 16;
      const int k = o / 448, cp = (o % 448) >> 4;
      const int G = (cp >> 1) ^ ((k >> 3) & 1);
      const int c = 2 * G + (cp & 1);
      src[j] = W + (size_t)k * ldw + 8 * c;
    }
    const size_t wstep = (size_t)32 * ldw;
    char* dst0 = wring + lw * 1024;
#pragma unroll
    for (int s = 0; s < 3; ++s)
#pragma unroll
      for (int j = 0; j < 7; ++j) glds16(src[j] + s * wstep, dst0 + s * K1_WST + j * 2048);
    asm volatile("s_waitcnt vmcnt(14)" ::: "memory");
    __builtin_amdgcn_s_barrier();
    int nb = 0;
    for (int t = 0; t < 14; ++t) {
      if (t + 2 < 14) asm volatile("s_waitcnt vmcnt(7)" ::: "memory");
      else asm volatile("s_waitcnt vmcnt(0)" ::: "memory");
      __builtin_amdgcn_s_barrier();
      if (t + 3 < 14) {
        char* dst = dst0 + nb * K1_WST;
#pragma unroll
        for (int j = 0; j < 7; ++j) glds16(src[j] + (t + 3) * wstep, dst + j * 2048);
        nb = (nb == 2) ? 0 : nb + 1;
      }
    }
    __builtin_amdgcn_s_barrier();
  } else {
    u32x4 xv[2][7];
    const int rr = lane >> 3, cc = lane & 7;
#pragma unroll
    for (int p = 0; p < 2; ++p) {
      const u16* xp = x + (size_t)(row0 + 16 * wave + 8 * p + rr) * D_ + cc * 8;
#pragma unroll
      for (int i = 0; i < 7; ++i) xv[p][i] = *(const u32x4*)(xp + 64 * i);
    }
#pragma unroll
    for (int p = 0; p < 2; ++p) {
      float s0 = 0.f, s1 = 0.f;
#pragma unroll
      for (int i = 0; i < 7; ++i) {
        s0 = dot2(xv[p][i][0], 0x3F803F80u, s0); s1 = dot2(xv[p][i][1], 0x3F803F80u, s1);
        s0 = dot2(xv[p][i][2], 0x3F803F80u, s0); s1 = dot2(xv[p][i][3], 0x3F803F80u, s1);
      }
      const float mean = red8_add(s0 + s1) * (1.f / 448.f);
      const float mb = rbf(mean);
      float q0 = 0.f, q1 = 0.f;
#pragma unroll
      for (int i = 0; i < 7; ++i)
#pragma unroll
        for (int j = 0; j < 4; ++j) {
          const unsigned d = pk2(lo_f(xv[p][i][j]) - mb, hi_f(xv[p][i][j]) - mb);
          xv[p][i][j] = d;
          if (j & 1) q1 = dot2(d, d, q1); else q0 = dot2(d, d, q0);
        }
      const float q = red8_add(q0 + q1);
      const float dm = mean - mb;
      const float var = fmaxf(q * (1.f / 448.f) - dm * dm, 0.f);
      const float sd = rbf(sqrtf(rbf(rbf(var) + 1e-5f)));
      const float inv = 1.f / sd;
      const int row = 16 * wave + 8 * p + rr;
      char* dst = sm + row * 896 + ((cc ^ ((row >> 1) & 7)) << 4);
#pragma unroll
      for (int i = 0; i < 7; ++i) {
        u32x4 o;
#pragma unroll
        for (int j = 0; j < 4; ++j) o[j] = pk2(lo_f(xv[p][i][j]) * inv, hi_f(xv[p][i][j]) * inv);
        *(u32x4*)(dst + 128 * i) = o;
      }
    }
    STAMP(0, 1);
    f32x4 acc[7][2];
#pragma unroll
    for (int nt = 0; nt < 7; ++nt) { acc[nt][0] = f32x4{0.f, 0.f, 0.f, 0.f}; acc[nt][1] = f32x4{0.f, 0.f, 0.f, 0.f}; }
    const int sw = (r16 >> 1) & 7;
    const char* xbase = sm + (32 * wm + r16) * 896;
    const int xoE = ((g ^ sw) << 4), xoO = (((4 | g) ^ sw) << 4);
    const int wrow = (8 * g + ((lane >> 2) & 3)) * 448 + (lane & 3) * 8 + 7 * wn * 32;
    const int wE = wrow + ((wn & 1) ? -32 * (g & 1) : 32 * (g & 1));
    const int wO = wrow + ((wn & 1) ? 32 * (g & 1) : -32 * (g & 1));
    bf16x8 xa[2], wa[7], xb[2], wb[7];
#define K1_READ(XF, WF, T, BUF)                                                              \
  do {                                                                                       \
    const char* xp_ = xbase + ((T) >> 1) * 128 + (((T)&1) ? xoO : xoE);                      \
    const char* wb_ = wring + (BUF)*K1_WST;                                                  \
    XF[0] = *(const bf16x8*)(xp_);                                                           \
    XF[1] = *(const bf16x8*)(xp_ + 16 * 896);                                                \
    _Pragma("unroll") for (int nt = 0; nt < 7; ++nt) {                                       \
      const char* p_ = wb_ + ((nt & 1) ? wO : wE) + nt * 32;                                 \
      WF[nt] = tr_frag(p_, p_ + 4 * 448);                                                    \
    }                                                                                        \
  } while (0)
#define K1_MMA(XF, WF)                                                                       \
  do {                                                                                       \
    _Pragma("unroll") for (int nt = 0; nt < 7; ++nt) acc[nt][0] = mfma16(WF[nt], XF[0], acc[nt][0]); \
    _Pragma("unroll") for (int nt = 0; nt < 7; ++nt) acc[nt][1] = mfma16(WF[nt], XF[1], acc[nt][1]); \
  } while (0)
#define K1_MIX()                                                                             \
  do {                                                                                       \
    SGB(0x100, 2);                                                                           \
    _Pragma("unroll") for (int i_ = 0; i_ < 14; ++i_) { SGB(0x008, 1); SGB(0x100, 1); }      \
  } while (0)
    BAR();
    STAMP(0, 2);
    K1_READ(xa, wa, 0, 0);
    int rb = 1;
#pragma unroll 1
    for (int t = 0; t < 12; t += 2) {
      BAR();
      STAMP(0, 3 + t);
      K1_READ(xb, wb, t + 1, rb);
      rb = (rb == 2) ? 0 : rb + 1;
      K1_MMA(xa, wa);
      K1_MIX();
      BAR();
      STAMP(0, 4 + t);
      K1_READ(xa, wa, t + 2, rb);
      rb = (rb == 2) ? 0 : rb + 1;
      K1_MMA(xb, wb);
      K1_MIX();
    }
    BAR();
    K1_READ(xb, wb, 13, 1);
    K1_MMA(xa, wa);
    K1_MIX();
    BAR();
    K1_MMA(xb, wb);
    BAR();
    STAMP(0, 17);
#pragma unroll
    for (int mt = 0; mt < 2; ++mt) {
      char* dst = sm + (32 * wm + 16 * mt + r16) * RST + (112 * wn + 4 * g) * 2;
#pragma unroll
      for (int nt = 0; nt < 7; ++nt)
        *(u32x2*)(dst + 32 * nt) = u32x2{pk2(acc[nt][mt][0], acc[nt][mt][1]), pk2(acc[nt][mt][2], acc[nt][mt][3])};
    }
  }
  __syncthreads();
  u16* dstb = (u16*)(ws + WS_Y) + (size_t)row0 * YLD + 224 * cb;
#pragma unroll
  for (int i = 0; i < 6; ++i) {
    const int id = tid + 640 * i;
    if (id < 128 * 28) {
      const int s = id / 28, c = id % 28;
      *(u32x4*)(dstb + (size_t)s * YLD + c * 8) = *(const u32x4*)(sm + s * RST + c * 16);
    }
  }
  STAMP(0, 31);
}

__device__ __forceinline__ float red64_add(float v) {
  v = red8_add(v);
  v += __uint_as_float(__builtin_amdgcn_mov_dpp(__float_as_uint(v), 0x140, 0xf, 0xf, true));
  return x32_add(x16_add(v));
}

constexpr int K2_PW = 64 * 80 + 32 * 64;
__global__ __launch_bounds__(512) void k2_kernel(const u16* __restrict__ wphi, char* __restrict__ ws) {
  __shared__ __attribute__((aligned(16))) char sm[8 * 56 * 32 * 4 + 2048];
  const int tid = threadIdx.x, lane = tid & 63, wave = __builtin_amdgcn_readfirstlane(tid >> 6);
  const int b = blockIdx.x;
  const u16* Y = (const u16*)(ws + WS_Y);
  STAMP(1, 0);
  if (b < 128) {
    const int h = b & 15, rg = b >> 4;
    const int g = lane >> 4, r16 = lane & 15;
    const int rowb = rg * 256 + wave * 32;
    u32x2 kv_[2][2];
#pragma unroll
    for (int st = 0; st < 2; ++st) {
      const u16* kp = Y + (size_t)(rowb + st * 16 + r16) * YLD + 448 + h * 28 + 8 * g;
      kv_[st][0] = *(const u32x2*)(kp);
      kv_[st][1] = *(const u32x2*)(kp + 4);
    }
    u32x2 vv[4];
#pragma unroll
    for (int i = 0; i < 4; ++i) {
      const int row = (lane >> 3) + 8 * i, pc = lane & 7;
      vv[i] = *(const u32x2*)(Y + (size_t)(rowb + row) * YLD + 896 + h * 28 + 4 * (pc < 7 ? pc : 0));
      if (pc == 7) vv[i] = u32x2{0x00003F80u, 0u};
    }
    u32x4 wraw = {0u, 0u, 0u, 0u};
    if (tid < 98) wraw = *(const u32x4*)(wphi + (size_t)h * 784 + tid * 8);
    char* WP = sm + 8 * 56 * 32 * 4;
    if (tid < 98) *(u32x4*)(WP + tid * 16) = wraw;
    char* PK = sm + wave * K2_PW;
    char* VS = PK + 64 * 80;
    __syncthreads();
    STAMP(1, 1);
    const char* wp = WP + (8 * g + ((lane >> 2) & 3)) * 56 + (lane & 3) * 8;
    bf16x8 wp0 = tr_frag(wp, wp + 4 * 56);
    bf16x8 wp1 = tr_frag(wp + 32, wp + 4 * 56 + 32);
    if (g == 3) {
#pragma unroll
      for (int j = 4; j < 8; ++j) { wp0[j] = 0; wp1[j] = 0; }
    }
    f32x4 kacc[4][2];
#pragma unroll
    for (int a = 0; a < 4; ++a) { kacc[a][0] = f32x4{0.f, 0.f, 0.f, 0.f}; kacc[a][1] = f32x4{0.f, 0.f, 0.f, 0.f}; }
#pragma unroll
    for (int pr = 0; pr < 1; ++pr) {
#pragma unroll
      for (int i = 0; i < 4; ++i) {
        const int row = (lane >> 3) + 8 * i;
        const int slot = 2 * (row & 15) + (row >> 4);
        *(u32x2*)(VS + slot * 64 + (lane & 7) * 8) = vv[4 * pr + i];
      }
      unsigned pp[2][2][2], pm[2][2][2];
#pragma unroll
      for (int mt = 0; mt < 2; ++mt) {
        u32x4 q4 = {kv_[2 * pr + mt][0][0], kv_[2 * pr + mt][0][1], kv_[2 * pr + mt][1][0], kv_[2 * pr + mt][1][1]};
        if (g == 3) { q4[2] = 0u; q4[3] = 0u; }
        phi_map(__builtin_bit_cast(bf16x8, q4), wp0, wp1, g, pp[mt], pm[mt]);
      }
#pragma unroll
      for (int ft = 0; ft < 2; ++ft) {
        if (ft == 0 || g != 3) {
          char* d0 = PK + (16 * ft + 4 * g) * 80 + r16 * 4;
#pragma unroll
          for (int j = 0; j < 2; ++j) {
            *(unsigned*)(d0 + (2 * j) * 80) = (pp[0][ft][j] & 0xffffu) | (pp[1][ft][j] << 16);
            *(unsigned*)(d0 + (2 * j + 1) * 80) = (pp[0][ft][j] >> 16) | (pp[1][ft][j] & 0xffff0000u);
            *(unsigned*)(d0 + (28 + 2 * j) * 80) = (pm[0][ft][j] & 0xffffu) | (pm[1][ft][j] << 16);
            *(unsigned*)(d0 + (28 + 2 * j + 1) * 80) = (pm[0][ft][j] >> 16) | (pm[1][ft][j] & 0xffff0000u);
          }
        }
      }
      __builtin_amdgcn_s_waitcnt(0xC07F);
      __builtin_amdgcn_wave_barrier();
      bf16x8 bfr[2];
      {
        const char* vp = VS + (8 * g + ((lane >> 2) & 3)) * 64 + (lane & 3) * 8;
        bfr[0] = tr_frag(vp, vp + 4 * 64);
        bfr[1] = tr_frag(vp + 32, vp + 4 * 64 + 32);
      }
#pragma unroll
      for (int a = 0; a < 4; ++a) {
        bf16x8 af = *(const bf16x8*)(PK + (16 * a + r16) * 80 + g * 16);
        kacc[a][0] = mfma16(af, bfr[0], kacc[a][0]);
        kacc[a][1] = mfma16(af, bfr[1], kacc[a][1]);
      }
      __builtin_amdgcn_s_waitcnt(0xC07F);
      __builtin_amdgcn_wave_barrier();
    }
    STAMP(1, 2);
    __syncthreads();
    float* red = (float*)sm + wave * (56 * 32);
#pragma unroll
    for (int a = 0; a < 4; ++a)
#pragma unroll
      for (int c = 0; c < 2; ++c)
#pragma unroll
        for (int j = 0; j < 4; ++j) {
          const int f = 16 * a + 4 * g + j;
          if (f < 56) red[f * 32 + 16 * c + r16] = kacc[a][c][j];
        }
    __syncthreads();
    if (tid < 448) {
      const f32x4* rp = (const f32x4*)sm + tid;
      f32x4 s = (rp[0] + rp[448]) + (rp[2 * 448] + rp[3 * 448]);
      s += (rp[4 * 448] + rp[5 * 448]) + (rp[6 * 448] + rp[7 * 448]);
      *((f32x4*)(ws + WS_KVP) + (size_t)(rg * 16 + h) * 448 + tid) = s;
    }
  } else if (b >= 256) {
    const int h = b & 15, rg = (b - 256) >> 4;
    const int g = lane >> 4, r16 = lane & 15;
    u32x2 qv[2][2];
#pragma unroll
    for (int st = 0; st < 2; ++st) {
      const u16* qp = Y + (size_t)(rg * 256 + wave * 32 + st * 16 + r16) * YLD + h * 28 + 8 * g;
      qv[st][0] = *(const u32x2*)(qp);
      qv[st][1] = *(const u32x2*)(qp + 4);
    }
    u32x4 wraw = {0u, 0u, 0u, 0u};
    if (tid < 98) wraw = *(const u32x4*)(wphi + (size_t)h * 784 + tid * 8);
    if (tid < 98) *(u32x4*)(sm + tid * 16) = wraw;
    __syncthreads();
    const char* wp = sm + (8 * g + ((lane >> 2) & 3)) * 56 + (lane & 3) * 8;
    bf16x8 wp0 = tr_frag(wp, wp + 4 * 56);
    bf16x8 wp1 = tr_frag(wp + 32, wp + 4 * 56 + 32);
    if (g == 3) {
#pragma unroll
      for (int j = 4; j < 8; ++j) { wp0[j] = 0; wp1[j] = 0; }
    }
#pragma unroll
    for (int st = 0; st < 2; ++st) {
      u32x4 q4 = {qv[st][0][0], qv[st][0][1], qv[st][1][0], qv[st][1][1]};
      if (g == 3) { q4[2] = 0u; q4[3] = 0u; }
      unsigned pp[2][2], pm[2][2];
      phi_map(__builtin_bit_cast(bf16x8, q4), wp0, wp1, g, pp, pm);
      u32x4* dst = (u32x4*)(ws + WS_PHQ) + ((size_t)(h * 128 + rg * 16 + wave * 2 + st) * 2) * 64 + lane;
      dst[0] = u32x4{pp[0][0], pp[0][1], pp[1][0], pp[1][1]};
      dst[64] = u32x4{pm[0][0], pm[0][1], pm[1][0], pm[1][1]};
    }
  } else {
    const int row0 = (b - 128) * 16 + wave * 2;
    u16* z2n = (u16*)(ws + WS_Z2N);
    const int le = (lane < 56) ? lane : 0;
    u32x4 v[2][2];
#pragma unroll
    for (int r = 0; r < 2; ++r)
#pragma unroll
      for (int c = 0; c < 2; ++c) v[r][c] = *(const u32x4*)(Y + (size_t)(row0 + r) * YLD + 2240 + (le + 56 * c) * 8);
#pragma unroll
    for (int r = 0; r < 2; ++r) {
      float s = 0.f;
#pragma unroll
      for (int c = 0; c < 2; ++c)
#pragma unroll
        for (int j = 0; j < 4; ++j) {
          v[r][c][j] = gelu_pk(v[r][c][j]);
          s = dot2(v[r][c][j], 0x3F803F80u, s);
        }
      if (lane >= 56) s = 0.f;
      const float mean = red64_add(s) * (1.f / 896.f);
      const float mb = rbf(mean);
      float q = 0.f;
#pragma unroll
      for (int c = 0; c < 2; ++c)
#pragma unroll
        for (int j = 0; j < 4; ++j) {
          const float a = lo_f(v[r][c][j]) - mean, e = hi_f(v[r][c][j]) - mean;
          q += a * a + e * e;
          v[r][c][j] = pk2(lo_f(v[r][c][j]) - mb, hi_f(v[r][c][j]) - mb);
        }
      if (lane >= 56) q = 0.f;
      const float var = red64_add(q) * (1.f / 896.f);
      const float sd = rbf(sqrtf(rbf(rbf(var) + 1e-5f)));
      const float inv = 1.f / sd;
      if (lane < 56) {
#pragma unroll
        for (int c = 0; c < 2; ++c) {
          u32x4 o;
#pragma unroll
          for (int j = 0; j < 4; ++j) o[j] = pk2(lo_f(v[r][c][j]) * inv, hi_f(v[r][c][j]) * inv);
          *(u32x4*)(sm + (wave * 2 + r) * 1808 + (lane + 56 * c) * 16) = o;
        }
      }
    }
    __syncthreads();
    {
      const int rb0 = (b - 128) * 16;
      const int kb = rb0 >> 5, half = (rb0 >> 4) & 1;
      const int g = lane >> 4;
#pragma unroll
      for (int i = 0; i < 4; ++i) {
        const int u = wave + 8 * i;
        if (u < 28) {
          const int T = 2 * u + (g >> 1), gp = g & 1;
          const char* p = sm + (8 * gp + ((lane >> 2) & 3)) * 1808 + T * 32 + (lane & 3) * 8;
          bf16x8 fr = tr_frag(p, p + 4 * 1808);
          const int pn = T / 7, n = T % 7;
          *(bf16x8*)(z2n + ((size_t)((pn * 64 + kb) * 7 + n) * 64 + (2 * half + gp) * 16 + (lane & 15)) * 8) = fr;
        }
      }
    }
  }
  STAMP(1, 31);
}

constexpr int K3_RS = 464;
constexpr int K3_STAGE = 64 * 256;
constexpr int K3_NB = 8;
constexpr int K3_AUX = K3_NB * K3_STAGE;
__device__ __forceinline__ void glds16_asm(const void* gsrc, unsigned lds_dst) {
  unsigned keep;
  asm volatile("s_mov_b32 %0, m0\n\ts_mov_b32 m0, %2\n\ts_nop 0\n\tglobal_load_lds_dwordx4 %1, off\n\ts_mov_b32 m0, %0"
               : "=&s"(keep) : "v"(gsrc), "s"(lds_dst) : "memory");
}
__global__ __launch_bounds__(512) void k3_kernel(const u16* __restrict__ wsp, const u16* __restrict__ bsp,
                                                  const u16* __restrict__ wphi, char* __restrict__ ws) {
  __shared__ __attribute__((aligned(16))) char sm[K3_NB * K3_STAGE + 32 * 144];
  const int tid = threadIdx.x, lane = tid & 63, wave = __builtin_amdgcn_readfirstlane(tid >> 6);
  const int b = blockIdx.x;
  const int xcd = b & 7, idx = b >> 3;
  const int mt_ = (xcd >> 1) * 8 + (idx & 7);
  const int nt_ = (xcd & 1) * 4 + (idx >> 3);
  const int t0 = mt_ * 64, f0 = nt_ * 112;
  const u16* z2n = (const u16*)(ws + WS_Z2N);
  const u16* Y = (const u16*)(ws + WS_Y);
  u16* oh = (u16*)(ws + WS_OH);
  const int g = lane >> 4, r16 = lane & 15;
  const int h = b & 15;
  u32x4 zv[2];
  float bb[2];
  STAMP(2, 0);
  const int kq = wave >> 1, wn = (wave & 1) ^ (wave >> 2);
  const u16* bp = z2n + ((size_t)(nt_ * 64 + kq) * 7 + 4 * wn) * 512 + lane * 8;
  const int arow = r16 * 256 + (((4 * kq + g) ^ r16) << 4);
  const u16* src0;
  const u16* src1;
  {
    const int row = 4 * wave + (lane >> 4);
    src0 = wsp + (size_t)(t0 + row) * S_ + (((lane & 15) ^ (row & 15)) << 3);
    src1 = src0 + (size_t)32 * S_;
  }
  const unsigned ldsb = __builtin_amdgcn_readfirstlane((unsigned)(size_t)(LDS_AS char*)sm) + wave * 1024;
#define K3_AISSUE(T)                                                                            \
  do {                                                                                          \
    glds16_asm(src0 + (T)*128, ldsb + ((T)&7) * K3_STAGE);                                      \
    glds16_asm(src1 + (T)*128, ldsb + ((T)&7) * K3_STAGE + 8192);                               \
  } while (0)
  f32x4 acc[4][4];
  bf16x8 aA[4], aB[4], b0[4], b1[4], b2[4], b3[4];
#define K3_BLOAD(BF, T, NT)                                                                     \
  do { _Pragma("unroll") for (int n = 0; n < NT; ++n) BF[n] = *(const bf16x8*)(bp + ((T)*28 + n) * 512); } while (0)
#define K3_AREAD(AF, T)                                                                         \
  do { _Pragma("unroll") for (int m = 0; m < 4; ++m) AF[m] = *(const bf16x8*)(sm + ((T)&7) * K3_STAGE + arow + m * 4096); } while (0)
#define K3_MMA(AF, BF, NT)                                                                      \
  do {                                                                                          \
    _Pragma("unroll") for (int n = 0; n < NT; ++n)                                              \
      _Pragma("unroll") for (int m = 0; m < 4; ++m) acc[n][m] = mfma16(BF[n], AF[m], acc[n][m]); \
  } while (0)
#define K3_STEP(T, AC, AN, BC, BL, NT)                                                          \
  do {                                                                                          \
    asm volatile("s_waitcnt vmcnt(12)" ::: "memory");                                           \
    BAR();                                                                                      \
    STAMP(2, 5 + (T));                                                                          \
    if ((T) + 8 < 16) K3_AISSUE((T) + 8);                                                       \
    if ((T) + 1 < 16) K3_AREAD(AN, (T) + 1);                                                    \
    if ((T) + 3 < 16) K3_BLOAD(BL, (T) + 3, NT);                                                \
    K3_MMA(AC, BC, NT);                                                                         \
    SGB(0x100, 4);                                                                              \
    _Pragma("unroll") for (int i_ = 0; i_ < NT; ++i_) { SGB(0x008, 2); SGB(0x020, 1); }         \
  } while (0)
#define K3_LOOP(NT) \
  do { \
    STAMP(2, 4); \
    K3_AREAD(aA, 0); \
    K3_STEP(0, aA, aB, b0, b3, NT); \
    K3_STEP(1, aB, aA, b1, b0, NT); \
    K3_STEP(2, aA, aB, b2, b1, NT); \
    K3_STEP(3, aB, aA, b3, b2, NT); \
    K3_STEP(4, aA, aB, b0, b3, NT); \
    K3_STEP(5, aB, aA, b1, b0, NT); \
    K3_STEP(6, aA, aB, b2, b1, NT); \
    K3_STEP(7, aB, aA, b3, b2, NT); \
    K3_STEP(8, aA, aB, b0, b3, NT); \
    K3_STEP(9, aB, aA, b1, b0, NT); \
    K3_STEP(10, aA, aB, b2, b1, NT); \
    K3_STEP(11, aB, aA, b3, b2, NT); \
    K3_STEP(12, aA, aB, b0, b3, NT); \
    _Pragma("unroll") for (int i = 0; i < 2; ++i) {                                             \
      int id = tid + 512 * i;                                                                   \
      if (id >= 896) id = tid;                                                                  \
      const int t = id / 14, c = id % 14;                                                       \
      zv[i] = *(const u32x4*)(Y + (size_t)(t0 + t) * YLD + 1344 + f0 + c * 8);                  \
      bb[i] = bf2f(bsp[t0 + t]);                                                                \
    }                                                                                           \
    K3_STEP(13, aB, aA, b1, b0, NT); \
    K3_STEP(14, aA, aB, b2, b1, NT); \
    K3_STEP(15, aB, aA, b3, b2, NT); \
  } while (0)
  u32x4 c1, c2;
  {
    f32x4 part[8];
    {
      const int te = (tid < 448) ? tid : 0;
      const f32x4* kvp = (const f32x4*)(ws + WS_KVP) + (size_t)h * 448 + te;
#pragma unroll
      for (int p = 0; p < 8; ++p) part[p] = kvp[(size_t)p * 16 * 448];
    }
    {
      const u32x4* pq = (const u32x4*)(ws + WS_PHQ) + ((size_t)(h * 128 + (b >> 4) * 8 + wave) * 2) * 64 + lane;
      c1 = pq[0];
      c2 = pq[64];
    }
    SCHED();
    K3_AISSUE(0);
    K3_AISSUE(1);
    if (wn == 0) { K3_BLOAD(b0, 0, 4); } else { K3_BLOAD(b0, 0, 3); }
    SCHED();
    char* KT = sm + K3_AUX;
    if (tid < 448) {
      const f32x4 s = ((part[0] + part[1]) + (part[2] + part[3])) + ((part[4] + part[5]) + (part[6] + part[7]));
      const int fp = tid >> 3, d0 = 4 * (tid & 7);
      const int mf = (fp >= 28) ? 1 : 0, f = fp - 28 * mf;
      const int pos = mf * 32 + ((f & 15) >> 2) * 8 + (f & 3) + 4 * (f >> 4);
      const unsigned a = pk2(s[0], s[1]), c = pk2(s[2], s[3]);
      char* d = KT + d0 * 144 + pos * 2;
      *(u16*)(d) = (u16)a; *(u16*)(d + 144) = (u16)(a >> 16);
      *(u16*)(d + 288) = (u16)c; *(u16*)(d + 432) = (u16)(c >> 16);
    } else {
      const int i = tid - 448, d = i >> 1, mf = i & 1;
      *(u32x2*)(KT + d * 144 + (mf * 32 + 28) * 2) = u32x2{0u, 0u};
    }
  }
  SCHED();
  K3_AISSUE(2);
  if (wn == 0) { K3_BLOAD(b1, 1, 4); } else { K3_BLOAD(b1, 1, 3); }
  K3_AISSUE(3);
  if (wn == 0) { K3_BLOAD(b2, 2, 4); } else { K3_BLOAD(b2, 2, 3); }
  K3_AISSUE(4); K3_AISSUE(5); K3_AISSUE(6); K3_AISSUE(7);
  asm volatile("s_waitcnt vmcnt(20)" ::: "memory");
  STAMP(2, 1);
  BAR();
  {
    const char* KT = sm + K3_AUX;
    bf16x8 af[2][2];
#pragma unroll
    for (int dt = 0; dt < 2; ++dt)
#pragma unroll
      for (int mf = 0; mf < 2; ++mf) af[dt][mf] = *(const bf16x8*)(KT + (16 * dt + r16) * 144 + (32 * mf + 8 * g) * 2);
    f32x4 o0 = {0.f, 0.f, 0.f, 0.f}, o1 = {0.f, 0.f, 0.f, 0.f};
    o0 = mfma16(af[0][0], __builtin_bit_cast(bf16x8, c1), o0);
    o1 = mfma16(af[1][0], __builtin_bit_cast(bf16x8, c1), o1);
    o0 = mfma16(af[0][1], __builtin_bit_cast(bf16x8, c2), o0);
    o1 = mfma16(af[1][1], __builtin_bit_cast(bf16x8, c2), o1);
    float den = __shfl(o1[0], 48 + r16, 64);
    den = rbf(rbf(den) + 1e-6f);
    const float inv = 1.f / den;
    const int mtile = (b >> 4) * 8 + wave;
    {
      const int k = h * 28 + 4 * g;
      u16* dst = oh + ((size_t)(mtile * 42 + (k >> 5)) * 64 + ((k >> 3) & 3) * 16 + r16) * 8 + (k & 7);
      *(u32x2*)(dst) = u32x2{pk2(rbf(o0[0]) * inv, rbf(o0[1]) * inv), pk2(rbf(o0[2]) * inv, rbf(o0[3]) * inv)};
    }
    if (g != 3) {
      const int k = h * 28 + 16 + 4 * g;
      u16* dst = oh + ((size_t)(mtile * 42 + (k >> 5)) * 64 + ((k >> 3) & 3) * 16 + r16) * 8 + (k & 7);
      *(u32x2*)(dst) = u32x2{pk2(rbf(o1[0]) * inv, rbf(o1[1]) * inv), pk2(rbf(o1[2]) * inv, rbf(o1[3]) * inv)};
    }
  }
  SCHED();
#pragma unroll
  for (int n = 0; n < 4; ++n)
#pragma unroll
    for (int m = 0; m < 4; ++m) acc[n][m] = f32x4{0.f, 0.f, 0.f, 0.f};
  if (wn == 0) K3_LOOP(4);
  else K3_LOOP(3);
  BAR();
  STAMP(2, 21);
  {
    char* rd = sm + kq * (64 * K3_RS) + r16 * K3_RS + (64 * wn + 4 * g) * 4;
#pragma unroll
    for (int n = 0; n < 4; ++n)
      if (n < 3 || wn == 0) {
#pragma unroll
        for (int m = 0; m < 4; ++m) *(f32x4*)(rd + 16 * m * K3_RS + 64 * n) = acc[n][m];
      }
  }
  BAR();
#pragma unroll
  for (int i = 0; i < 2; ++i) {
    const int id = tid + 512 * i;
    if (id < 896) {
      const int t = id / 14, c = id % 14;
      const char* p = sm + t * K3_RS + c * 32;
      f32x4 v0 = (*(const f32x4*)(p) + *(const f32x4*)(p + 64 * K3_RS)) +
                 (*(const f32x4*)(p + 128 * K3_RS) + *(const f32x4*)(p + 192 * K3_RS));
      f32x4 v1 = (*(const f32x4*)(p + 16) + *(const f32x4*)(p + 64 * K3_RS + 16)) +
                 (*(const f32x4*)(p + 128 * K3_RS + 16) + *(const f32x4*)(p + 192 * K3_RS + 16));
      u32x4 z;
#pragma unroll
      for (int j = 0; j < 4; ++j) z[j] = gelu_pk(zv[i][j]);
      u32x4 o;
      o[0] = pk2(lo_f(z[0]) * rbf(rbf(v0[0]) + bb[i]), hi_f(z[0]) * rbf(rbf(v0[1]) + bb[i]));
      o[1] = pk2(lo_f(z[1]) * rbf(rbf(v0[2]) + bb[i]), hi_f(z[1]) * rbf(rbf(v0[3]) + bb[i]));
      o[2] = pk2(lo_f(z[2]) * rbf(rbf(v1[0]) + bb[i]), hi_f(z[2]) * rbf(rbf(v1[1]) + bb[i]));
      o[3] = pk2(lo_f(z[3]) * rbf(rbf(v1[2]) + bb[i]), hi_f(z[3]) * rbf(rbf(v1[3]) + bb[i]));
      const int k = 448 + f0 + c * 8, tt = t0 + t;
      *(u32x4*)(oh + ((size_t)((tt >> 4) * 42 + (k >> 5)) * 64 + ((k >> 3) & 3) * 16 + (tt & 15)) * 8) = o;
    }
  }
  STAMP(2, 31);
}

__global__ __launch_bounds__(384) void k4_kernel(const u16* __restrict__ x, const char* __restrict__ ws,
                                                  u16* __restrict__ out) {
  __shared__ __attribute__((aligned(16))) char sm[6 * 32 * K3_RS];
  const int tid = threadIdx.x, lane = tid & 63, wave = __builtin_amdgcn_readfirstlane(tid >> 6);
  const int b = blockIdx.x;
  const int xcd = b & 7, idx = b >> 3;
  const int mt_ = xcd * 8 + (idx & 7), nt_ = idx >> 3;
  const int t0 = mt_ * 32, n0 = nt_ * 112;
  const int g = lane >> 4, r16 = lane & 15;
  STAMP(3, 0);
  const u16* ap = (const u16*)(ws + WS_OH) + ((size_t)(mt_ * 2 * 42 + wave * 7) * 64 + lane) * 8;
  const u16* bp = (const u16*)(ws + WS_WT) + ((size_t)(nt_ * 7 * 42 + wave * 7) * 64 + lane) * 8;
  bf16x8 a0[2], b0[7], a1[2], b1[7], a2[2], b2[7], a3[2], b3[7];
#define K4_LOAD(AF, BF, KS)                                                                     \
  do {                                                                                          \
    _Pragma("unroll") for (int m = 0; m < 2; ++m) AF[m] = *(const bf16x8*)(ap + (m * 42 + (KS)) * 512); \
    _Pragma("unroll") for (int n = 0; n < 7; ++n) BF[n] = *(const bf16x8*)(bp + (n * 42 + (KS)) * 512); \
  } while (0)
  K4_LOAD(a0, b0, 0);
  K4_LOAD(a1, b1, 1);
  K4_LOAD(a2, b2, 2);
  K4_LOAD(a3, b3, 3);
  u32x4 xv[2];
#pragma unroll
  for (int i = 0; i < 2; ++i) {
    int id = tid + 384 * i;
    if (id >= 448) id = tid;
    xv[i] = *(const u32x4*)(x + (size_t)(t0 + id / 14) * D_ + n0 + (id % 14) * 8);
  }
  SCHED();
  f32x4 acc[7][2];
#pragma unroll
  for (int n = 0; n < 7; ++n) { acc[n][0] = f32x4{0.f, 0.f, 0.f, 0.f}; acc[n][1] = f32x4{0.f, 0.f, 0.f, 0.f}; }
#define K4_MMA(AF, BF)                                                                          \
  do {                                                                                          \
    _Pragma("unroll") for (int n = 0; n < 7; ++n) {                                             \
      acc[n][0] = mfma16(BF[n], AF[0], acc[n][0]);                                              \
      acc[n][1] = mfma16(BF[n], AF[1], acc[n][1]);                                              \
    }                                                                                           \
    SCHED();                                                                                    \
  } while (0)
  K4_MMA(a0, b0); K4_LOAD(a0, b0, 4); SCHED();
  K4_MMA(a1, b1); K4_LOAD(a1, b1, 5); SCHED();
  K4_MMA(a2, b2); K4_LOAD(a2, b2, 6); SCHED();
  K4_MMA(a3, b3);
  K4_MMA(a0, b0);
  K4_MMA(a1, b1);
  K4_MMA(a2, b2);
  STAMP(3, 1);
  char* rd = sm + wave * (32 * K3_RS) + r16 * K3_RS + 16 * g;
#pragma unroll
  for (int n = 0; n < 7; ++n)
#pragma unroll
    for (int m = 0; m < 2; ++m) *(f32x4*)(rd + 16 * m * K3_RS + 64 * n) = acc[n][m];
  __syncthreads();
#pragma unroll
  for (int i = 0; i < 2; ++i) {
    const int id = tid + 384 * i;
    if (id < 448) {
      const int et = id / 14, ec = id % 14;
      const char* p = sm + et * K3_RS + ec * 32;
      f32x4 v0 = {0.f, 0.f, 0.f, 0.f}, v1 = {0.f, 0.f, 0.f, 0.f};
#pragma unroll
      for (int w = 0; w < 6; ++w) {
        v0 += *(const f32x4*)(p + w * 32 * K3_RS);
        v1 += *(const f32x4*)(p + w * 32 * K3_RS + 16);
      }
      u32x4 o;
      o[0] = pk2(lo_f(xv[i][0]) + v0[0], hi_f(xv[i][0]) + v0[1]);
      o[1] = pk2(lo_f(xv[i][1]) + v0[2], hi_f(xv[i][1]) + v0[3]);
      o[2] = pk2(lo_f(xv[i][2]) + v1[0], hi_f(xv[i][2]) + v1[1]);
      o[3] = pk2(lo_f(xv[i][3]) + v1[2], hi_f(xv[i][3]) + v1[3]);
      *(u32x4*)(out + (size_t)(t0 + et) * D_ + n0 + ec * 8) = o;
    }
  }
  STAMP(3, 31);
}

extern "C" void kernel_launch(void* const* d_in, const int* in_sizes, int n_in, void* d_out, int out_size,
                              void* d_ws, size_t ws_size, hipStream_t stream) {
  const u16* x = (const u16*)d_in[0];
  const u16* wqkv = (const u16*)d_in[1];
  const u16* wphi = (const u16*)d_in[2];
  const u16* wo = (const u16*)d_in[3];
  const u16* win = (const u16*)d_in[5];
  const u16* wsp = (const u16*)d_in[6];
  const u16* bsp = (const u16*)d_in[7];
  const u16* wout = (const u16*)d_in[8];
  char* ws = (char*)d_ws;
  k1_kernel<<<256, 640, 0, stream>>>(x, wqkv, wo, win, wout, wsp, ws);
  k2_kernel<<<384, 512, 0, stream>>>(wphi, ws);
  k3_kernel<<<256, 512, 0, stream>>>(wsp, bsp, wphi, ws);
  k4_kernel<<<256, 384, 0, stream>>>(x, ws, (u16*)d_out);
}
```

```cpp
#include <hip/hip_runtime.h>
#include <hip/hip_bf16.h>
#include <stdint.h>

typedef __attribute__((ext_vector_type(8))) short bf16x8;
typedef __attribute__((ext_vector_type(4))) short s16x4;
typedef __attribute__((ext_vector_type(4))) float f32x4;
typedef __attribute__((ext_vector_type(2))) float f32x2;
typedef __attribute__((ext_vector_type(4))) unsigned u32x4;
typedef __attribute__((ext_vector_type(2))) unsigned u32x2;
typedef __attribute__((ext_vector_type(2))) __bf16 bf2_t;
typedef unsigned short u16;
#define LDS_AS __attribute__((address_space(3)))

constexpr int S_ = 2048, D_ = 448, NQKV = 1344, NIN = 1792, FH = 896, KO = 1344;

__device__ __forceinline__ unsigned pk2(float a, float b) {
  f32x2 v = {a, b};
  return __builtin_bit_cast(unsigned, __builtin_convertvector(v, bf2_t));
}
__device__ __forceinline__ float lo_f(unsigned u) { return __uint_as_float(u << 16); }
__device__ __forceinline__ float hi_f(unsigned u) { return __uint_as_float(u & 0xffff0000u); }
__device__ __forceinline__ float rbf(float a) { return lo_f(pk2(a, 0.f)); }
__device__ __forceinline__ float bf2f(u16 v) { return __uint_as_float(((unsigned)v) << 16); }
__device__ __forceinline__ float dot2(unsigned a, unsigned b, float c) {
  return __builtin_amdgcn_fdot2_f32_bf16(__builtin_bit_cast(bf2_t, a), __builtin_bit_cast(bf2_t, b), c, false);
}

__device__ __forceinline__ float x16_max(float v) {
  auto r = __builtin_amdgcn_permlane16_swap(__float_as_uint(v), __float_as_uint(v), false, false);
  return fmaxf(__uint_as_float(r[0]), __uint_as_float(r[1]));
}
__device__ __forceinline__ float x32_max(float v) {
  auto r = __builtin_amdgcn_permlane32_swap(__float_as_uint(v), __float_as_uint(v), false, false);
  return fmaxf(__uint_as_float(r[0]), __uint_as_float(r[1]));
}
__device__ __forceinline__ float x16_min(float v) {
  auto r = __builtin_amdgcn_permlane16_swap(__float_as_uint(v), __float_as_uint(v), false, false);
  return fminf(__uint_as_float(r[0]), __uint_as_float(r[1]));
}
__device__ __forceinline__ float x32_min(float v) {
  auto r = __builtin_amdgcn_permlane32_swap(__float_as_uint(v), __float_as_uint(v), false, false);
  return fminf(__uint_as_float(r[0]), __uint_as_float(r[1]));
}
__device__ __forceinline__ float x16_add(float v) {
  auto r = __builtin_amdgcn_permlane16_swap(__float_as_uint(v), __float_as_uint(v), false, false);
  return __uint_as_float(r[0]) + __uint_as_float(r[1]);
}
__device__ __forceinline__ float x32_add(float v) {
  auto r = __builtin_amdgcn_permlane32_swap(__float_as_uint(v), __float_as_uint(v), false, false);
  return __uint_as_float(r[0]) + __uint_as_float(r[1]);
}
__device__ __forceinline__ float red8_add(float v) {
  v += __uint_as_float(__builtin_amdgcn_mov_dpp(__float_as_uint(v), 0xB1, 0xf, 0xf, true));
  v += __uint_as_float(__builtin_amdgcn_mov_dpp(__float_as_uint(v), 0x4E, 0xf, 0xf, true));
  v += __uint_as_float(__builtin_amdgcn_mov_dpp(__float_as_uint(v), 0x141, 0xf, 0xf, true));
  return v;
}
__device__ __forceinline__ f32x4 mfma16(bf16x8 a, bf16x8 b, f32x4 c) {
  return __builtin_amdgcn_mfma_f32_16x16x32_bf16(a, b, c, 0, 0, 0);
}
__device__ __forceinline__ unsigned gelu_pk(unsigned p) {
  const f32x2 u = {lo_f(p), hi_f(p)};
  const f32x2 c1 = {-2.3022081986f, -2.3022081986f};
  const f32x2 c2 = {-0.1029432397f, -0.1029432397f};
  f32x2 t = u * u;
  f32x2 a = u * (t * c2 + c1);
  f32x2 d = {1.f + __builtin_amdgcn_exp2f(a[0]), 1.f + __builtin_amdgcn_exp2f(a[1])};
  f32x2 r = {__builtin_amdgcn_rcpf(d[0]), __builtin_amdgcn_rcpf(d[1])};
  f32x2 o = u * r;
  return pk2(o[0], o[1]);
}
#define SCHED() __builtin_amdgcn_sched_barrier(0)
#define BAR() do { SCHED(); asm volatile("" ::: "memory"); __builtin_amdgcn_s_waitcnt(0xC07F); __builtin_amdgcn_s_barrier(); asm volatile("" ::: "memory"); SCHED(); } while (0)
__device__ __forceinline__ void glds16(const void* g, char* l) {
  __builtin_amdgcn_global_load_lds((const unsigned*)g, (LDS_AS unsigned*)l, 16, 0, 0);
}
__device__ __forceinline__ bf16x8 tr_frag(const char* p0, const char* p1) {
  s16x4 a = __builtin_amdgcn_ds_read_tr16_b64_v4i16((LDS_AS s16x4*)p0);
  s16x4 c = __builtin_amdgcn_ds_read_tr16_b64_v4i16((LDS_AS s16x4*)p1);
  return __builtin_shufflevector(a, c, 0, 1, 2, 3, 4, 5, 6, 7);
}

constexpr int YLD = 3136;
constexpr size_t WS_Y = 0;
constexpr size_t WS_Z2N = WS_Y + (size_t)S_ * YLD * 2 + 256;
constexpr size_t WS_OH = WS_Z2N + (size_t)S_ * 896 * 2;
constexpr size_t WS_WT = WS_OH + (size_t)S_ * KO * 2;
constexpr size_t WS_KVP = WS_WT + (size_t)D_ * KO * 2;
constexpr size_t WS_PHQ = WS_KVP + (size_t)8 * 16 * 56 * 32 * 4;
constexpr size_t WS_END = WS_PHQ + (size_t)16 * 128 * 2048;

#define STAMP(K, I) do { } while (0)
#define SGB(mask, n) __builtin_amdgcn_sched_group_barrier(mask, n, 0)

__device__ __forceinline__ void phi_map(bf16x8 qf, bf16x8 wp0, bf16x8 wp1, int g, unsigned (&pp)[2][2],
                                        unsigned (&pm)[2][2]) {
  f32x4 z = {0.f, 0.f, 0.f, 0.f};
  f32x4 p0 = mfma16(wp0, qf, z);
  f32x4 p1 = mfma16(wp1, qf, z);
  float v[8];
  {
    unsigned a = pk2(p0[0], p0[1]), b = pk2(p0[2], p0[3]), c = pk2(p1[0], p1[1]), d = pk2(p1[2], p1[3]);
    v[0] = lo_f(a); v[1] = hi_f(a); v[2] = lo_f(b); v[3] = hi_f(b);
    v[4] = lo_f(c); v[5] = hi_f(c); v[6] = lo_f(d); v[7] = hi_f(d);
  }
  const bool valid1 = (g != 3);
  if (!valid1) { v[4] = v[0]; v[5] = v[0]; v[6] = v[0]; v[7] = v[0]; }
  float mx = fmaxf(fmaxf(fmaxf(v[0], v[1]), fmaxf(v[2], v[3])), fmaxf(fmaxf(v[4], v[5]), fmaxf(v[6], v[7])));
  float mn = fminf(fminf(fminf(v[0], v[1]), fminf(v[2], v[3])), fminf(fminf(v[4], v[5]), fminf(v[6], v[7])));
  mx = x32_max(x16_max(mx));
  mn = x32_min(x16_min(mn));
  const float L2E = 1.4426950408889634f;
  const float mxl = mx * L2E, mnl = mn * L2E;
  float ep[8], em[8];
  float sp = 0.f, sm = 0.f;
#pragma unroll
  for (int j = 0; j < 8; ++j) {
    ep[j] = __builtin_amdgcn_exp2f(__builtin_fmaf(v[j], L2E, -mxl));
    em[j] = __builtin_amdgcn_exp2f(__builtin_fmaf(v[j], -L2E, mnl));
    if (j >= 4) { ep[j] = valid1 ? ep[j] : 0.f; em[j] = valid1 ? em[j] : 0.f; }
    sp += ep[j];
    sm += em[j];
  }
  sp = x32_add(x16_add(sp));
  sm = x32_add(x16_add(sm));
  const float rp = __builtin_amdgcn_rcpf(sp), rm = __builtin_amdgcn_rcpf(sm);
  pp[0][0] = pk2(ep[0] * rp, ep[1] * rp); pp[0][1] = pk2(ep[2] * rp, ep[3] * rp);
  pp[1][0] = pk2(ep[4] * rp, ep[5] * rp); pp[1][1] = pk2(ep[6] * rp, ep[7] * rp);
  pm[0][0] = pk2(em[0] * rm, em[1] * rm); pm[0][1] = pk2(em[2] * rm, em[3] * rm);
  pm[1][0] = pk2(em[4] * rm, em[5] * rm); pm[1][1] = pk2(em[6] * rm, em[7] * rm);
}

constexpr int K1_XST = 128 * 128;
constexpr int K1_STAGE = K1_XST + 64 * 448;
constexpr int K1_LDS = 3 * K1_STAGE;

__global__ __launch_bounds__(768) void k1_kernel(const u16* __restrict__ x, const u16* __restrict__ wqkv,
                                                  const u16* __restrict__ wo, const u16* __restrict__ win,
                                                  const u16* __restrict__ wout, const u16* __restrict__ wsp,
                                                  char* __restrict__ ws) {
  __shared__ __attribute__((aligned(16))) char sm[K1_LDS];
  const int tid = threadIdx.x, lane = tid & 63, wave = __builtin_amdgcn_readfirstlane(tid >> 6);
  const int b = blockIdx.x;
  STAMP(0, 0);
  if (b >= 224) {
    const int kb = b - 224;
    const int k0 = kb * 64;
    if (kb < 21) {
    u16* T = (u16*)sm;
    constexpr int TS = 456;
    u32x4 v[5];
#pragma unroll
    for (int j = 0; j < 5; ++j) {
      int i = tid + 768 * j;
      if (i >= 64 * 56) i = tid;
      const int r = i / 56, c = i % 56;
      const int k = k0 + r;
      const u16* src = (k < 448) ? (wo + (size_t)k * 448) : (wout + (size_t)(k - 448) * 448);
      v[j] = *(const u32x4*)(src + c * 8);
    }
#pragma unroll
    for (int j = 0; j < 5; ++j) {
      const int i = tid + 768 * j;
      if (i < 64 * 56) *(u32x4*)(T + (i / 56) * TS + (i % 56) * 8) = v[j];
    }
    __syncthreads();
    u16* wT = (u16*)(ws + WS_WT);
    for (int i = tid; i < 448 * 8; i += 768) {
      const int n = i >> 3, c = i & 7;
      u16 t[8];
#pragma unroll
      for (int j = 0; j < 8; ++j) t[j] = T[(c * 8 + j) * TS + n];
      u32x4 o;
#pragma unroll
      for (int j = 0; j < 4; ++j) o[j] = (unsigned)t[2 * j] | ((unsigned)t[2 * j + 1] << 16);
      *(u32x4*)(wT + ((size_t)((n >> 4) * 42 + (k0 >> 5) + (c >> 2)) * 64 + (c & 3) * 16 + (n & 15)) * 8) = o;
    }
    }
    STAMP(0, 1);
    {
      const char* p = (const char*)wsp + (size_t)kb * 262144 + tid * 16;
      unsigned a = 0;
#pragma unroll
      for (int i = 0; i < 21; ++i) {
        u32x4 v = *(const u32x4*)(p + i * 12288);
        a |= v[0] & v[1] & v[2] & v[3];
      }
      asm volatile("" ::"v"(a));
    }
    STAMP(0, 31);
    return;
  }

  const int xcd = b & 7, idx = b >> 3;
  const int cb = (xcd & 1) * 7 + idx % 7;
  const int rt = (xcd >> 1) * 4 + idx / 7;
  const int row0 = rt * 128;
  const int g = lane >> 4, r16 = lane & 15;
  const int wm = (wave >> 1) & 3, wn = wave & 1;
  constexpr int RST = 464;

  if (wave >= 8) {
    const int lw = wave - 8;
    const u16* W = (cb < 6) ? (wqkv + 224 * cb) : (win + 224 * (cb - 6));
    const int ldw = (cb < 6) ? NQKV : NIN;
    const u16* src[11];
#pragma unroll
    for (int j = 0; j < 11; ++j) {
      const int i = lw + 4 * j;
      if (j < 4) {
        const int row = 8 * i + (lane >> 3);
        src[j] = x + (size_t)(row0 + row) * D_ + (((lane & 7) ^ ((row >> 1) & 7)) << 3);
      } else {
        const int o = (i - 16) * 1024 + lane * 16;
        const int k = o / 448, cp = (o % 448) >> 4;
        const int G = (cp >> 1) ^ ((k >> 3) & 1);
        src[j] = W + (size_t)k * ldw + 8 * (2 * G + (cp & 1));
      }
    }
    const int wstep = 64 * ldw;
    char* dst0 = sm + lw * 1024;
#pragma unroll
    for (int s = 0; s < 2; ++s)
#pragma unroll
      for (int j = 0; j < 11; ++j) glds16(src[j] + s * (j < 4 ? 64 : wstep), dst0 + s * K1_STAGE + j * 4096);
    int nb = 2;
#pragma unroll 1
    for (int t = 0; t < 7; ++t) {
      if (t < 6) asm volatile("s_waitcnt vmcnt(11)" ::: "memory");
      else asm volatile("s_waitcnt vmcnt(0)" ::: "memory");
      __builtin_amdgcn_s_barrier();
      if (t + 2 < 7) {
        char* dst = dst0 + nb * K1_STAGE;
#pragma unroll
        for (int j = 0; j < 11; ++j) glds16(src[j] + (t + 2) * (j < 4 ? 64 : wstep), dst + j * 4096);
        nb = (nb == 2) ? 0 : nb + 1;
      }
    }
    __builtin_amdgcn_s_barrier();
    __builtin_amdgcn_s_barrier();
  } else {
    f32x4 acc[7][2];
#pragma unroll
    for (int nt = 0; nt < 7; ++nt) { acc[nt][0] = f32x4{0.f, 0.f, 0.f, 0.f}; acc[nt][1] = f32x4{0.f, 0.f, 0.f, 0.f}; }
    float sx[2] = {0.f, 0.f}, sq[2] = {0.f, 0.f}, cs[7] = {0.f, 0.f, 0.f, 0.f, 0.f, 0.f, 0.f};
    const int sw = (r16 >> 1) & 7;
    const int xo0 = (32 * wm + r16) * 128 + ((g ^ sw) << 4), xo1 = (32 * wm + r16) * 128 + (((4 | g) ^ sw) << 4);
    const int wrow = K1_XST + (8 * g + ((lane >> 2) & 3)) * 448 + (lane & 3) * 8 + 7 * wn * 32;
    const int wE = wrow + ((wn & 1) ? -32 * (g & 1) : 32 * (g & 1));
    const int wO = wrow + ((wn & 1) ? 32 * (g & 1) : -32 * (g & 1));
    bf16x8 xa[2], wa[7], xb[2], wb[7];
#define K1_READ(XF, WF, SB, KS)                                                              \
  do {                                                                                       \
    XF[0] = *(const bf16x8*)((SB) + ((KS) ? xo1 : xo0));                                     \
    XF[1] = *(const bf16x8*)((SB) + ((KS) ? xo1 : xo0) + 2048);                              \
    _Pragma("unroll") for (int nt = 0; nt < 7; ++nt) {                                       \
      const char* p_ = (SB) + ((nt & 1) ? wO : wE) + nt * 32 + (KS)*32 * 448;                \
      WF[nt] = tr_frag(p_, p_ + 4 * 448);                                                    \
    }                                                                                        \
  } while (0)
#define K1_CS(WF)                                                                            \
  do {                                                                                       \
    if (wm == 0) { _Pragma("unroll") for (int nt = 0; nt < 7; ++nt) cs[nt] = dot2(__builtin_bit_cast(u32x4, WF[nt])[0], 0x3F803F80u, cs[nt]); } \
    else if (wm == 1) { _Pragma("unroll") for (int nt = 0; nt < 7; ++nt) cs[nt] = dot2(__builtin_bit_cast(u32x4, WF[nt])[1], 0x3F803F80u, cs[nt]); } \
    else if (wm == 2) { _Pragma("unroll") for (int nt = 0; nt < 7; ++nt) cs[nt] = dot2(__builtin_bit_cast(u32x4, WF[nt])[2], 0x3F803F80u, cs[nt]); } \
    else { _Pragma("unroll") for (int nt = 0; nt < 7; ++nt) cs[nt] = dot2(__builtin_bit_cast(u32x4, WF[nt])[3], 0x3F803F80u, cs[nt]); } \
    SCHED();                                                                                 \
  } while (0)
#define K1_MMA(XF, WF)                                                                       \
  do {                                                                                       \
    _Pragma("unroll") for (int nt = 0; nt < 7; ++nt) acc[nt][0] = mfma16(WF[nt], XF[0], acc[nt][0]); \
    _Pragma("unroll") for (int nt = 0; nt < 7; ++nt) acc[nt][1] = mfma16(WF[nt], XF[1], acc[nt][1]); \
    _Pragma("unroll") for (int mt = 0; mt < 2; ++mt) {                                       \
      const u32x4 u_ = __builtin_bit_cast(u32x4, XF[mt]);                                    \
      _Pragma("unroll") for (int j = 0; j < 4; ++j) {                                        \
        sx[mt] = dot2(u_[j], 0x3F803F80u, sx[mt]);                                           \
        sq[mt] = dot2(u_[j], u_[j], sq[mt]);                                                 \
      }                                                                                      \
    }                                                                                        \
  } while (0)
    int rb = 0;
#pragma unroll 1
    for (int t = 0; t < 7; ++t) {
      BAR();
      STAMP(0, 3 + t);
      const char* sb = sm + rb * K1_STAGE;
      rb = (rb == 2) ? 0 : rb + 1;
      K1_READ(xa, wa, sb, 0);
      SCHED();
      K1_READ(xb, wb, sb, 1);
      K1_MMA(xa, wa);
      _Pragma("unroll") for (int i_ = 0; i_ < 8; ++i_) { SGB(0x008, 1); SGB(0x100, 2); }
      _Pragma("unroll") for (int i_ = 0; i_ < 6; ++i_) { SGB(0x008, 1); SGB(0x002, 3); }
      SCHED();
      K1_CS(wa);
      K1_MMA(xb, wb);
      _Pragma("unroll") for (int i_ = 0; i_ < 14; ++i_) { SGB(0x008, 1); SGB(0x002, 1); }
      SCHED();
      K1_CS(wb);
    }
    BAR();
    STAMP(0, 17);
    float* CS = (float*)(sm + 128 * RST);
#pragma unroll
    for (int nt = 0; nt < 7; ++nt) {
      const float v = x32_add(x16_add(cs[nt]));
      if (g == 0) CS[wm * 224 + 112 * wn + 16 * nt + r16] = v;
    }
    float rs[2], rm[2];
#pragma unroll
    for (int mt = 0; mt < 2; ++mt) {
      const float mean = x32_add(x16_add(sx[mt])) * (1.f / 448.f);
      const float ex2 = x32_add(x16_add(sq[mt])) * (1.f / 448.f);
      const float var = fmaxf(ex2 - mean * mean, 0.f);
      const float sd = rbf(sqrtf(rbf(rbf(var) + 1e-5f)));
      rs[mt] = 1.f / sd;
      rm[mt] = rs[mt] * rbf(mean);
    }
    BAR();
#pragma unroll
    for (int nt = 0; nt < 7; ++nt) {
      const f32x4* cp = (const f32x4*)(CS + 112 * wn + 16 * nt + 4 * g);
      const f32x4 c4 = (cp[0] + cp[56]) + (cp[112] + cp[168]);
#pragma unroll
      for (int mt = 0; mt < 2; ++mt) {
        char* dst = sm + (32 * wm + 16 * mt + r16) * RST + (112 * wn + 4 * g) * 2 + 32 * nt;
        const f32x4 v = acc[nt][mt] * rs[mt] - c4 * rm[mt];
        *(u32x2*)(dst) = u32x2{pk2(v[0], v[1]), pk2(v[2], v[3])};
      }
    }
  }
  __syncthreads();
  u16* dstb = (u16*)(ws + WS_Y) + (size_t)row0 * YLD + 224 * cb;
#pragma unroll
  for (int i = 0; i < 5; ++i) {
    const int id = tid + 768 * i;
    if (id < 128 * 28) {
      const int s = id / 28, c = id % 28;
      *(u32x4*)(dstb + (size_t)s * YLD + c * 8) = *(const u32x4*)(sm + s * RST + c * 16);
    }
  }
  STAMP(0, 31);
}

__device__ __forceinline__ float red64_add(float v) {
  v = red8_add(v);
  v += __uint_as_float(__builtin_amdgcn_mov_dpp(__float_as_uint(v), 0x140, 0xf, 0xf, true));
  return x32_add(x16_add(v));
}

constexpr int K2_PW = 64 * 80 + 32 * 64;
__global__ __launch_bounds__(512) void k2_kernel(const u16* __restrict__ wphi, char* __restrict__ ws) {
  __shared__ __attribute__((aligned(16))) char sm[8 * 56 * 32 * 4 + 2048];
  const int tid = threadIdx.x, lane = tid & 63, wave = __builtin_amdgcn_readfirstlane(tid >> 6);
  const int b = blockIdx.x;
  const u16* Y = (const u16*)(ws + WS_Y);
  STAMP(1, 0);
  if (b < 128) {
    const int h = b & 15, rg = b >> 4;
    const int g = lane >> 4, r16 = lane & 15;
    const int rowb = rg * 256 + wave * 32;
    u32x2 kv_[2][2];
#pragma unroll
    for (int st = 0; st < 2; ++st) {
      const u16* kp = Y + (size_t)(rowb + st * 16 + r16) * YLD + 448 + h * 28 + 8 * g;
      kv_[st][0] = *(const u32x2*)(kp);
      kv_[st][1] = *(const u32x2*)(kp + 4);
    }
    u32x2 vv[4];
#pragma unroll
    for (int i = 0; i < 4; ++i) {
      const int row = (lane >> 3) + 8 * i, pc = lane & 7;
      vv[i] = *(const u32x2*)(Y + (size_t)(rowb + row) * YLD + 896 + h * 28 + 4 * (pc < 7 ? pc : 0));
      if (pc == 7) vv[i] = u32x2{0x00003F80u, 0u};
    }
    u32x4 wraw = {0u, 0u, 0u, 0u};
    if (tid < 98) wraw = *(const u32x4*)(wphi + (size_t)h * 784 + tid * 8);
    char* WP = sm + 8 * 56 * 32 * 4;
    if (tid < 98) *(u32x4*)(WP + tid * 16) = wraw;
    char* PK = sm + wave * K2_PW;
    char* VS = PK + 64 * 80;
    __syncthreads();
    STAMP(1, 1);
    const char* wp = WP + (8 * g + ((lane >> 2) & 3)) * 56 + (lane & 3) * 8;
    bf16x8 wp0 = tr_frag(wp, wp + 4 * 56);
    bf16x8 wp1 = tr_frag(wp + 32, wp + 4 * 56 + 32);
    if (g == 3) {
#pragma unroll
      for (int j = 4; j < 8; ++j) { wp0[j] = 0; wp1[j] = 0; }
    }
    f32x4 kacc[4][2];
#pragma unroll
    for (int a = 0; a < 4; ++a) { kacc[a][0] = f32x4{0.f, 0.f, 0.f, 0.f}; kacc[a][1] = f32x4{0.f, 0.f, 0.f, 0.f}; }
#pragma unroll
    for (int pr = 0; pr < 1; ++pr) {
#pragma unroll
      for (int i = 0; i < 4; ++i) {
        const int row = (lane >> 3) + 8 * i;
        const int slot = 2 * (row & 15) + (row >> 4);
        *(u32x2*)(VS + slot * 64 + (lane & 7) * 8) = vv[4 * pr + i];
      }
      unsigned pp[2][2][2], pm[2][2][2];
#pragma unroll
      for (int mt = 0; mt < 2; ++mt) {
        u32x4 q4 = {kv_[2 * pr + mt][0][0], kv_[2 * pr + mt][0][1], kv_[2 * pr + mt][1][0], kv_[2 * pr + mt][1][1]};
        if (g == 3) { q4[2] = 0u; q4[3] = 0u; }
        phi_map(__builtin_bit_cast(bf16x8, q4), wp0, wp1, g, pp[mt], pm[mt]);
      }
#pragma unroll
      for (int ft = 0; ft < 2; ++ft) {
        if (ft == 0 || g != 3) {
          char* d0 = PK + (16 * ft + 4 * g) * 80 + r16 * 4;
#pragma unroll
          for (int j = 0; j < 2; ++j) {
            *(unsigned*)(d0 + (2 * j) * 80) = (pp[0][ft][j] & 0xffffu) | (pp[1][ft][j] << 16);
            *(unsigned*)(d0 + (2 * j + 1) * 80) = (pp[0][ft][j] >> 16) | (pp[1][ft][j] & 0xffff0000u);
            *(unsigned*)(d0 + (28 + 2 * j) * 80) = (pm[0][ft][j] & 0xffffu) | (pm[1][ft][j] << 16);
            *(unsigned*)(d0 + (28 + 2 * j + 1) * 80) = (pm[0][ft][j] >> 16) | (pm[1][ft][j] & 0xffff0000u);
          }
        }
      }
      __builtin_amdgcn_s_waitcnt(0xC07F);
      __builtin_amdgcn_wave_barrier();
      bf16x8 bfr[2];
      {
        const char* vp = VS + (8 * g + ((lane >> 2) & 3)) * 64 + (lane & 3) * 8;
        bfr[0] = tr_frag(vp, vp + 4 * 64);
        bfr[1] = tr_frag(vp + 32, vp + 4 * 64 + 32);
      }
#pragma unroll
      for (int a = 0; a < 4; ++a) {
        bf16x8 af = *(const bf16x8*)(PK + (16 * a + r16) * 80 + g * 16);
        kacc[a][0] = mfma16(af, bfr[0], kacc[a][0]);
        kacc[a][1] = mfma16(af, bfr[1], kacc[a][1]);
      }
      __builtin_amdgcn_s_waitcnt(0xC07F);
      __builtin_amdgcn_wave_barrier();
    }
    STAMP(1, 2);
    __syncthreads();
    float* red = (float*)sm + wave * (56 * 32);
#pragma unroll
    for (int a = 0; a < 4; ++a)
#pragma unroll
      for (int c = 0; c < 2; ++c)
#pragma unroll
        for (int j = 0; j < 4; ++j) {
          const int f = 16 * a + 4 * g + j;
          if (f < 56) red[f * 32 + 16 * c + r16] = kacc[a][c][j];
        }
    __syncthreads();
    if (tid < 448) {
      const f32x4* rp = (const f32x4*)sm + tid;
      f32x4 s = (rp[0] + rp[448]) + (rp[2 * 448] + rp[3 * 448]);
      s += (rp[4 * 448] + rp[5 * 448]) + (rp[6 * 448] + rp[7 * 448]);
      *((f32x4*)(ws + WS_KVP) + (size_t)(rg * 16 + h) * 448 + tid) = s;
    }
  } else if (b >= 256) {
    const int h = b & 15, rg = (b - 256) >> 4;
    const int g = lane >> 4, r16 = lane & 15;
    u32x2 qv[2][2];
#pragma unroll
    for (int st = 0; st < 2; ++st) {
      const u16* qp = Y + (size_t)(rg * 256 + wave * 32 + st * 16 + r16) * YLD + h * 28 + 8 * g;
      qv[st][0] = *(const u32x2*)(qp);
      qv[st][1] = *(const u32x2*)(qp + 4);
    }
    u32x4 wraw = {0u, 0u, 0u, 0u};
    if (tid < 98) wraw = *(const u32x4*)(wphi + (size_t)h * 784 + tid * 8);
    if (tid < 98) *(u32x4*)(sm + tid * 16) = wraw;
    __syncthreads();
    const char* wp = sm + (8 * g + ((lane >> 2) & 3)) * 56 + (lane & 3) * 8;
    bf16x8 wp0 = tr_frag(wp, wp + 4 * 56);
    bf16x8 wp1 = tr_frag(wp + 32, wp + 4 * 56 + 32);
    if (g == 3) {
#pragma unroll
      for (int j = 4; j < 8; ++j) { wp0[j] = 0; wp1[j] = 0; }
    }
#pragma unroll
    for (int st = 0; st < 2; ++st) {
      u32x4 q4 = {qv[st][0][0], qv[st][0][1], qv[st][1][0], qv[st][1][1]};
      if (g == 3) { q4[2] = 0u; q4[3] = 0u; }
      unsigned pp[2][2], pm[2][2];
      phi_map(__builtin_bit_cast(bf16x8, q4), wp0, wp1, g, pp, pm);
      u32x4* dst = (u32x4*)(ws + WS_PHQ) + ((size_t)(h * 128 + rg * 16 + wave * 2 + st) * 2) * 64 + lane;
      dst[0] = u32x4{pp[0][0], pp[0][1], pp[1][0], pp[1][1]};
      dst[64] = u32x4{pm[0][0], pm[0][1], pm[1][0], pm[1][1]};
    }
  } else {
    const int row0 = (b - 128) * 16 + wave * 2;
    u16* z2n = (u16*)(ws + WS_Z2N);
    const int le = (lane < 56) ? lane : 0;
    u32x4 v[2][2];
#pragma unroll
    for (int r = 0; r < 2; ++r)
#pragma unroll
      for (int c = 0; c < 2; ++c) v[r][c] = *(const u32x4*)(Y + (size_t)(row0 + r) * YLD + 2240 + (le + 56 * c) * 8);
#pragma unroll
    for (int r = 0; r < 2; ++r) {
      float s = 0.f;
#pragma unroll
      for (int c = 0; c < 2; ++c)
#pragma unroll
        for (int j = 0; j < 4; ++j) {
          v[r][c][j] = gelu_pk(v[r][c][j]);
          s = dot2(v[r][c][j], 0x3F803F80u, s);
        }
      if (lane >= 56) s = 0.f;
      const float mean = red64_add(s) * (1.f / 896.f);
      const float mb = rbf(mean);
      float q = 0.f;
#pragma unroll
      for (int c = 0; c < 2; ++c)
#pragma unroll
        for (int j = 0; j < 4; ++j) {
          const float a = lo_f(v[r][c][j]) - mean, e = hi_f(v[r][c][j]) - mean;
          q += a * a + e * e;
          v[r][c][j] = pk2(lo_f(v[r][c][j]) - mb, hi_f(v[r][c][j]) - mb);
        }
      if (lane >= 56) q = 0.f;
      const float var = red64_add(q) * (1.f / 896.f);
      const float sd = rbf(sqrtf(rbf(rbf(var) + 1e-5f)));
      const float inv = 1.f / sd;
      if (lane < 56) {
#pragma unroll
        for (int c = 0; c < 2; ++c) {
          u32x4 o;
#pragma unroll
          for (int j = 0; j < 4; ++j) o[j] = pk2(lo_f(v[r][c][j]) * inv, hi_f(v[r][c][j]) * inv);
          *(u32x4*)(sm + (wave * 2 + r) * 1808 + (lane + 56 * c) * 16) = o;
        }
      }
    }
    __syncthreads();
    {
      const int rb0 = (b - 128) * 16;
      const int kb = rb0 >> 5, half = (rb0 >> 4) & 1;
      const int g = lane >> 4;
#pragma unroll
      for (int i = 0; i < 4; ++i) {
        const int u = wave + 8 * i;
        if (u < 28) {
          const int T = 2 * u + (g >> 1), gp = g & 1;
          const char* p = sm + (8 * gp + ((lane >> 2) & 3)) * 1808 + T * 32 + (lane & 3) * 8;
          bf16x8 fr = tr_frag(p, p + 4 * 1808);
          const int pn = T / 7, n = T % 7;
          *(bf16x8*)(z2n + ((size_t)((pn * 64 + kb) * 7 + n) * 64 + (2 * half + gp) * 16 + (lane & 15)) * 8) = fr;
        }
      }
    }
  }
  STAMP(1, 31);
}

constexpr int K3_RS = 464;
constexpr int K3_STAGE = 64 * 256;
constexpr int K3_NB = 8;
constexpr int K3_AUX = K3_NB * K3_STAGE;
__device__ __forceinline__ void glds16_asm(const void* gsrc, unsigned lds_dst) {
  unsigned keep;
  asm volatile("s_mov_b32 %0, m0\n\ts_mov_b32 m0, %2\n\ts_nop 0\n\tglobal_load_lds_dwordx4 %1, off\n\ts_mov_b32 m0, %0"
               : "=&s"(keep) : "v"(gsrc), "s"(lds_dst) : "memory");
}
__global__ __launch_bounds__(512) void k3_kernel(const u16* __restrict__ wsp, const u16* __restrict__ bsp,
                                                  const u16* __restrict__ wphi, char* __restrict__ ws) {
  __shared__ __attribute__((aligned(16))) char sm[K3_NB * K3_STAGE + 32 * 144];
  const int tid = threadIdx.x, lane = tid & 63, wave = __builtin_amdgcn_readfirstlane(tid >> 6);
  const int b = blockIdx.x;
  const int xcd = b & 7, idx = b >> 3;
  const int mt_ = (xcd >> 1) * 8 + (idx & 7);
  const int nt_ = (xcd & 1) * 4 + (idx >> 3);
  const int t0 = mt_ * 64, f0 = nt_ * 112;
  const u16* z2n = (const u16*)(ws + WS_Z2N);
  const u16* Y = (const u16*)(ws + WS_Y);
  u16* oh = (u16*)(ws + WS_OH);
  const int g = lane >> 4, r16 = lane & 15;
  const int h = b & 15;
  u32x4 zv[2];
  float bb[2];
  STAMP(2, 0);
  const int kq = wave >> 1, wn = (wave & 1) ^ (wave >> 2);
  const u16* bp = z2n + ((size_t)(nt_ * 64 + kq) * 7 + 4 * wn) * 512 + lane * 8;
  const int arow = r16 * 256 + (((4 * kq + g) ^ r16) << 4);
  const u16* src0;
  const u16* src1;
  {
    const int row = 4 * wave + (lane >> 4);
    src0 = wsp + (size_t)(t0 + row) * S_ + (((lane & 15) ^ (row & 15)) << 3);
    src1 = src0 + (size_t)32 * S_;
  }
  const unsigned ldsb = __builtin_amdgcn_readfirstlane((unsigned)(size_t)(LDS_AS char*)sm) + wave * 1024;
#define K3_AISSUE(T)                                                                            \
  do {                                                                                          \
    glds16_asm(src0 + (T)*128, ldsb + ((T)&7) * K3_STAGE);                                      \
    glds16_asm(src1 + (T)*128, ldsb + ((T)&7) * K3_STAGE + 8192);                               \
  } while (0)
  f32x4 acc[4][4];
  bf16x8 aA[4], aB[4], b0[4], b1[4], b2[4], b3[4];
#define K3_BLOAD(BF, T, NT)                                                                     \
  do { _Pragma("unroll") for (int n = 0; n < NT; ++n) BF[n] = *(const bf16x8*)(bp + ((T)*28 + n) * 512); } while (0)
#define K3_AREAD(AF, T)                                                                         \
  do { _Pragma("unroll") for (int m = 0; m < 4; ++m) AF[m] = *(const bf16x8*)(sm + ((T)&7) * K3_STAGE + arow + m * 4096); } while (0)
#define K3_MMA(AF, BF, NT)                                                                      \
  do {                                                                                          \
    _Pragma("unroll") for (int n = 0; n < NT; ++n)                                              \
      _Pragma("unroll") for (int m = 0; m < 4; ++m) acc[n][m] = mfma16(BF[n], AF[m], acc[n][m]); \
  } while (0)
#define K3_STEP(T, AC, AN, BC, BL, NT)                                                          \
  do {                                                                                          \
    asm volatile("s_waitcnt vmcnt(12)" ::: "memory");                                           \
    BAR();                                                                                      \
    STAMP(2, 5 + (T));                                                                          \
    if ((T) + 8 < 16) K3_AISSUE((T) + 8);                                                       \
    if ((T) + 1 < 16) K3_AREAD(AN, (T) + 1);                                                    \
    if ((T) + 3 < 16) K3_BLOAD(BL, (T) + 3, NT);                                                \
    K3_MMA(AC, BC, NT);                                                                         \
    SGB(0x100, 4);                                                                              \
    _Pragma("unroll") for (int i_ = 0; i_ < NT; ++i_) { SGB(0x008, 2); SGB(0x020, 1); }         \
  } while (0)
#define K3_LOOP(NT) \
  do { \
    STAMP(2, 4); \
    K3_AREAD(aA, 0); \
    K3_STEP(0, aA, aB, b0, b3, NT); \
    K3_STEP(1, aB, aA, b1, b0, NT); \
    K3_STEP(2, aA, aB, b2, b1, NT); \
    K3_STEP(3, aB, aA, b3, b2, NT); \
    K3_STEP(4, aA, aB, b0, b3, NT); \
    K3_STEP(5, aB, aA, b1, b0, NT); \
    K3_STEP(6, aA, aB, b2, b1, NT); \
    K3_STEP(7, aB, aA, b3, b2, NT); \
    K3_STEP(8, aA, aB, b0, b3, NT); \
    K3_STEP(9, aB, aA, b1, b0, NT); \
    K3_STEP(10, aA, aB, b2, b1, NT); \
    K3_STEP(11, aB, aA, b3, b2, NT); \
    K3_STEP(12, aA, aB, b0, b3, NT); \
    _Pragma("unroll") for (int i = 0; i < 2; ++i) {                                             \
      int id = tid + 512 * i;                                                                   \
      if (id >= 896) id = tid;                                                                  \
      const int t = id / 14, c = id % 14;                                                       \
      zv[i] = *(const u32x4*)(Y + (size_t)(t0 + t) * YLD + 1344 + f0 + c * 8);                  \
      bb[i] = bf2f(bsp[t0 + t]);                                                                \
    }                                                                                           \
    K3_STEP(13, aB, aA, b1, b0, NT); \
    K3_STEP(14, aA, aB, b2, b1, NT); \
    K3_STEP(15, aB, aA, b3, b2, NT); \
  } while (0)
  u32x4 c1, c2;
  {
    f32x4 part[8];
    {
      const int te = (tid < 448) ? tid : 0;
      const f32x4* kvp = (const f32x4*)(ws + WS_KVP) + (size_t)h * 448 + te;
#pragma unroll
      for (int p = 0; p < 8; ++p) part[p] = kvp[(size_t)p * 16 * 448];
    }
    {
      const u32x4* pq = (const u32x4*)(ws + WS_PHQ) + ((size_t)(h * 128 + (b >> 4) * 8 + wave) * 2) * 64 + lane;
      c1 = pq[0];
      c2 = pq[64];
    }
    SCHED();
    K3_AISSUE(0);
    K3_AISSUE(1);
    if (wn == 0) { K3_BLOAD(b0, 0, 4); } else { K3_BLOAD(b0, 0, 3); }
    SCHED();
    char* KT = sm + K3_AUX;
    if (tid < 448) {
      const f32x4 s = ((part[0] + part[1]) + (part[2] + part[3])) + ((part[4] + part[5]) + (part[6] + part[7]));
      const int fp = tid >> 3, d0 = 4 * (tid & 7);
      const int mf = (fp >= 28) ? 1 : 0, f = fp - 28 * mf;
      const int pos = mf * 32 + ((f & 15) >> 2) * 8 + (f & 3) + 4 * (f >> 4);
      const unsigned a = pk2(s[0], s[1]), c = pk2(s[2], s[3]);
      char* d = KT + d0 * 144 + pos * 2;
      *(u16*)(d) = (u16)a; *(u16*)(d + 144) = (u16)(a >> 16);
      *(u16*)(d + 288) = (u16)c; *(u16*)(d + 432) = (u16)(c >> 16);
    } else {
      const int i = tid - 448, d = i >> 1, mf = i & 1;
      *(u32x2*)(KT + d * 144 + (mf * 32 + 28) * 2) = u32x2{0u, 0u};
    }
  }
  SCHED();
  K3_AISSUE(2);
  if (wn == 0) { K3_BLOAD(b1, 1, 4); } else { K3_BLOAD(b1, 1, 3); }
  K3_AISSUE(3);
  if (wn == 0) { K3_BLOAD(b2, 2, 4); } else { K3_BLOAD(b2, 2, 3); }
  K3_AISSUE(4); K3_AISSUE(5); K3_AISSUE(6); K3_AISSUE(7);
  asm volatile("s_waitcnt vmcnt(20)" ::: "memory");
  STAMP(2, 1);
  BAR();
  {
    const char* KT = sm + K3_AUX;
    bf16x8 af[2][2];
#pragma unroll
    for (int dt = 0; dt < 2; ++dt)
#pragma unroll
      for (int mf = 0; mf < 2; ++mf) af[dt][mf] = *(const bf16x8*)(KT + (16 * dt + r16) * 144 + (32 * mf + 8 * g) * 2);
    f32x4 o0 = {0.f, 0.f, 0.f, 0.f}, o1 = {0.f, 0.f, 0.f, 0.f};
    o0 = mfma16(af[0][0], __builtin_bit_cast(bf16x8, c1), o0);
    o1 = mfma16(af[1][0], __builtin_bit_cast(bf16x8, c1), o1);
    o0 = mfma16(af[0][1], __builtin_bit_cast(bf16x8, c2), o0);
    o1 = mfma16(af[1][1], __builtin_bit_cast(bf16x8, c2), o1);
    float den = __shfl(o1[0], 48 + r16, 64);
    den = rbf(rbf(den) + 1e-6f);
    const float inv = 1.f / den;
    const int mtile = (b >> 4) * 8 + wave;
    {
      const int k = h * 28 + 4 * g;
      u16* dst = oh + ((size_t)(mtile * 42 + (k >> 5)) * 64 + ((k >> 3) & 3) * 16 + r16) * 8 + (k & 7);
      *(u32x2*)(dst) = u32x2{pk2(rbf(o0[0]) * inv, rbf(o0[1]) * inv), pk2(rbf(o0[2]) * inv, rbf(o0[3]) * inv)};
    }
    if (g != 3) {
      const int k = h * 28 + 16 + 4 * g;
      u16* dst = oh + ((size_t)(mtile * 42 + (k >> 5)) * 64 + ((k >> 3) & 3) * 16 + r16) * 8 + (k & 7);
      *(u32x2*)(dst) = u32x2{pk2(rbf(o1[0]) * inv, rbf(o1[1]) * inv), pk2(rbf(o1[2]) * inv, rbf(o1[3]) * inv)};
    }
  }
  SCHED();
#pragma unroll
  for (int n = 0; n < 4; ++n)
#pragma unroll
    for (int m = 0; m < 4; ++m) acc[n][m] = f32x4{0.f, 0.f, 0.f, 0.f};
  if (wn == 0) K3_LOOP(4);
  else K3_LOOP(3);
  BAR();
  STAMP(2, 21);
  {
    char* rd = sm + kq * (64 * K3_RS) + r16 * K3_RS + (64 * wn + 4 * g) * 4;
#pragma unroll
    for (int n = 0; n < 4; ++n)
      if (n < 3 || wn == 0) {
#pragma unroll
        for (int m = 0; m < 4; ++m) *(f32x4*)(rd + 16 * m * K3_RS + 64 * n) = acc[n][m];
      }
  }
  BAR();
#pragma unroll
  for (int i = 0; i < 2; ++i) {
    const int id = tid + 512 * i;
    if (id < 896) {
      const int t = id / 14, c = id % 14;
      const char* p = sm + t * K3_RS + c * 32;
      f32x4 v0 = (*(const f32x4*)(p) + *(const f32x4*)(p + 64 * K3_RS)) +
                 (*(const f32x4*)(p + 128 * K3_RS) + *(const f32x4*)(p + 192 * K3_RS));
      f32x4 v1 = (*(const f32x4*)(p + 16) + *(const f32x4*)(p + 64 * K3_RS + 16)) +
                 (*(const f32x4*)(p + 128 * K3_RS + 16) + *(const f32x4*)(p + 192 * K3_RS + 16));
      u32x4 z;
#pragma unroll
      for (int j = 0; j < 4; ++j) z[j] = gelu_pk(zv[i][j]);
      u32x4 o;
      o[0] = pk2(lo_f(z[0]) * rbf(rbf(v0[0]) + bb[i]), hi_f(z[0]) * rbf(rbf(v0[1]) + bb[i]));
      o[1] = pk2(lo_f(z[1]) * rbf(rbf(v0[2]) + bb[i]), hi_f(z[1]) * rbf(rbf(v0[3]) + bb[i]));
      o[2] = pk2(lo_f(z[2]) * rbf(rbf(v1[0]) + bb[i]), hi_f(z[2]) * rbf(rbf(v1[1]) + bb[i]));
      o[3] = pk2(lo_f(z[3]) * rbf(rbf(v1[2]) + bb[i]), hi_f(z[3]) * rbf(rbf(v1[3]) + bb[i]));
      const int k = 448 + f0 + c * 8, tt = t0 + t;
      *(u32x4*)(oh + ((size_t)((tt >> 4) * 42 + (k >> 5)) * 64 + ((k >> 3) & 3) * 16 + (tt & 15)) * 8) = o;
    }
  }
  STAMP(2, 31);
}

__global__ __launch_bounds__(384) void k4_kernel(const u16* __restrict__ x, const char* __restrict__ ws,
                                                  u16* __restrict__ out) {
  __shared__ __attribute__((aligned(16))) char sm[6 * 32 * K3_RS];
  const int tid = threadIdx.x, lane = tid & 63, wave = __builtin_amdgcn_readfirstlane(tid >> 6);
  const int b = blockIdx.x;
  const int xcd = b & 7, idx = b >> 3;
  const int mt_ = xcd * 8 + (idx & 7), nt_ = idx >> 3;
  const int t0 = mt_ * 32, n0 = nt_ * 112;
  const int g = lane >> 4, r16 = lane & 15;
  STAMP(3, 0);
  const u16* ap = (const u16*)(ws + WS_OH) + ((size_t)(mt_ * 2 * 42 + wave * 7) * 64 + lane) * 8;
  const u16* bp = (const u16*)(ws + WS_WT) + ((size_t)(nt_ * 7 * 42 + wave * 7) * 64 + lane) * 8;
  bf16x8 a0[2], b0[7], a1[2], b1[7], a2[2], b2[7], a3[2], b3[7];
#define K4_LOAD(AF, BF, KS)                                                                     \
  do {                                                                                          \
    _Pragma("unroll") for (int m = 0; m < 2; ++m) AF[m] = *(const bf16x8*)(ap + (m * 42 + (KS)) * 512); \
    _Pragma("unroll") for (int n = 0; n < 7; ++n) BF[n] = *(const bf16x8*)(bp + (n * 42 + (KS)) * 512); \
  } while (0)
  K4_LOAD(a0, b0, 0);
  K4_LOAD(a1, b1, 1);
  K4_LOAD(a2, b2, 2);
  K4_LOAD(a3, b3, 3);
  u32x4 xv[2];
#pragma unroll
  for (int i = 0; i < 2; ++i) {
    int id = tid + 384 * i;
    if (id >= 448) id = tid;
    xv[i] = *(const u32x4*)(x + (size_t)(t0 + id / 14) * D_ + n0 + (id % 14) * 8);
  }
  SCHED();
  f32x4 acc[7][2];
#pragma unroll
  for (int n = 0; n < 7; ++n) { acc[n][0] = f32x4{0.f, 0.f, 0.f, 0.f}; acc[n][1] = f32x4{0.f, 0.f, 0.f, 0.f}; }
#define K4_MMA(AF, BF)                                                                          \
  do {                                                                                          \
    _Pragma("unroll") for (int n = 0; n < 7; ++n) {                                             \
      acc[n][0] = mfma16(BF[n], AF[0], acc[n][0]);                                              \
      acc[n][1] = mfma16(BF[n], AF[1], acc[n][1]);                                              \
    }                                                                                           \
    SCHED();                                                                                    \
  } while (0)
  K4_MMA(a0, b0); K4_LOAD(a0, b0, 4); SCHED();
  K4_MMA(a1, b1); K4_LOAD(a1, b1, 5); SCHED();
  K4_MMA(a2, b2); K4_LOAD(a2, b2, 6); SCHED();
  K4_MMA(a3, b3);
  K4_MMA(a0, b0);
  K4_MMA(a1, b1);
  K4_MMA(a2, b2);
  STAMP(3, 1);
  char* rd = sm + wave * (32 * K3_RS) + r16 * K3_RS + 16 * g;
#pragma unroll
  for (int n = 0; n < 7; ++n)
#pragma unroll
    for (int m = 0; m < 2; ++m) *(f32x4*)(rd + 16 * m * K3_RS + 64 * n) = acc[n][m];
  __syncthreads();
#pragma unroll
  for (int i = 0; i < 2; ++i) {
    const int id = tid + 384 * i;
    if (id < 448) {
      const int et = id / 14, ec = id % 14;
      const char* p = sm + et * K3_RS + ec * 32;
      f32x4 v0 = {0.f, 0.f, 0.f, 0.f}, v1 = {0.f, 0.f, 0.f, 0.f};
#pragma unroll
      for (int w = 0; w < 6; ++w) {
        v0 += *(const f32x4*)(p + w * 32 * K3_RS);
        v1 += *(const f32x4*)(p + w * 32 * K3_RS + 16);
      }
      u32x4 o;
      o[0] = pk2(lo_f(xv[i][0]) + v0[0], hi_f(xv[i][0]) + v0[1]);
      o[1] = pk2(lo_f(xv[i][1]) + v0[2], hi_f(xv[i][1]) + v0[3]);
      o[2] = pk2(lo_f(xv[i][2]) + v1[0], hi_f(xv[i][2]) + v1[1]);
      o[3] = pk2(lo_f(xv[i][3]) + v1[2], hi_f(xv[i][3]) + v1[3]);
      *(u32x4*)(out + (size_t)(t0 + et) * D_ + n0 + ec * 8) = o;
    }
  }
  STAMP(3, 31);
}

extern "C" void kernel_launch(void* const* d_in, const int* in_sizes, int n_in, void* d_out, int out_size,
                              void* d_ws, size_t ws_size, hipStream_t stream) {
  const u16* x = (const u16*)d_in[0];
  const u16* wqkv = (const u16*)d_in[1];
  const u16* wphi = (const u16*)d_in[2];
  const u16* wo = (const u16*)d_in[3];
  const u16* win = (const u16*)d_in[5];
  const u16* wsp = (const u16*)d_in[6];
  const u16* bsp = (const u16*)d_in[7];
  const u16* wout = (const u16*)d_in[8];
  char* ws = (char*)d_ws;
  k1_kernel<<<256, 768, 0, stream>>>(x, wqkv, wo, win, wout, wsp, ws);
  k2_kernel<<<384, 512, 0, stream>>>(wphi, ws);
  k3_kernel<<<256, 512, 0, stream>>>(wsp, bsp, wphi, ws);
  k4_kernel<<<256, 384, 0, stream>>>(x, ws, (u16*)d_out);
}
```

```cpp
#include <hip/hip_runtime.h>
#include <hip/hip_bf16.h>
#include <stdint.h>

typedef __attribute__((ext_vector_type(8))) short bf16x8;
typedef __attribute__((ext_vector_type(4))) short s16x4;
typedef __attribute__((ext_vector_type(4))) float f32x4;
typedef __attribute__((ext_vector_type(2))) float f32x2;
typedef __attribute__((ext_vector_type(4))) unsigned u32x4;
typedef __attribute__((ext_vector_type(2))) unsigned u32x2;
typedef __attribute__((ext_vector_type(2))) __bf16 bf2_t;
typedef unsigned short u16;
#define LDS_AS __attribute__((address_space(3)))

constexpr int S_ = 2048, D_ = 448, NQKV = 1344, NIN = 1792, FH = 896, KO = 1344;

__device__ __forceinline__ unsigned pk2(float a, float b) {
  f32x2 v = {a, b};
  return __builtin_bit_cast(unsigned, __builtin_convertvector(v, bf2_t));
}
__device__ __forceinline__ float lo_f(unsigned u) { return __uint_as_float(u << 16); }
__device__ __forceinline__ float hi_f(unsigned u) { return __uint_as_float(u & 0xffff0000u); }
__device__ __forceinline__ float rbf(float a) { return lo_f(pk2(a, 0.f)); }
__device__ __forceinline__ float bf2f(u16 v) { return __uint_as_float(((unsigned)v) << 16); }
__device__ __forceinline__ float dot2(unsigned a, unsigned b, float c) {
  return __builtin_amdgcn_fdot2_f32_bf16(__builtin_bit_cast(bf2_t, a), __builtin_bit_cast(bf2_t, b), c, false);
}

__device__ __forceinline__ float x16_max(float v) {
  auto r = __builtin_amdgcn_permlane16_swap(__float_as_uint(v), __float_as_uint(v), false, false);
  return fmaxf(__uint_as_float(r[0]), __uint_as_float(r[1]));
}
__device__ __forceinline__ float x32_max(float v) {
  auto r = __builtin_amdgcn_permlane32_swap(__float_as_uint(v), __float_as_uint(v), false, false);
  return fmaxf(__uint_as_float(r[0]), __uint_as_float(r[1]));
}
__device__ __forceinline__ float x16_min(float v) {
  auto r = __builtin_amdgcn_permlane16_swap(__float_as_uint(v), __float_as_uint(v), false, false);
  return fminf(__uint_as_float(r[0]), __uint_as_float(r[1]));
}
__device__ __forceinline__ float x32_min(float v) {
  auto r = __builtin_amdgcn_permlane32_swap(__float_as_uint(v), __float_as_uint(v), false, false);
  return fminf(__uint_as_float(r[0]), __uint_as_float(r[1]));
}
__device__ __forceinline__ float x16_add(float v) {
  auto r = __builtin_amdgcn_permlane16_swap(__float_as_uint(v), __float_as_uint(v), false, false);
  return __uint_as_float(r[0]) + __uint_as_float(r[1]);
}
__device__ __forceinline__ float x32_add(float v) {
  auto r = __builtin_amdgcn_permlane32_swap(__float_as_uint(v), __float_as_uint(v), false, false);
  return __uint_as_float(r[0]) + __uint_as_float(r[1]);
}
__device__ __forceinline__ float red8_add(float v) {
  v += __uint_as_float(__builtin_amdgcn_mov_dpp(__float_as_uint(v), 0xB1, 0xf, 0xf, true));
  v += __uint_as_float(__builtin_amdgcn_mov_dpp(__float_as_uint(v), 0x4E, 0xf, 0xf, true));
  v += __uint_as_float(__builtin_amdgcn_mov_dpp(__float_as_uint(v), 0x141, 0xf, 0xf, true));
  return v;
}
__device__ __forceinline__ f32x4 mfma16(bf16x8 a, bf16x8 b, f32x4 c) {
  return __builtin_amdgcn_mfma_f32_16x16x32_bf16(a, b, c, 0, 0, 0);
}
__device__ __forceinline__ unsigned gelu_pk(unsigned p) {
  const f32x2 u = {lo_f(p), hi_f(p)};
  const f32x2 c1 = {-2.3022081986f, -2.3022081986f};
  const f32x2 c2 = {-0.1029432397f, -0.1029432397f};
  f32x2 t = u * u;
  f32x2 a = u * (t * c2 + c1);
  f32x2 d = {1.f + __builtin_amdgcn_exp2f(a[0]), 1.f + __builtin_amdgcn_exp2f(a[1])};
  f32x2 r = {__builtin_amdgcn_rcpf(d[0]), __builtin_amdgcn_rcpf(d[1])};
  f32x2 o = u * r;
  return pk2(o[0], o[1]);
}
#define SCHED() __builtin_amdgcn_sched_barrier(0)
#define BAR() do { SCHED(); asm volatile("" ::: "memory"); __builtin_amdgcn_s_waitcnt(0xC07F); __builtin_amdgcn_s_barrier(); asm volatile("" ::: "memory"); SCHED(); } while (0)
__device__ __forceinline__ void glds16(const void* g, char* l) {
  __builtin_amdgcn_global_load_lds((const unsigned*)g, (LDS_AS unsigned*)l, 16, 0, 0);
}
__device__ __forceinline__ bf16x8 tr_frag(const char* p0, const char* p1) {
  s16x4 a = __builtin_amdgcn_ds_read_tr16_b64_v4i16((LDS_AS s16x4*)p0);
  s16x4 c = __builtin_amdgcn_ds_read_tr16_b64_v4i16((LDS_AS s16x4*)p1);
  return __builtin_shufflevector(a, c, 0, 1, 2, 3, 4, 5, 6, 7);
}

constexpr int YLD = 3136;
constexpr size_t WS_Y = 0;
constexpr size_t WS_Z2N = WS_Y + (size_t)S_ * YLD * 2 + 256;
constexpr size_t WS_OH = WS_Z2N + (size_t)S_ * 896 * 2;
constexpr size_t WS_WT = WS_OH + (size_t)S_ * KO * 2;
constexpr size_t WS_KVP = WS_WT + (size_t)D_ * KO * 2;
constexpr size_t WS_PHQ = WS_KVP + (size_t)8 * 16 * 56 * 32 * 4;
constexpr size_t WS_END = WS_PHQ + (size_t)16 * 128 * 2048;

#define STAMP(K, I) do { } while (0)
#define SGB(mask, n) __builtin_amdgcn_sched_group_barrier(mask, n, 0)

__device__ __forceinline__ void phi_map(bf16x8 qf, bf16x8 wp0, bf16x8 wp1, int g, unsigned (&pp)[2][2],
                                        unsigned (&pm)[2][2]) {
  f32x4 z = {0.f, 0.f, 0.f, 0.f};
  f32x4 p0 = mfma16(wp0, qf, z);
  f32x4 p1 = mfma16(wp1, qf, z);
  float v[8];
  {
    unsigned a = pk2(p0[0], p0[1]), b = pk2(p0[2], p0[3]), c = pk2(p1[0], p1[1]), d = pk2(p1[2], p1[3]);
    v[0] = lo_f(a); v[1] = hi_f(a); v[2] = lo_f(b); v[3] = hi_f(b);
    v[4] = lo_f(c); v[5] = hi_f(c); v[6] = lo_f(d); v[7] = hi_f(d);
  }
  const bool valid1 = (g != 3);
  if (!valid1) { v[4] = v[0]; v[5] = v[0]; v[6] = v[0]; v[7] = v[0]; }
  float mx = fmaxf(fmaxf(fmaxf(v[0], v[1]), fmaxf(v[2], v[3])), fmaxf(fmaxf(v[4], v[5]), fmaxf(v[6], v[7])));
  float mn = fminf(fminf(fminf(v[0], v[1]), fminf(v[2], v[3])), fminf(fminf(v[4], v[5]), fminf(v[6], v[7])));
  mx = x32_max(x16_max(mx));
  mn = x32_min(x16_min(mn));
  const float L2E = 1.4426950408889634f;
  const float mxl = mx * L2E, mnl = mn * L2E;
  float ep[8], em[8];
  float sp = 0.f, sm = 0.f;
#pragma unroll
  for (int j = 0; j < 8; ++j) {
    ep[j] = __builtin_amdgcn_exp2f(__builtin_fmaf(v[j], L2E, -mxl));
    em[j] = __builtin_amdgcn_exp2f(__builtin_fmaf(v[j], -L2E, mnl));
    if (j >= 4) { ep[j] = valid1 ? ep[j] : 0.f; em[j] = valid1 ? em[j] : 0.f; }
    sp += ep[j];
    sm += em[j];
  }
  sp = x32_add(x16_add(sp));
  sm = x32_add(x16_add(sm));
  const float rp = __builtin_amdgcn_rcpf(sp), rm = __builtin_amdgcn_rcpf(sm);
  pp[0][0] = pk2(ep[0] * rp, ep[1] * rp); pp[0][1] = pk2(ep[2] * rp, ep[3] * rp);
  pp[1][0] = pk2(ep[4] * rp, ep[5] * rp); pp[1][1] = pk2(ep[6] * rp, ep[7] * rp);
  pm[0][0] = pk2(em[0] * rm, em[1] * rm); pm[0][1] = pk2(em[2] * rm, em[3] * rm);
  pm[1][0] = pk2(em[4] * rm, em[5] * rm); pm[1][1] = pk2(em[6] * rm, em[7] * rm);
}

constexpr int K1_XST = 128 * 128;
constexpr int K1_STAGE = K1_XST + 64 * 448;
constexpr int K1_LDS = 3 * K1_STAGE;

__global__ __launch_bounds__(768) void k1_kernel(const u16* __restrict__ x, const u16* __restrict__ wqkv,
                                                  const u16* __restrict__ wo, const u16* __restrict__ win,
                                                  const u16* __restrict__ wout, const u16* __restrict__ wsp,
                                                  char* __restrict__ ws) {
  __shared__ __attribute__((aligned(16))) char sm[K1_LDS];
  const int tid = threadIdx.x, lane = tid & 63, wave = __builtin_amdgcn_readfirstlane(tid >> 6);
  const int b = blockIdx.x;
  STAMP(0, 0);
  if (b >= 224) {
    const int kb = b - 224;
    const int k0 = kb * 64;
    if (kb < 21) {
    u16* T = (u16*)sm;
    constexpr int TS = 456;
    u32x4 v[5];
#pragma unroll
    for (int j = 0; j < 5; ++j) {
      int i = tid + 768 * j;
      if (i >= 64 * 56) i = tid;
      const int r = i / 56, c = i % 56;
      const int k = k0 + r;
      const u16* src = (k < 448) ? (wo + (size_t)k * 448) : (wout + (size_t)(k - 448) * 448);
      v[j] = *(const u32x4*)(src + c * 8);
    }
#pragma unroll
    for (int j = 0; j < 5; ++j) {
      const int i = tid + 768 * j;
      if (i < 64 * 56) *(u32x4*)(T + (i / 56) * TS + (i % 56) * 8) = v[j];
    }
    __syncthreads();
    u16* wT = (u16*)(ws + WS_WT);
    for (int i = tid; i < 448 * 8; i += 768) {
      const int n = i >> 3, c = i & 7;
      u16 t[8];
#pragma unroll
      for (int j = 0; j < 8; ++j) t[j] = T[(c * 8 + j) * TS + n];
      u32x4 o;
#pragma unroll
      for (int j = 0; j < 4; ++j) o[j] = (unsigned)t[2 * j] | ((unsigned)t[2 * j + 1] << 16);
      *(u32x4*)(wT + ((size_t)((n >> 4) * 42 + (k0 >> 5) + (c >> 2)) * 64 + (c & 3) * 16 + (n & 15)) * 8) = o;
    }
    }
    STAMP(0, 1);
    {
      const char* p = (const char*)wsp + (size_t)kb * 262144 + tid * 16;
      unsigned a = 0;
#pragma unroll
      for (int i = 0; i < 21; ++i) {
        u32x4 v = *(const u32x4*)(p + i * 12288);
        a |= v[0] & v[1] & v[2] & v[3];
      }
      asm volatile("" ::"v"(a));
    }
    STAMP(0, 31);
    return;
  }

  const int xcd = b & 7, idx = b >> 3;
  const int cb = (xcd & 1) * 7 + idx % 7;
  const int rt = (xcd >> 1) * 4 + idx / 7;
  const int row0 = rt * 128;
  const int g = lane >> 4, r16 = lane & 15;
  const int wm = (wave >> 1) & 3, wn = wave & 1;
  constexpr int RST = 464;

  if (wave >= 8) {
    const int lw = wave - 8;
    const u16* W = (cb < 6) ? (wqkv + 224 * cb) : (win + 224 * (cb - 6));
    const int ldw = (cb < 6) ? NQKV : NIN;
    const u16* src[11];
#pragma unroll
    for (int j = 0; j < 11; ++j) {
      const int i = lw + 4 * j;
      if (j < 4) {
        const int row = 8 * i + (lane >> 3);
        src[j] = x + (size_t)(row0 + row) * D_ + (((lane & 7) ^ ((row >> 1) & 7)) << 3);
      } else {
        const int o = (i - 16) * 1024 + lane * 16;
        const int k = o / 448, cp = (o % 448) >> 4;
        const int G = (cp >> 1) ^ ((k >> 3) & 1);
        src[j] = W + (size_t)k * ldw + 8 * (2 * G + (cp & 1));
      }
    }
    const int wstep = 64 * ldw;
    char* dst0 = sm + lw * 1024;
#pragma unroll
    for (int s = 0; s < 2; ++s)
#pragma unroll
      for (int j = 0; j < 11; ++j) glds16(src[j] + s * (j < 4 ? 64 : wstep), dst0 + s * K1_STAGE + j * 4096);
    int nb = 2;
#pragma unroll 1
    for (int t = 0; t < 7; ++t) {
      if (t < 6) asm volatile("s_waitcnt vmcnt(11)" ::: "memory");
      else asm volatile("s_waitcnt vmcnt(0)" ::: "memory");
      __builtin_amdgcn_s_barrier();
      if (t + 2 < 7) {
        char* dst = dst0 + nb * K1_STAGE;
#pragma unroll
        for (int j = 0; j < 11; ++j) glds16(src[j] + (t + 2) * (j < 4 ? 64 : wstep), dst + j * 4096);
        nb = (nb == 2) ? 0 : nb + 1;
      }
    }
    __builtin_amdgcn_s_barrier();
    __builtin_amdgcn_s_barrier();
  } else {
    f32x4 acc[7][2], sacc = {0.f, 0.f, 0.f, 0.f}, qacc = {0.f, 0.f, 0.f, 0.f}, cacc[2];
#pragma unroll
    for (int nt = 0; nt < 7; ++nt) { acc[nt][0] = f32x4{0.f, 0.f, 0.f, 0.f}; acc[nt][1] = f32x4{0.f, 0.f, 0.f, 0.f}; }
    cacc[0] = f32x4{0.f, 0.f, 0.f, 0.f}; cacc[1] = f32x4{0.f, 0.f, 0.f, 0.f};
    const bf16x8 ones = {0x3F80, 0x3F80, 0x3F80, 0x3F80, 0x3F80, 0x3F80, 0x3F80, 0x3F80};
    const int sw = (r16 >> 1) & 7;
    const int xrow = (32 * wm + 16 * wn + r16) * 128, xd = wn ? -2048 : 2048;
    const int xo0 = xrow + ((g ^ sw) << 4), xo1 = xrow + (((4 | g) ^ sw) << 4);
    int woff[7];
#pragma unroll
    for (int i = 0; i < 7; ++i) {
      int nt = i + 2 * wm;
      nt = (nt >= 7) ? nt - 7 : nt;
      const int G = 7 * wn + nt;
      woff[i] = K1_XST + (8 * g + ((lane >> 2) & 3)) * 448 + (lane & 3) * 8 + ((G ^ (g & 1)) << 5);
    }
    bf16x8 xa[2], wa[7], xb[2], wb[7];
#define K1_READ(XF, WF, SB, KS)                                                              \
  do {                                                                                       \
    XF[0] = *(const bf16x8*)((SB) + ((KS) ? xo1 : xo0));                                     \
    XF[1] = *(const bf16x8*)((SB) + ((KS) ? xo1 : xo0) + xd);                                \
    _Pragma("unroll") for (int nt = 0; nt < 7; ++nt) {                                       \
      const char* p_ = (SB) + woff[nt] + (KS)*32 * 448;                                      \
      WF[nt] = tr_frag(p_, p_ + 4 * 448);                                                    \
    }                                                                                        \
  } while (0)
#define K1_MMA(XF, WF)                                                                       \
  do {                                                                                       \
    _Pragma("unroll") for (int nt = 0; nt < 7; ++nt) acc[nt][0] = mfma16(WF[nt], XF[0], acc[nt][0]); \
    sacc = mfma16(ones, XF[0], sacc);                                                        \
    qacc = mfma16(XF[0], XF[0], qacc);                                                       \
    _Pragma("unroll") for (int nt = 0; nt < 7; ++nt) acc[nt][1] = mfma16(WF[nt], XF[1], acc[nt][1]); \
    cacc[0] = mfma16(WF[0], ones, cacc[0]);                                                  \
    cacc[1] = mfma16(WF[1], ones, cacc[1]);                                                  \
  } while (0)
    int rb = 0;
#pragma unroll 1
    for (int t = 0; t < 7; ++t) {
      BAR();
      STAMP(0, 3 + t);
      const char* sb = sm + rb * K1_STAGE;
      rb = (rb == 2) ? 0 : rb + 1;
      K1_READ(xa, wa, sb, 0);
      SCHED();
      K1_READ(xb, wb, sb, 1);
      K1_MMA(xa, wa);
      _Pragma("unroll") for (int i_ = 0; i_ < 8; ++i_) { SGB(0x008, 1); SGB(0x100, 2); }
      SCHED();
      K1_MMA(xb, wb);
    }
    BAR();
    STAMP(0, 17);
    float* CS = (float*)(sm + 128 * RST);
    f32x2* RS_ = (f32x2*)(CS + 224);
    if (r16 == 0) {
#pragma unroll
      for (int i = 0; i < 2; ++i) {
        int nt = i + 2 * wm;
        nt = (nt >= 7) ? nt - 7 : nt;
        *(f32x4*)(CS + 112 * wn + 16 * nt + 4 * g) = cacc[i];
      }
    }
    {
      const float qd = (r16 & 3) == 0 ? qacc[0] : (r16 & 3) == 1 ? qacc[1] : (r16 & 3) == 2 ? qacc[2] : qacc[3];
      if (g == (r16 >> 2)) {
        const float mean = sacc[0] * (1.f / 448.f);
        const float var = fmaxf(qd * (1.f / 448.f) - mean * mean, 0.f);
        const float sd = rbf(sqrtf(rbf(rbf(var) + 1e-5f)));
        const float r = 1.f / sd;
        RS_[32 * wm + 16 * wn + r16] = f32x2{r, r * rbf(mean)};
      }
    }
    BAR();
    f32x2 st[2];
    st[0] = RS_[32 * wm + 16 * wn + r16];
    st[1] = RS_[32 * wm + 16 * (wn ^ 1) + r16];
#pragma unroll
    for (int i = 0; i < 7; ++i) {
      int nt = i + 2 * wm;
      nt = (nt >= 7) ? nt - 7 : nt;
      const f32x4 c4 = *(const f32x4*)(CS + 112 * wn + 16 * nt + 4 * g);
#pragma unroll
      for (int j = 0; j < 2; ++j) {
        char* dst = sm + (32 * wm + 16 * (j ^ wn) + r16) * RST + (112 * wn + 4 * g) * 2 + 32 * nt;
        const f32x4 v = acc[i][j] * st[j][0] - c4 * st[j][1];
        *(u32x2*)(dst) = u32x2{pk2(v[0], v[1]), pk2(v[2], v[3])};
      }
    }
  }
  __syncthreads();
  u16* dstb = (u16*)(ws + WS_Y) + (size_t)row0 * YLD + 224 * cb;
#pragma unroll
  for (int i = 0; i < 5; ++i) {
    const int id = tid + 768 * i;
    if (id < 128 * 28) {
      const int s = id / 28, c = id % 28;
      *(u32x4*)(dstb + (size_t)s * YLD + c * 8) = *(const u32x4*)(sm + s * RST + c * 16);
    }
  }
  STAMP(0, 31);
}

__device__ __forceinline__ float red64_add(float v) {
  v = red8_add(v);
  v += __uint_as_float(__builtin_amdgcn_mov_dpp(__float_as_uint(v), 0x140, 0xf, 0xf, true));
  return x32_add(x16_add(v));
}

constexpr int K2_PW = 64 * 80 + 32 * 64;
__global__ __launch_bounds__(512) void k2_kernel(const u16* __restrict__ wphi, char* __restrict__ ws) {
  __shared__ __attribute__((aligned(16))) char sm[8 * 56 * 32 * 4 + 2048];
  const int tid = threadIdx.x, lane = tid & 63, wave = __builtin_amdgcn_readfirstlane(tid >> 6);
  const int b = blockIdx.x;
  const u16* Y = (const u16*)(ws + WS_Y);
  STAMP(1, 0);
  if (b < 128) {
    const int h = b & 15, rg = b >> 4;
    const int g = lane >> 4, r16 = lane & 15;
    const int rowb = rg * 256 + wave * 32;
    u32x2 kv_[2][2];
#pragma unroll
    for (int st = 0; st < 2; ++st) {
      const u16* kp = Y + (size_t)(rowb + st * 16 + r16) * YLD + 448 + h * 28 + 8 * g;
      kv_[st][0] = *(const u32x2*)(kp);
      kv_[st][1] = *(const u32x2*)(kp + 4);
    }
    u32x2 vv[4];
#pragma unroll
    for (int i = 0; i < 4; ++i) {
      const int row = (lane >> 3) + 8 * i, pc = lane & 7;
      vv[i] = *(const u32x2*)(Y + (size_t)(rowb + row) * YLD + 896 + h * 28 + 4 * (pc < 7 ? pc : 0));
      if (pc == 7) vv[i] = u32x2{0x00003F80u, 0u};
    }
    u32x4 wraw = {0u, 0u, 0u, 0u};
    if (tid < 98) wraw = *(const u32x4*)(wphi + (size_t)h * 784 + tid * 8);
    char* WP = sm + 8 * 56 * 32 * 4;
    if (tid < 98) *(u32x4*)(WP + tid * 16) = wraw;
    char* PK = sm + wave * K2_PW;
    char* VS = PK + 64 * 80;
    __syncthreads();
    STAMP(1, 1);
    const char* wp = WP + (8 * g + ((lane >> 2) & 3)) * 56 + (lane & 3) * 8;
    bf16x8 wp0 = tr_frag(wp, wp + 4 * 56);
    bf16x8 wp1 = tr_frag(wp + 32, wp + 4 * 56 + 32);
    if (g == 3) {
#pragma unroll
      for (int j = 4; j < 8; ++j) { wp0[j] = 0; wp1[j] = 0; }
    }
    f32x4 kacc[4][2];
#pragma unroll
    for (int a = 0; a < 4; ++a) { kacc[a][0] = f32x4{0.f, 0.f, 0.f, 0.f}; kacc[a][1] = f32x4{0.f, 0.f, 0.f, 0.f}; }
#pragma unroll
    for (int pr = 0; pr < 1; ++pr) {
#pragma unroll
      for (int i = 0; i < 4; ++i) {
        const int row = (lane >> 3) + 8 * i;
        const int slot = 2 * (row & 15) + (row >> 4);
        *(u32x2*)(VS + slot * 64 + (lane & 7) * 8) = vv[4 * pr + i];
      }
      unsigned pp[2][2][2], pm[2][2][2];
#pragma unroll
      for (int mt = 0; mt < 2; ++mt) {
        u32x4 q4 = {kv_[2 * pr + mt][0][0], kv_[2 * pr + mt][0][1], kv_[2 * pr + mt][1][0], kv_[2 * pr + mt][1][1]};
        if (g == 3) { q4[2] = 0u; q4[3] = 0u; }
        phi_map(__builtin_bit_cast(bf16x8, q4), wp0, wp1, g, pp[mt], pm[mt]);
      }
#pragma unroll
      for (int ft = 0; ft < 2; ++ft) {
        if (ft == 0 || g != 3) {
          char* d0 = PK + (16 * ft + 4 * g) * 80 + r16 * 4;
#pragma unroll
          for (int j = 0; j < 2; ++j) {
            *(unsigned*)(d0 + (2 * j) * 80) = (pp[0][ft][j] & 0xffffu) | (pp[1][ft][j] << 16);
            *(unsigned*)(d0 + (2 * j + 1) * 80) = (pp[0][ft][j] >> 16) | (pp[1][ft][j] & 0xffff0000u);
            *(unsigned*)(d0 + (28 + 2 * j) * 80) = (pm[0][ft][j] & 0xffffu) | (pm[1][ft][j] << 16);
            *(unsigned*)(d0 + (28 + 2 * j + 1) * 80) = (pm[0][ft][j] >> 16) | (pm[1][ft][j] & 0xffff0000u);
          }
        }
      }
      __builtin_amdgcn_s_waitcnt(0xC07F);
      __builtin_amdgcn_wave_barrier();
      bf16x8 bfr[2];
      {
        const char* vp = VS + (8 * g + ((lane >> 2) & 3)) * 64 + (lane & 3) * 8;
        bfr[0] = tr_frag(vp, vp + 4 * 64);
        bfr[1] = tr_frag(vp + 32, vp + 4 * 64 + 32);
      }
#pragma unroll
      for (int a = 0; a < 4; ++a) {
        bf16x8 af = *(const bf16x8*)(PK + (16 * a + r16) * 80 + g * 16);
        kacc[a][0] = mfma16(af, bfr[0], kacc[a][0]);
        kacc[a][1] = mfma16(af, bfr[1], kacc[a][1]);
      }
      __builtin_amdgcn_s_waitcnt(0xC07F);
      __builtin_amdgcn_wave_barrier();
    }
    STAMP(1, 2);
    __syncthreads();
    float* red = (float*)sm + wave * (56 * 32);
#pragma unroll
    for (int a = 0; a < 4; ++a)
#pragma unroll
      for (int c = 0; c < 2; ++c)
#pragma unroll
        for (int j = 0; j < 4; ++j) {
          const int f = 16 * a + 4 * g + j;
          if (f < 56) red[f * 32 + 16 * c + r16] = kacc[a][c][j];
        }
    __syncthreads();
    if (tid < 448) {
      const f32x4* rp = (const f32x4*)sm + tid;
      f32x4 s = (rp[0] + rp[448]) + (rp[2 * 448] + rp[3 * 448]);
      s += (rp[4 * 448] + rp[5 * 448]) + (rp[6 * 448] + rp[7 * 448]);
      *((f32x4*)(ws + WS_KVP) + (size_t)(rg * 16 + h) * 448 + tid) = s;
    }
  } else if (b >= 256) {
    const int h = b & 15, rg = (b - 256) >> 4;
    const int g = lane >> 4, r16 = lane & 15;
    u32x2 qv[2][2];
#pragma unroll
    for (int st = 0; st < 2; ++st) {
      const u16* qp = Y + (size_t)(rg * 256 + wave * 32 + st * 16 + r16) * YLD + h * 28 + 8 * g;
      qv[st][0] = *(const u32x2*)(qp);
      qv[st][1] = *(const u32x2*)(qp + 4);
    }
    u32x4 wraw = {0u, 0u, 0u, 0u};
    if (tid < 98) wraw = *(const u32x4*)(wphi + (size_t)h * 784 + tid * 8);
    if (tid < 98) *(u32x4*)(sm + tid * 16) = wraw;
    __syncthreads();
    const char* wp = sm + (8 * g + ((lane >> 2) & 3)) * 56 + (lane & 3) * 8;
    bf16x8 wp0 = tr_frag(wp, wp + 4 * 56);
    bf16x8 wp1 = tr_frag(wp + 32, wp + 4 * 56 + 32);
    if (g == 3) {
#pragma unroll
      for (int j = 4; j < 8; ++j) { wp0[j] = 0; wp1[j] = 0; }
    }
#pragma unroll
    for (int st = 0; st < 2; ++st) {
      u32x4 q4 = {qv[st][0][0], qv[st][0][1], qv[st][1][0], qv[st][1][1]};
      if (g == 3) { q4[2] = 0u; q4[3] = 0u; }
      unsigned pp[2][2], pm[2][2];
      phi_map(__builtin_bit_cast(bf16x8, q4), wp0, wp1, g, pp, pm);
      u32x4* dst = (u32x4*)(ws + WS_PHQ) + ((size_t)(h * 128 + rg * 16 + wave * 2 + st) * 2) * 64 + lane;
      dst[0] = u32x4{pp[0][0], pp[0][1], pp[1][0], pp[1][1]};
      dst[64] = u32x4{pm[0][0], pm[0][1], pm[1][0], pm[1][1]};
    }
  } else {
    const int row0 = (b - 128) * 16 + wave * 2;
    u16* z2n = (u16*)(ws + WS_Z2N);
    const int le = (lane < 56) ? lane : 0;
    u32x4 v[2][2];
#pragma unroll
    for (int r = 0; r < 2; ++r)
#pragma unroll
      for (int c = 0; c < 2; ++c) v[r][c] = *(const u32x4*)(Y + (size_t)(row0 + r) * YLD + 2240 + (le + 56 * c) * 8);
#pragma unroll
    for (int r = 0; r < 2; ++r) {
      float s = 0.f;
#pragma unroll
      for (int c = 0; c < 2; ++c)
#pragma unroll
        for (int j = 0; j < 4; ++j) {
          v[r][c][j] = gelu_pk(v[r][c][j]);
          s = dot2(v[r][c][j], 0x3F803F80u, s);
        }
      if (lane >= 56) s = 0.f;
      const float mean = red64_add(s) * (1.f / 896.f);
      const float mb = rbf(mean);
      float q = 0.f;
#pragma unroll
      for (int c = 0; c < 2; ++c)
#pragma unroll
        for (int j = 0; j < 4; ++j) {
          const float a = lo_f(v[r][c][j]) - mean, e = hi_f(v[r][c][j]) - mean;
          q += a * a + e * e;
          v[r][c][j] = pk2(lo_f(v[r][c][j]) - mb, hi_f(v[r][c][j]) - mb);
        }
      if (lane >= 56) q = 0.f;
      const float var = red64_add(q) * (1.f / 896.f);
      const float sd = rbf(sqrtf(rbf(rbf(var) + 1e-5f)));
      const float inv = 1.f / sd;
      if (lane < 56) {
#pragma unroll
        for (int c = 0; c < 2; ++c) {
          u32x4 o;
#pragma unroll
          for (int j = 0; j < 4; ++j) o[j] = pk2(lo_f(v[r][c][j]) * inv, hi_f(v[r][c][j]) * inv);
          *(u32x4*)(sm + (wave * 2 + r) * 1808 + (lane + 56 * c) * 16) = o;
        }
      }
    }
    __syncthreads();
    {
      const int rb0 = (b - 128) * 16;
      const int kb = rb0 >> 5, half = (rb0 >> 4) & 1;
      const int g = lane >> 4;
#pragma unroll
      for (int i = 0; i < 4; ++i) {
        const int u = wave + 8 * i;
        if (u < 28) {
          const int T = 2 * u + (g >> 1), gp = g & 1;
          const char* p = sm + (8 * gp + ((lane >> 2) & 3)) * 1808 + T * 32 + (lane & 3) * 8;
          bf16x8 fr = tr_frag(p, p + 4 * 1808);
          const int pn = T / 7, n = T % 7;
          *(bf16x8*)(z2n + ((size_t)((pn * 64 + kb) * 7 + n) * 64 + (2 * half + gp) * 16 + (lane & 15)) * 8) = fr;
        }
      }
    }
  }
  STAMP(1, 31);
}

constexpr int K3_RS = 464;
constexpr int K3_STAGE = 64 * 256;
constexpr int K3_NB = 8;
constexpr int K3_AUX = K3_NB * K3_STAGE;
__device__ __forceinline__ void glds16_asm(const void* gsrc, unsigned lds_dst) {
  unsigned keep;
  asm volatile("s_mov_b32 %0, m0\n\ts_mov_b32 m0, %2\n\ts_nop 0\n\tglobal_load_lds_dwordx4 %1, off\n\ts_mov_b32 m0, %0"
               : "=&s"(keep) : "v"(gsrc), "s"(lds_dst) : "memory");
}
__global__ __launch_bounds__(512) void k3_kernel(const u16* __restrict__ wsp, const u16* __restrict__ bsp,
                                                  const u16* __restrict__ wphi, char* __restrict__ ws) {
  __shared__ __attribute__((aligned(16))) char sm[K3_NB * K3_STAGE + 32 * 144];
  const int tid = threadIdx.x, lane = tid & 63, wave = __builtin_amdgcn_readfirstlane(tid >> 6);
  const int b = blockIdx.x;
  const int xcd = b & 7, idx = b >> 3;
  const int mt_ = (xcd >> 1) * 8 + (idx & 7);
  const int nt_ = (xcd & 1) * 4 + (idx >> 3);
  const int t0 = mt_ * 64, f0 = nt_ * 112;
  const u16* z2n = (const u16*)(ws + WS_Z2N);
  const u16* Y = (const u16*)(ws + WS_Y);
  u16* oh = (u16*)(ws + WS_OH);
  const int g = lane >> 4, r16 = lane & 15;
  const int h = b & 15;
  u32x4 zv[2];
  float bb[2];
  STAMP(2, 0);
  const int kq = wave >> 1, wn = (wave & 1) ^ (wave >> 2);
  const u16* bp = z2n + ((size_t)(nt_ * 64 + kq) * 7 + 4 * wn) * 512 + lane * 8;
  const int arow = r16 * 256 + (((4 * kq + g) ^ r16) << 4);
  const u16* src0;
  const u16* src1;
  {
    const int row = 4 * wave + (lane >> 4);
    src0 = wsp + (size_t)(t0 + row) * S_ + (((lane & 15) ^ (row & 15)) << 3);
    src1 = src0 + (size_t)32 * S_;
  }
  const unsigned ldsb = __builtin_amdgcn_readfirstlane((unsigned)(size_t)(LDS_AS char*)sm) + wave * 1024;
#define K3_AISSUE(T)                                                                            \
  do {                                                                                          \
    glds16_asm(src0 + (T)*128, ldsb + ((T)&7) * K3_STAGE);                                      \
    glds16_asm(src1 + (T)*128, ldsb + ((T)&7) * K3_STAGE + 8192);                               \
  } while (0)
  f32x4 acc[4][4];
  bf16x8 aA[4], aB[4], b0[4], b1[4], b2[4], b3[4];
#define K3_BLOAD(BF, T, NT)                                                                     \
  do { _Pragma("unroll") for (int n = 0; n < NT; ++n) BF[n] = *(const bf16x8*)(bp + ((T)*28 + n) * 512); } while (0)
#define K3_AREAD(AF, T)                                                                         \
  do { _Pragma("unroll") for (int m = 0; m < 4; ++m) AF[m] = *(const bf16x8*)(sm + ((T)&7) * K3_STAGE + arow + m * 4096); } while (0)
#define K3_MMA(AF, BF, NT)                                                                      \
  do {                                                                                          \
    _Pragma("unroll") for (int n = 0; n < NT; ++n)                                              \
      _Pragma("unroll") for (int m = 0; m < 4; ++m) acc[n][m] = mfma16(BF[n], AF[m], acc[n][m]); \
  } while (0)
#define K3_STEP(T, AC, AN, BC, BL, NT)                                                          \
  do {                                                                                          \
    asm volatile("s_waitcnt vmcnt(12)" ::: "memory");                                           \
    BAR();                                                                                      \
    STAMP(2, 5 + (T));                                                                          \
    if ((T) + 8 < 16) K3_AISSUE((T) + 8);                                                       \
    if ((T) + 1 < 16) K3_AREAD(AN, (T) + 1);                                                    \
    if ((T) + 3 < 16) K3_BLOAD(BL, (T) + 3, NT);                                                \
    K3_MMA(AC, BC, NT);                                                                         \
    SGB(0x100, 4);                                                                              \
    _Pragma("unroll") for (int i_ = 0; i_ < NT; ++i_) { SGB(0x008, 2); SGB(0x020, 1); }         \
  } while (0)
#define K3_LOOP(NT) \
  do { \
    STAMP(2, 4); \
    K3_AREAD(aA, 0); \
    K3_STEP(0, aA, aB, b0, b3, NT); \
    K3_STEP(1, aB, aA, b1, b0, NT); \
    K3_STEP(2, aA, aB, b2, b1, NT); \
    K3_STEP(3, aB, aA, b3, b2, NT); \
    K3_STEP(4, aA, aB, b0, b3, NT); \
    K3_STEP(5, aB, aA, b1, b0, NT); \
    K3_STEP(6, aA, aB, b2, b1, NT); \
    K3_STEP(7, aB, aA, b3, b2, NT); \
    K3_STEP(8, aA, aB, b0, b3, NT); \
    K3_STEP(9, aB, aA, b1, b0, NT); \
    K3_STEP(10, aA, aB, b2, b1, NT); \
    K3_STEP(11, aB, aA, b3, b2, NT); \
    K3_STEP(12, aA, aB, b0, b3, NT); \
    _Pragma("unroll") for (int i = 0; i < 2; ++i) {                                             \
      int id = tid + 512 * i;                                                                   \
      if (id >= 896) id = tid;                                                                  \
      const int t = id / 14, c = id % 14;                                                       \
      zv[i] = *(const u32x4*)(Y + (size_t)(t0 + t) * YLD + 1344 + f0 + c * 8);                  \
      bb[i] = bf2f(bsp[t0 + t]);                                                                \
    }                                                                                           \
    K3_STEP(13, aB, aA, b1, b0, NT); \
    K3_STEP(14, aA, aB, b2, b1, NT); \
    K3_STEP(15, aB, aA, b3, b2, NT); \
  } while (0)
  u32x4 c1, c2;
  {
    f32x4 part[8];
    {
      const int te = (tid < 448) ? tid : 0;
      const f32x4* kvp = (const f32x4*)(ws + WS_KVP) + (size_t)h * 448 + te;
#pragma unroll
      for (int p = 0; p < 8; ++p) part[p] = kvp[(size_t)p * 16 * 448];
    }
    {
      const u32x4* pq = (const u32x4*)(ws + WS_PHQ) + ((size_t)(h * 128 + (b >> 4) * 8 + wave) * 2) * 64 + lane;
      c1 = pq[0];
      c2 = pq[64];
    }
    SCHED();
    K3_AISSUE(0);
    K3_AISSUE(1);
    if (wn == 0) { K3_BLOAD(b0, 0, 4); } else { K3_BLOAD(b0, 0, 3); }
    SCHED();
    char* KT = sm + K3_AUX;
    if (tid < 448) {
      const f32x4 s = ((part[0] + part[1]) + (part[2] + part[3])) + ((part[4] + part[5]) + (part[6] + part[7]));
      const int fp = tid >> 3, d0 = 4 * (tid & 7);
      const int mf = (fp >= 28) ? 1 : 0, f = fp - 28 * mf;
      const int pos = mf * 32 + ((f & 15) >> 2) * 8 + (f & 3) + 4 * (f >> 4);
      const unsigned a = pk2(s[0], s[1]), c = pk2(s[2], s[3]);
      char* d = KT + d0 * 144 + pos * 2;
      *(u16*)(d) = (u16)a; *(u16*)(d + 144) = (u16)(a >> 16);
      *(u16*)(d + 288) = (u16)c; *(u16*)(d + 432) = (u16)(c >> 16);
    } else {
      const int i = tid - 448, d = i >> 1, mf = i & 1;
      *(u32x2*)(KT + d * 144 + (mf * 32 + 28) * 2) = u32x2{0u, 0u};
    }
  }
  SCHED();
  K3_AISSUE(2);
  if (wn == 0) { K3_BLOAD(b1, 1, 4); } else { K3_BLOAD(b1, 1, 3); }
  K3_AISSUE(3);
  if (wn == 0) { K3_BLOAD(b2, 2, 4); } else { K3_BLOAD(b2, 2, 3); }
  K3_AISSUE(4); K3_AISSUE(5); K3_AISSUE(6); K3_AISSUE(7);
  asm volatile("s_waitcnt vmcnt(20)" ::: "memory");
  STAMP(2, 1);
  BAR();
  {
    const char* KT = sm + K3_AUX;
    bf16x8 af[2][2];
#pragma unroll
    for (int dt = 0; dt < 2; ++dt)
#pragma unroll
      for (int mf = 0; mf < 2; ++mf) af[dt][mf] = *(const bf16x8*)(KT + (16 * dt + r16) * 144 + (32 * mf + 8 * g) * 2);
    f32x4 o0 = {0.f, 0.f, 0.f, 0.f}, o1 = {0.f, 0.f, 0.f, 0.f};
    o0 = mfma16(af[0][0], __builtin_bit_cast(bf16x8, c1), o0);
    o1 = mfma16(af[1][0], __builtin_bit_cast(bf16x8, c1), o1);
    o0 = mfma16(af[0][1], __builtin_bit_cast(bf16x8, c2), o0);
    o1 = mfma16(af[1][1], __builtin_bit_cast(bf16x8, c2), o1);
    float den = __shfl(o1[0], 48 + r16, 64);
    den = rbf(rbf(den) + 1e-6f);
    const float inv = 1.f / den;
    const int mtile = (b >> 4) * 8 + wave;
    {
      const int k = h * 28 + 4 * g;
      u16* dst = oh + ((size_t)(mtile * 42 + (k >> 5)) * 64 + ((k >> 3) & 3) * 16 + r16) * 8 + (k & 7);
      *(u32x2*)(dst) = u32x2{pk2(rbf(o0[0]) * inv, rbf(o0[1]) * inv), pk2(rbf(o0[2]) * inv, rbf(o0[3]) * inv)};
    }
    if (g != 3) {
      const int k = h * 28 + 16 + 4 * g;
      u16* dst = oh + ((size_t)(mtile * 42 + (k >> 5)) * 64 + ((k >> 3) & 3) * 16 + r16) * 8 + (k & 7);
      *(u32x2*)(dst) = u32x2{pk2(rbf(o1[0]) * inv, rbf(o1[1]) * inv), pk2(rbf(o1[2]) * inv, rbf(o1[3]) * inv)};
    }
  }
  SCHED();
#pragma unroll
  for (int n = 0; n < 4; ++n)
#pragma unroll
    for (int m = 0; m < 4; ++m) acc[n][m] = f32x4{0.f, 0.f, 0.f, 0.f};
  if (wn == 0) K3_LOOP(4);
  else K3_LOOP(3);
  BAR();
  STAMP(2, 21);
  {
    char* rd = sm + kq * (64 * K3_RS) + r16 * K3_RS + (64 * wn + 4 * g) * 4;
#pragma unroll
    for (int n = 0; n < 4; ++n)
      if (n < 3 || wn == 0) {
#pragma unroll
        for (int m = 0; m < 4; ++m) *(f32x4*)(rd + 16 * m * K3_RS + 64 * n) = acc[n][m];
      }
  }
  BAR();
#pragma unroll
  for (int i = 0; i < 2; ++i) {
    const int id = tid + 512 * i;
    if (id < 896) {
      const int t = id / 14, c = id % 14;
      const char* p = sm + t * K3_RS + c * 32;
      f32x4 v0 = (*(const f32x4*)(p) + *(const f32x4*)(p + 64 * K3_RS)) +
                 (*(const f32x4*)(p + 128 * K3_RS) + *(const f32x4*)(p + 192 * K3_RS));
      f32x4 v1 = (*(const f32x4*)(p + 16) + *(const f32x4*)(p + 64 * K3_RS + 16)) +
                 (*(const f32x4*)(p + 128 * K3_RS + 16) + *(const f32x4*)(p + 192 * K3_RS + 16));
      u32x4 z;
#pragma unroll
      for (int j = 0; j < 4; ++j) z[j] = gelu_pk(zv[i][j]);
      u32x4 o;
      o[0] = pk2(lo_f(z[0]) * rbf(rbf(v0[0]) + bb[i]), hi_f(z[0]) * rbf(rbf(v0[1]) + bb[i]));
      o[1] = pk2(lo_f(z[1]) * rbf(rbf(v0[2]) + bb[i]), hi_f(z[1]) * rbf(rbf(v0[3]) + bb[i]));
      o[2] = pk2(lo_f(z[2]) * rbf(rbf(v1[0]) + bb[i]), hi_f(z[2]) * rbf(rbf(v1[1]) + bb[i]));
      o[3] = pk2(lo_f(z[3]) * rbf(rbf(v1[2]) + bb[i]), hi_f(z[3]) * rbf(rbf(v1[3]) + bb[i]));
      const int k = 448 + f0 + c * 8, tt = t0 + t;
      *(u32x4*)(oh + ((size_t)((tt >> 4) * 42 + (k >> 5)) * 64 + ((k >> 3) & 3) * 16 + (tt & 15)) * 8) = o;
    }
  }
  STAMP(2, 31);
}

__global__ __launch_bounds__(384) void k4_kernel(const u16* __restrict__ x, const char* __restrict__ ws,
                                                  u16* __restrict__ out) {
  __shared__ __attribute__((aligned(16))) char sm[6 * 32 * K3_RS];
  const int tid = threadIdx.x, lane = tid & 63, wave = __builtin_amdgcn_readfirstlane(tid >> 6);
  const int b = blockIdx.x;
  const int xcd = b & 7, idx = b >> 3;
  const int mt_ = xcd * 8 + (idx & 7), nt_ = idx >> 3;
  const int t0 = mt_ * 32, n0 = nt_ * 112;
  const int g = lane >> 4, r16 = lane & 15;
  STAMP(3, 0);
  const u16* ap = (const u16*)(ws + WS_OH) + ((size_t)(mt_ * 2 * 42 + wave * 7) * 64 + lane) * 8;
  const u16* bp = (const u16*)(ws + WS_WT) + ((size_t)(nt_ * 7 * 42 + wave * 7) * 64 + lane) * 8;
  bf16x8 a0[2], b0[7], a1[2], b1[7], a2[2], b2[7], a3[2], b3[7];
#define K4_LOAD(AF, BF, KS)                                                                     \
  do {                                                                                          \
    _Pragma("unroll") for (int m = 0; m < 2; ++m) AF[m] = *(const bf16x8*)(ap + (m * 42 + (KS)) * 512); \
    _Pragma("unroll") for (int n = 0; n < 7; ++n) BF[n] = *(const bf16x8*)(bp + (n * 42 + (KS)) * 512); \
  } while (0)
  K4_LOAD(a0, b0, 0);
  K4_LOAD(a1, b1, 1);
  K4_LOAD(a2, b2, 2);
  K4_LOAD(a3, b3, 3);
  u32x4 xv[2];
#pragma unroll
  for (int i = 0; i < 2; ++i) {
    int id = tid + 384 * i;
    if (id >= 448) id = tid;
    xv[i] = *(const u32x4*)(x + (size_t)(t0 + id / 14) * D_ + n0 + (id % 14) * 8);
  }
  SCHED();
  f32x4 acc[7][2];
#pragma unroll
  for (int n = 0; n < 7; ++n) { acc[n][0] = f32x4{0.f, 0.f, 0.f, 0.f}; acc[n][1] = f32x4{0.f, 0.f, 0.f, 0.f}; }
#define K4_MMA(AF, BF)                                                                          \
  do {                                                                                          \
    _Pragma("unroll") for (int n = 0; n < 7; ++n) {                                             \
      acc[n][0] = mfma16(BF[n], AF[0], acc[n][0]);                                              \
      acc[n][1] = mfma16(BF[n], AF[1], acc[n][1]);                                              \
    }                                                                                           \
    SCHED();                                                                                    \
  } while (0)
  K4_MMA(a0, b0); K4_LOAD(a0, b0, 4); SCHED();
  K4_MMA(a1, b1); K4_LOAD(a1, b1, 5); SCHED();
  K4_MMA(a2, b2); K4_LOAD(a2, b2, 6); SCHED();
  K4_MMA(a3, b3);
  K4_MMA(a0, b0);
  K4_MMA(a1, b1);
  K4_MMA(a2, b2);
  STAMP(3, 1);
  char* rd = sm + wave * (32 * K3_RS) + r16 * K3_RS + 16 * g;
#pragma unroll
  for (int n = 0; n < 7; ++n)
#pragma unroll
    for (int m = 0; m < 2; ++m) *(f32x4*)(rd + 16 * m * K3_RS + 64 * n) = acc[n][m];
  __syncthreads();
#pragma unroll
  for (int i = 0; i < 2; ++i) {
    const int id = tid + 384 * i;
    if (id < 448) {
      const int et = id / 14, ec = id % 14;
      const char* p = sm + et * K3_RS + ec * 32;
      f32x4 v0 = {0.f, 0.f, 0.f, 0.f}, v1 = {0.f, 0.f, 0.f, 0.f};
#pragma unroll
      for (int w = 0; w < 6; ++w) {
        v0 += *(const f32x4*)(p + w * 32 * K3_RS);
        v1 += *(const f32x4*)(p + w * 32 * K3_RS + 16);
      }
      u32x4 o;
      o[0] = pk2(lo_f(xv[i][0]) + v0[0], hi_f(xv[i][0]) + v0[1]);
      o[1] = pk2(lo_f(xv[i][1]) + v0[2], hi_f(xv[i][1]) + v0[3]);
      o[2] = pk2(lo_f(xv[i][2]) + v1[0], hi_f(xv[i][2]) + v1[1]);
      o[3] = pk2(lo_f(xv[i][3]) + v1[2], hi_f(xv[i][3]) + v1[3]);
      *(u32x4*)(out + (size_t)(t0 + et) * D_ + n0 + ec * 8) = o;
    }
  }
  STAMP(3, 31);
}

extern "C" void kernel_launch(void* const* d_in, const int* in_sizes, int n_in, void* d_out, int out_size,
                              void* d_ws, size_t ws_size, hipStream_t stream) {
  const u16* x = (const u16*)d_in[0];
  const u16* wqkv = (const u16*)d_in[1];
  const u16* wphi = (const u16*)d_in[2];
  const u16* wo = (const u16*)d_in[3];
  const u16* win = (const u16*)d_in[5];
  const u16* wsp = (const u16*)d_in[6];
  const u16* bsp = (const u16*)d_in[7];
  const u16* wout = (const u16*)d_in[8];
  char* ws = (char*)d_ws;
  k1_kernel<<<256, 768, 0, stream>>>(x, wqkv, wo, win, wout, wsp, ws);
  k2_kernel<<<384, 512, 0, stream>>>(wphi, ws);
  k3_kernel<<<256, 512, 0, stream>>>(wsp, bsp, wphi, ws);
  k4_kernel<<<256, 384, 0, stream>>>(x, ws, (u16*)d_out);
}
```

```cpp
#include <hip/hip_runtime.h>
#include <hip/hip_bf16.h>
#include <stdint.h>

typedef __attribute__((ext_vector_type(8))) short bf16x8;
typedef __attribute__((ext_vector_type(4))) short s16x4;
typedef __attribute__((ext_vector_type(4))) float f32x4;
typedef __attribute__((ext_vector_type(2))) float f32x2;
typedef __attribute__((ext_vector_type(4))) unsigned u32x4;
typedef __attribute__((ext_vector_type(2))) unsigned u32x2;
typedef __attribute__((ext_vector_type(2))) __bf16 bf2_t;
typedef unsigned short u16;
#define LDS_AS __attribute__((address_space(3)))

constexpr int S_ = 2048, D_ = 448, NQKV = 1344, NIN = 1792, FH = 896, KO = 1344;

__device__ __forceinline__ unsigned pk2(float a, float b) {
  f32x2 v = {a, b};
  return __builtin_bit_cast(unsigned, __builtin_convertvector(v, bf2_t));
}
__device__ __forceinline__ float lo_f(unsigned u) { return __uint_as_float(u << 16); }
__device__ __forceinline__ float hi_f(unsigned u) { return __uint_as_float(u & 0xffff0000u); }
__device__ __forceinline__ float rbf(float a) { return lo_f(pk2(a, 0.f)); }
__device__ __forceinline__ float bf2f(u16 v) { return __uint_as_float(((unsigned)v) << 16); }
__device__ __forceinline__ float dot2(unsigned a, unsigned b, float c) {
  return __builtin_amdgcn_fdot2_f32_bf16(__builtin_bit_cast(bf2_t, a), __builtin_bit_cast(bf2_t, b), c, false);
}

__device__ __forceinline__ float x16_max(float v) {
  auto r = __builtin_amdgcn_permlane16_swap(__float_as_uint(v), __float_as_uint(v), false, false);
  return fmaxf(__uint_as_float(r[0]), __uint_as_float(r[1]));
}
__device__ __forceinline__ float x32_max(float v) {
  auto r = __builtin_amdgcn_permlane32_swap(__float_as_uint(v), __float_as_uint(v), false, false);
  return fmaxf(__uint_as_float(r[0]), __uint_as_float(r[1]));
}
__device__ __forceinline__ float x16_min(float v) {
  auto r = __builtin_amdgcn_permlane16_swap(__float_as_uint(v), __float_as_uint(v), false, false);
  return fminf(__uint_as_float(r[0]), __uint_as_float(r[1]));
}
__device__ __forceinline__ float x32_min(float v) {
  auto r = __builtin_amdgcn_permlane32_swap(__float_as_uint(v), __float_as_uint(v), false, false);
  return fminf(__uint_as_float(r[0]), __uint_as_float(r[1]));
}
__device__ __forceinline__ float x16_add(float v) {
  auto r = __builtin_amdgcn_permlane16_swap(__float_as_uint(v), __float_as_uint(v), false, false);
  return __uint_as_float(r[0]) + __uint_as_float(r[1]);
}
__device__ __forceinline__ float x32_add(float v) {
  auto r = __builtin_amdgcn_permlane32_swap(__float_as_uint(v), __float_as_uint(v), false, false);
  return __uint_as_float(r[0]) + __uint_as_float(r[1]);
}
__device__ __forceinline__ float red8_add(float v) {
  v += __uint_as_float(__builtin_amdgcn_mov_dpp(__float_as_uint(v), 0xB1, 0xf, 0xf, true));
  v += __uint_as_float(__builtin_amdgcn_mov_dpp(__float_as_uint(v), 0x4E, 0xf, 0xf, true));
  v += __uint_as_float(__builtin_amdgcn_mov_dpp(__float_as_uint(v), 0x141, 0xf, 0xf, true));
  return v;
}
__device__ __forceinline__ f32x4 mfma16(bf16x8 a, bf16x8 b, f32x4 c) {
  return __builtin_amdgcn_mfma_f32_16x16x32_bf16(a, b, c, 0, 0, 0);
}
__device__ __forceinline__ unsigned gelu_pk(unsigned p) {
  const f32x2 u = {lo_f(p), hi_f(p)};
  const f32x2 c1 = {-2.3022081986f, -2.3022081986f};
  const f32x2 c2 = {-0.1029432397f, -0.1029432397f};
  f32x2 t = u * u;
  f32x2 a = u * (t * c2 + c1);
  f32x2 d = {1.f + __builtin_amdgcn_exp2f(a[0]), 1.f + __builtin_amdgcn_exp2f(a[1])};
  f32x2 r = {__builtin_amdgcn_rcpf(d[0]), __builtin_amdgcn_rcpf(d[1])};
  f32x2 o = u * r;
  return pk2(o[0], o[1]);
}
#define SCHED() __builtin_amdgcn_sched_barrier(0)
#define BAR() do { SCHED(); asm volatile("" ::: "memory"); __builtin_amdgcn_s_waitcnt(0xC07F); __builtin_amdgcn_s_barrier(); asm volatile("" ::: "memory"); SCHED(); } while (0)
__device__ __forceinline__ void glds16(const void* g, char* l) {
  __builtin_amdgcn_global_load_lds((const unsigned*)g, (LDS_AS unsigned*)l, 16, 0, 0);
}
__device__ __forceinline__ bf16x8 tr_frag(const char* p0, const char* p1) {
  s16x4 a = __builtin_amdgcn_ds_read_tr16_b64_v4i16((LDS_AS s16x4*)p0);
  s16x4 c = __builtin_amdgcn_ds_read_tr16_b64_v4i16((LDS_AS s16x4*)p1);
  return __builtin_shufflevector(a, c, 0, 1, 2, 3, 4, 5, 6, 7);
}

constexpr int YLD = 3136;
constexpr size_t WS_Y = 0;
constexpr size_t WS_Z2N = WS_Y + (size_t)S_ * YLD * 2 + 256;
constexpr size_t WS_OH = WS_Z2N + (size_t)S_ * 896 * 2;
constexpr size_t WS_WT = WS_OH + (size_t)S_ * KO * 2;
constexpr size_t WS_KVP = WS_WT + (size_t)D_ * KO * 2;
constexpr size_t WS_PHQ = WS_KVP + (size_t)8 * 16 * 56 * 32 * 4;
constexpr size_t WS_END = WS_PHQ + (size_t)16 * 128 * 2048;

#define STAMP(K, I) do { } while (0)
__device__ __forceinline__ void st16_wt(void* p, u32x4 v) {
  asm volatile("global_store_dwordx4 %0, %1, off sc0 sc1\n\ts_nop 1" ::"v"(p), "v"(v) : "memory");
}
__device__ __forceinline__ void st8_wt(void* p, u32x2 v) {
  asm volatile("global_store_dwordx2 %0, %1, off sc0 sc1\n\ts_nop 1" ::"v"(p), "v"(v) : "memory");
}
#define SGB(mask, n) __builtin_amdgcn_sched_group_barrier(mask, n, 0)

__device__ __forceinline__ void phi_map(bf16x8 qf, bf16x8 wp0, bf16x8 wp1, int g, unsigned (&pp)[2][2],
                                        unsigned (&pm)[2][2]) {
  f32x4 z = {0.f, 0.f, 0.f, 0.f};
  f32x4 p0 = mfma16(wp0, qf, z);
  f32x4 p1 = mfma16(wp1, qf, z);
  float v[8];
  {
    unsigned a = pk2(p0[0], p0[1]), b = pk2(p0[2], p0[3]), c = pk2(p1[0], p1[1]), d = pk2(p1[2], p1[3]);
    v[0] = lo_f(a); v[1] = hi_f(a); v[2] = lo_f(b); v[3] = hi_f(b);
    v[4] = lo_f(c); v[5] = hi_f(c); v[6] = lo_f(d); v[7] = hi_f(d);
  }
  const bool valid1 = (g != 3);
  if (!valid1) { v[4] = v[0]; v[5] = v[0]; v[6] = v[0]; v[7] = v[0]; }
  float mx = fmaxf(fmaxf(fmaxf(v[0], v[1]), fmaxf(v[2], v[3])), fmaxf(fmaxf(v[4], v[5]), fmaxf(v[6], v[7])));
  float mn = fminf(fminf(fminf(v[0], v[1]), fminf(v[2], v[3])), fminf(fminf(v[4], v[5]), fminf(v[6], v[7])));
  mx = x32_max(x16_max(mx));
  mn = x32_min(x16_min(mn));
  const float L2E = 1.4426950408889634f;
  const float mxl = mx * L2E, mnl = mn * L2E;
  float ep[8], em[8];
  float sp = 0.f, sm = 0.f;
#pragma unroll
  for (int j = 0; j < 8; ++j) {
    ep[j] = __builtin_amdgcn_exp2f(__builtin_fmaf(v[j], L2E, -mxl));
    em[j] = __builtin_amdgcn_exp2f(__builtin_fmaf(v[j], -L2E, mnl));
    if (j >= 4) { ep[j] = valid1 ? ep[j] : 0.f; em[j] = valid1 ? em[j] : 0.f; }
    sp += ep[j];
    sm += em[j];
  }
  sp = x32_add(x16_add(sp));
  sm = x32_add(x16_add(sm));
  const float rp = __builtin_amdgcn_rcpf(sp), rm = __builtin_amdgcn_rcpf(sm);
  pp[0][0] = pk2(ep[0] * rp, ep[1] * rp); pp[0][1] = pk2(ep[2] * rp, ep[3] * rp);
  pp[1][0] = pk2(ep[4] * rp, ep[5] * rp); pp[1][1] = pk2(ep[6] * rp, ep[7] * rp);
  pm[0][0] = pk2(em[0] * rm, em[1] * rm); pm[0][1] = pk2(em[2] * rm, em[3] * rm);
  pm[1][0] = pk2(em[4] * rm, em[5] * rm); pm[1][1] = pk2(em[6] * rm, em[7] * rm);
}

constexpr int K1_XST = 128 * 128;
constexpr int K1_STAGE = K1_XST + 64 * 448;
constexpr int K1_LDS = 3 * K1_STAGE;

__global__ __launch_bounds__(768) void k1_kernel(const u16* __restrict__ x, const u16* __restrict__ wqkv,
                                                  const u16* __restrict__ wo, const u16* __restrict__ win,
                                                  const u16* __restrict__ wout, const u16* __restrict__ wsp,
                                                  char* __restrict__ ws) {
  __shared__ __attribute__((aligned(16))) char sm[K1_LDS];
  const int tid = threadIdx.x, lane = tid & 63, wave = __builtin_amdgcn_readfirstlane(tid >> 6);
  const int b = blockIdx.x;
  STAMP(0, 0);
  if (b >= 224) {
    const int kb = b - 224;
    const int k0 = kb * 64;
    if (kb < 21) {
    u16* T = (u16*)sm;
    constexpr int TS = 456;
    u32x4 v[5];
#pragma unroll
    for (int j = 0; j < 5; ++j) {
      int i = tid + 768 * j;
      if (i >= 64 * 56) i = tid;
      const int r = i / 56, c = i % 56;
      const int k = k0 + r;
      const u16* src = (k < 448) ? (wo + (size_t)k * 448) : (wout + (size_t)(k - 448) * 448);
      v[j] = *(const u32x4*)(src + c * 8);
    }
#pragma unroll
    for (int j = 0; j < 5; ++j) {
      const int i = tid + 768 * j;
      if (i < 64 * 56) *(u32x4*)(T + (i / 56) * TS + (i % 56) * 8) = v[j];
    }
    __syncthreads();
    u16* wT = (u16*)(ws + WS_WT);
    for (int i = tid; i < 448 * 8; i += 768) {
      const int n = i >> 3, c = i & 7;
      u16 t[8];
#pragma unroll
      for (int j = 0; j < 8; ++j) t[j] = T[(c * 8 + j) * TS + n];
      u32x4 o;
#pragma unroll
      for (int j = 0; j < 4; ++j) o[j] = (unsigned)t[2 * j] | ((unsigned)t[2 * j + 1] << 16);
      st16_wt(wT + ((size_t)((n >> 4) * 42 + (k0 >> 5) + (c >> 2)) * 64 + (c & 3) * 16 + (n & 15)) * 8, o);
    }
    }
    STAMP(0, 1);
    {
      const char* p = (const char*)wsp + (size_t)kb * 262144 + tid * 16;
      unsigned a = 0;
#pragma unroll
      for (int i = 0; i < 21; ++i) {
        u32x4 v = *(const u32x4*)(p + i * 12288);
        a |= v[0] & v[1] & v[2] & v[3];
      }
      asm volatile("" ::"v"(a));
    }
    STAMP(0, 31);
    return;
  }

  const int xcd = b & 7, idx = b >> 3;
  const int cb = (xcd & 1) * 7 + idx % 7;
  const int rt = (xcd >> 1) * 4 + idx / 7;
  const int row0 = rt * 128;
  const int g = lane >> 4, r16 = lane & 15;
  const int wm = (wave >> 1) & 3, wn = wave & 1;
  constexpr int RST = 464;

  if (wave >= 8) {
    const int lw = wave - 8;
    const u16* W = (cb < 6) ? (wqkv + 224 * cb) : (win + 224 * (cb - 6));
    const int ldw = (cb < 6) ? NQKV : NIN;
    const u16* src[11];
#pragma unroll
    for (int j = 0; j < 11; ++j) {
      const int i = lw + 4 * j;
      if (j < 4) {
        const int row = 8 * i + (lane >> 3);
        src[j] = x + (size_t)(row0 + row) * D_ + (((lane & 7) ^ ((row >> 1) & 7)) << 3);
      } else {
        const int o = (i - 16) * 1024 + lane * 16;
        const int k = o / 448, cp = (o % 448) >> 4;
        const int G = (cp >> 1) ^ ((k >> 3) & 1);
        src[j] = W + (size_t)k * ldw + 8 * (2 * G + (cp & 1));
      }
    }
    const int wstep = 64 * ldw;
    char* dst0 = sm + lw * 1024;
#pragma unroll
    for (int s = 0; s < 2; ++s)
#pragma unroll
      for (int j = 0; j < 11; ++j) glds16(src[j] + s * (j < 4 ? 64 : wstep), dst0 + s * K1_STAGE + j * 4096);
    int nb = 2;
#pragma unroll 1
    for (int t = 0; t < 7; ++t) {
      if (t < 6) asm volatile("s_waitcnt vmcnt(11)" ::: "memory");
      else asm volatile("s_waitcnt vmcnt(0)" ::: "memory");
      __builtin_amdgcn_s_barrier();
      if (t + 2 < 7) {
        char* dst = dst0 + nb * K1_STAGE;
#pragma unroll
        for (int j = 0; j < 11; ++j) glds16(src[j] + (t + 2) * (j < 4 ? 64 : wstep), dst + j * 4096);
        nb = (nb == 2) ? 0 : nb + 1;
      }
    }
    __builtin_amdgcn_s_barrier();
    __builtin_amdgcn_s_barrier();
  } else {
    f32x4 acc[7][2], sacc = {0.f, 0.f, 0.f, 0.f}, qacc = {0.f, 0.f, 0.f, 0.f}, cacc[2];
#pragma unroll
    for (int nt = 0; nt < 7; ++nt) { acc[nt][0] = f32x4{0.f, 0.f, 0.f, 0.f}; acc[nt][1] = f32x4{0.f, 0.f, 0.f, 0.f}; }
    cacc[0] = f32x4{0.f, 0.f, 0.f, 0.f}; cacc[1] = f32x4{0.f, 0.f, 0.f, 0.f};
    const bf16x8 ones = {0x3F80, 0x3F80, 0x3F80, 0x3F80, 0x3F80, 0x3F80, 0x3F80, 0x3F80};
    const int sw = (r16 >> 1) & 7;
    const int xrow = (32 * wm + 16 * wn + r16) * 128, xd = wn ? -2048 : 2048;
    const int xo0 = xrow + ((g ^ sw) << 4), xo1 = xrow + (((4 | g) ^ sw) << 4);
    int woff[7];
#pragma unroll
    for (int i = 0; i < 7; ++i) {
      int nt = i + 2 * wm;
      nt = (nt >= 7) ? nt - 7 : nt;
      const int G = 7 * wn + nt;
      woff[i] = K1_XST + (8 * g + ((lane >> 2) & 3)) * 448 + (lane & 3) * 8 + ((G ^ (g & 1)) << 5);
    }
    bf16x8 xa[2], wa[7], xb[2], wb[7];
#define K1_READ(XF, WF, SB, KS)                                                              \
  do {                                                                                       \
    XF[0] = *(const bf16x8*)((SB) + ((KS) ? xo1 : xo0));                                     \
    XF[1] = *(const bf16x8*)((SB) + ((KS) ? xo1 : xo0) + xd);                                \
    _Pragma("unroll") for (int nt = 0; nt < 7; ++nt) {                                       \
      const char* p_ = (SB) + woff[nt] + (KS)*32 * 448;                                      \
      WF[nt] = tr_frag(p_, p_ + 4 * 448);                                                    \
    }                                                                                        \
  } while (0)
#define K1_MMA(XF, WF)                                                                       \
  do {                                                                                       \
    _Pragma("unroll") for (int nt = 0; nt < 7; ++nt) acc[nt][0] = mfma16(WF[nt], XF[0], acc[nt][0]); \
    sacc = mfma16(ones, XF[0], sacc);                                                        \
    qacc = mfma16(XF[0], XF[0], qacc);                                                       \
    _Pragma("unroll") for (int nt = 0; nt < 7; ++nt) acc[nt][1] = mfma16(WF[nt], XF[1], acc[nt][1]); \
    cacc[0] = mfma16(WF[0], ones, cacc[0]);                                                  \
    cacc[1] = mfma16(WF[1], ones, cacc[1]);                                                  \
  } while (0)
    int rb = 0;
#pragma unroll 1
    for (int t = 0; t < 7; ++t) {
      BAR();
      STAMP(0, 3 + t);
      const char* sb = sm + rb * K1_STAGE;
      rb = (rb == 2) ? 0 : rb + 1;
      K1_READ(xa, wa, sb, 0);
      SCHED();
      K1_READ(xb, wb, sb, 1);
      K1_MMA(xa, wa);
      _Pragma("unroll") for (int i_ = 0; i_ < 8; ++i_) { SGB(0x008, 1); SGB(0x100, 2); }
      SCHED();
      K1_MMA(xb, wb);
    }
    BAR();
    STAMP(0, 17);
    float* CS = (float*)(sm + 128 * RST);
    f32x2* RS_ = (f32x2*)(CS + 224);
    if (r16 == 0) {
#pragma unroll
      for (int i = 0; i < 2; ++i) {
        int nt = i + 2 * wm;
        nt = (nt >= 7) ? nt - 7 : nt;
        *(f32x4*)(CS + 112 * wn + 16 * nt + 4 * g) = cacc[i];
      }
    }
    {
      const float qd = (r16 & 3) == 0 ? qacc[0] : (r16 & 3) == 1 ? qacc[1] : (r16 & 3) == 2 ? qacc[2] : qacc[3];
      if (g == (r16 >> 2)) {
        const float mean = sacc[0] * (1.f / 448.f);
        const float var = fmaxf(qd * (1.f / 448.f) - mean * mean, 0.f);
        const float sd = rbf(sqrtf(rbf(rbf(var) + 1e-5f)));
        const float r = 1.f / sd;
        RS_[32 * wm + 16 * wn + r16] = f32x2{r, r * rbf(mean)};
      }
    }
    BAR();
    f32x2 st[2];
    st[0] = RS_[32 * wm + 16 * wn + r16];
    st[1] = RS_[32 * wm + 16 * (wn ^ 1) + r16];
#pragma unroll
    for (int i = 0; i < 7; ++i) {
      int nt = i + 2 * wm;
      nt = (nt >= 7) ? nt - 7 : nt;
      const f32x4 c4 = *(const f32x4*)(CS + 112 * wn + 16 * nt + 4 * g);
#pragma unroll
      for (int j = 0; j < 2; ++j) {
        char* dst = sm + (32 * wm + 16 * (j ^ wn) + r16) * RST + (112 * wn + 4 * g) * 2 + 32 * nt;
        const f32x4 v = acc[i][j] * st[j][0] - c4 * st[j][1];
        *(u32x2*)(dst) = u32x2{pk2(v[0], v[1]), pk2(v[2], v[3])};
      }
    }
  }
  __syncthreads();
  u16* dstb = (u16*)(ws + WS_Y) + (size_t)row0 * YLD + 224 * cb;
#pragma unroll
  for (int i = 0; i < 5; ++i) {
    const int id = tid + 768 * i;
    if (id < 128 * 28) {
      const int s = id / 28, c = id % 28;
      st16_wt(dstb + (size_t)s * YLD + c * 8, *(const u32x4*)(sm + s * RST + c * 16));
    }
  }
  STAMP(0, 31);
}

__device__ __forceinline__ float red64_add(float v) {
  v = red8_add(v);
  v += __uint_as_float(__builtin_amdgcn_mov_dpp(__float_as_uint(v), 0x140, 0xf, 0xf, true));
  return x32_add(x16_add(v));
}

constexpr int K2_PW = 64 * 80 + 32 * 64;
__global__ __launch_bounds__(512) void k2_kernel(const u16* __restrict__ wphi, char* __restrict__ ws) {
  __shared__ __attribute__((aligned(16))) char sm[8 * 56 * 32 * 4 + 2048];
  const int tid = threadIdx.x, lane = tid & 63, wave = __builtin_amdgcn_readfirstlane(tid >> 6);
  const int b = blockIdx.x;
  const u16* Y = (const u16*)(ws + WS_Y);
  STAMP(1, 0);
  if (b < 128) {
    const int h = b & 15, rg = b >> 4;
    const int g = lane >> 4, r16 = lane & 15;
    const int rowb = rg * 256 + wave * 32;
    u32x2 kv_[2][2];
#pragma unroll
    for (int st = 0; st < 2; ++st) {
      const u16* kp = Y + (size_t)(rowb + st * 16 + r16) * YLD + 448 + h * 28 + 8 * g;
      kv_[st][0] = *(const u32x2*)(kp);
      kv_[st][1] = *(const u32x2*)(kp + 4);
    }
    u32x2 vv[4];
#pragma unroll
    for (int i = 0; i < 4; ++i) {
      const int row = (lane >> 3) + 8 * i, pc = lane & 7;
      vv[i] = *(const u32x2*)(Y + (size_t)(rowb + row) * YLD + 896 + h * 28 + 4 * (pc < 7 ? pc : 0));
      if (pc == 7) vv[i] = u32x2{0x00003F80u, 0u};
    }
    u32x4 wraw = {0u, 0u, 0u, 0u};
    if (tid < 98) wraw = *(const u32x4*)(wphi + (size_t)h * 784 + tid * 8);
    char* WP = sm + 8 * 56 * 32 * 4;
    if (tid < 98) *(u32x4*)(WP + tid * 16) = wraw;
    char* PK = sm + wave * K2_PW;
    char* VS = PK + 64 * 80;
    __syncthreads();
    STAMP(1, 1);
    const char* wp = WP + (8 * g + ((lane >> 2) & 3)) * 56 + (lane & 3) * 8;
    bf16x8 wp0 = tr_frag(wp, wp + 4 * 56);
    bf16x8 wp1 = tr_frag(wp + 32, wp + 4 * 56 + 32);
    if (g == 3) {
#pragma unroll
      for (int j = 4; j < 8; ++j) { wp0[j] = 0; wp1[j] = 0; }
    }
    f32x4 kacc[4][2];
#pragma unroll
    for (int a = 0; a < 4; ++a) { kacc[a][0] = f32x4{0.f, 0.f, 0.f, 0.f}; kacc[a][1] = f32x4{0.f, 0.f, 0.f, 0.f}; }
#pragma unroll
    for (int pr = 0; pr < 1; ++pr) {
#pragma unroll
      for (int i = 0; i < 4; ++i) {
        const int row = (lane >> 3) + 8 * i;
        const int slot = 2 * (row & 15) + (row >> 4);
        *(u32x2*)(VS + slot * 64 + (lane & 7) * 8) = vv[4 * pr + i];
      }
      unsigned pp[2][2][2], pm[2][2][2];
#pragma unroll
      for (int mt = 0; mt < 2; ++mt) {
        u32x4 q4 = {kv_[2 * pr + mt][0][0], kv_[2 * pr + mt][0][1], kv_[2 * pr + mt][1][0], kv_[2 * pr + mt][1][1]};
        if (g == 3) { q4[2] = 0u; q4[3] = 0u; }
        phi_map(__builtin_bit_cast(bf16x8, q4), wp0, wp1, g, pp[mt], pm[mt]);
      }
#pragma unroll
      for (int ft = 0; ft < 2; ++ft) {
        if (ft == 0 || g != 3) {
          char* d0 = PK + (16 * ft + 4 * g) * 80 + r16 * 4;
#pragma unroll
          for (int j = 0; j < 2; ++j) {
            *(unsigned*)(d0 + (2 * j) * 80) = (pp[0][ft][j] & 0xffffu) | (pp[1][ft][j] << 16);
            *(unsigned*)(d0 + (2 * j + 1) * 80) = (pp[0][ft][j] >> 16) | (pp[1][ft][j] & 0xffff0000u);
            *(unsigned*)(d0 + (28 + 2 * j) * 80) = (pm[0][ft][j] & 0xffffu) | (pm[1][ft][j] << 16);
            *(unsigned*)(d0 + (28 + 2 * j + 1) * 80) = (pm[0][ft][j] >> 16) | (pm[1][ft][j] & 0xffff0000u);
          }
        }
      }
      __builtin_amdgcn_s_waitcnt(0xC07F);
      __builtin_amdgcn_wave_barrier();
      bf16x8 bfr[2];
      {
        const char* vp = VS + (8 * g + ((lane >> 2) & 3)) * 64 + (lane & 3) * 8;
        bfr[0] = tr_frag(vp, vp + 4 * 64);
        bfr[1] = tr_frag(vp + 32, vp + 4 * 64 + 32);
      }
#pragma unroll
      for (int a = 0; a < 4; ++a) {
        bf16x8 af = *(const bf16x8*)(PK + (16 * a + r16) * 80 + g * 16);
        kacc[a][0] = mfma16(af, bfr[0], kacc[a][0]);
        kacc[a][1] = mfma16(af, bfr[1], kacc[a][1]);
      }
      __builtin_amdgcn_s_waitcnt(0xC07F);
      __builtin_amdgcn_wave_barrier();
    }
    STAMP(1, 2);
    __syncthreads();
    float* red = (float*)sm + wave * (56 * 32);
#pragma unroll
    for (int a = 0; a < 4; ++a)
#pragma unroll
      for (int c = 0; c < 2; ++c)
#pragma unroll
        for (int j = 0; j < 4; ++j) {
          const int f = 16 * a + 4 * g + j;
          if (f < 56) red[f * 32 + 16 * c + r16] = kacc[a][c][j];
        }
    __syncthreads();
    if (tid < 448) {
      const f32x4* rp = (const f32x4*)sm + tid;
      f32x4 s = (rp[0] + rp[448]) + (rp[2 * 448] + rp[3 * 448]);
      s += (rp[4 * 448] + rp[5 * 448]) + (rp[6 * 448] + rp[7 * 448]);
      st16_wt((f32x4*)(ws + WS_KVP) + (size_t)(rg * 16 + h) * 448 + tid, __builtin_bit_cast(u32x4, s));
    }
  } else if (b >= 256) {
    const int h = b & 15, rg = (b - 256) >> 4;
    const int g = lane >> 4, r16 = lane & 15;
    u32x2 qv[2][2];
#pragma unroll
    for (int st = 0; st < 2; ++st) {
      const u16* qp = Y + (size_t)(rg * 256 + wave * 32 + st * 16 + r16) * YLD + h * 28 + 8 * g;
      qv[st][0] = *(const u32x2*)(qp);
      qv[st][1] = *(const u32x2*)(qp + 4);
    }
    u32x4 wraw = {0u, 0u, 0u, 0u};
    if (tid < 98) wraw = *(const u32x4*)(wphi + (size_t)h * 784 + tid * 8);
    if (tid < 98) *(u32x4*)(sm + tid * 16) = wraw;
    __syncthreads();
    const char* wp = sm + (8 * g + ((lane >> 2) & 3)) * 56 + (lane & 3) * 8;
    bf16x8 wp0 = tr_frag(wp, wp + 4 * 56);
    bf16x8 wp1 = tr_frag(wp + 32, wp + 4 * 56 + 32);
    if (g == 3) {
#pragma unroll
      for (int j = 4; j < 8; ++j) { wp0[j] = 0; wp1[j] = 0; }
    }
#pragma unroll
    for (int st = 0; st < 2; ++st) {
      u32x4 q4 = {qv[st][0][0], qv[st][0][1], qv[st][1][0], qv[st][1][1]};
      if (g == 3) { q4[2] = 0u; q4[3] = 0u; }
      unsigned pp[2][2], pm[2][2];
      phi_map(__builtin_bit_cast(bf16x8, q4), wp0, wp1, g, pp, pm);
      u32x4* dst = (u32x4*)(ws + WS_PHQ) + ((size_t)(h * 128 + rg * 16 + wave * 2 + st) * 2) * 64 + lane;
      st16_wt(dst, u32x4{pp[0][0], pp[0][1], pp[1][0], pp[1][1]});
      st16_wt(dst + 64, u32x4{pm[0][0], pm[0][1], pm[1][0], pm[1][1]});
    }
  } else {
    const int row0 = (b - 128) * 16 + wave * 2;
    u16* z2n = (u16*)(ws + WS_Z2N);
    const int le = (lane < 56) ? lane : 0;
    u32x4 v[2][2];
#pragma unroll
    for (int r = 0; r < 2; ++r)
#pragma unroll
      for (int c = 0; c < 2; ++c) v[r][c] = *(const u32x4*)(Y + (size_t)(row0 + r) * YLD + 2240 + (le + 56 * c) * 8);
#pragma unroll
    for (int r = 0; r < 2; ++r) {
      float s = 0.f;
#pragma unroll
      for (int c = 0; c < 2; ++c)
#pragma unroll
        for (int j = 0; j < 4; ++j) {
          v[r][c][j] = gelu_pk(v[r][c][j]);
          s = dot2(v[r][c][j], 0x3F803F80u, s);
        }
      if (lane >= 56) s = 0.f;
      const float mean = red64_add(s) * (1.f / 896.f);
      const float mb = rbf(mean);
      float q = 0.f;
#pragma unroll
      for (int c = 0; c < 2; ++c)
#pragma unroll
        for (int j = 0; j < 4; ++j) {
          const float a = lo_f(v[r][c][j]) - mean, e = hi_f(v[r][c][j]) - mean;
          q += a * a + e * e;
          v[r][c][j] = pk2(lo_f(v[r][c][j]) - mb, hi_f(v[r][c][j]) - mb);
        }
      if (lane >= 56) q = 0.f;
      const float var = red64_add(q) * (1.f / 896.f);
      const float sd = rbf(sqrtf(rbf(rbf(var) + 1e-5f)));
      const float inv = 1.f / sd;
      if (lane < 56) {
#pragma unroll
        for (int c = 0; c < 2; ++c) {
          u32x4 o;
#pragma unroll
          for (int j = 0; j < 4; ++j) o[j] = pk2(lo_f(v[r][c][j]) * inv, hi_f(v[r][c][j]) * inv);
          *(u32x4*)(sm + (wave * 2 + r) * 1808 + (lane + 56 * c) * 16) = o;
        }
      }
    }
    __syncthreads();
    {
      const int rb0 = (b - 128) * 16;
      const int kb = rb0 >> 5, half = (rb0 >> 4) & 1;
      const int g = lane >> 4;
#pragma unroll
      for (int i = 0; i < 4; ++i) {
        const int u = wave + 8 * i;
        if (u < 28) {
          const int T = 2 * u + (g >> 1), gp = g & 1;
          const char* p = sm + (8 * gp + ((lane >> 2) & 3)) * 1808 + T * 32 + (lane & 3) * 8;
          bf16x8 fr = tr_frag(p, p + 4 * 1808);
          const int pn = T / 7, n = T % 7;
          st16_wt(z2n + ((size_t)((pn * 64 + kb) * 7 + n) * 64 + (2 * half + gp) * 16 + (lane & 15)) * 8, __builtin_bit_cast(u32x4, fr));
        }
      }
    }
  }
  STAMP(1, 31);
}

constexpr int K3_RS = 464;
constexpr int K3_STAGE = 64 * 256;
constexpr int K3_NB = 8;
constexpr int K3_AUX = K3_NB * K3_STAGE;
__device__ __forceinline__ void glds16_asm(const void* gsrc, unsigned lds_dst) {
  unsigned keep;
  asm volatile("s_mov_b32 %0, m0\n\ts_mov_b32 m0, %2\n\ts_nop 0\n\tglobal_load_lds_dwordx4 %1, off\n\ts_mov_b32 m0, %0"
               : "=&s"(keep) : "v"(gsrc), "s"(lds_dst) : "memory");
}
__global__ __launch_bounds__(512) void k3_kernel(const u16* __restrict__ wsp, const u16* __restrict__ bsp,
                                                  const u16* __restrict__ wphi, char* __restrict__ ws) {
  __shared__ __attribute__((aligned(16))) char sm[K3_NB * K3_STAGE + 32 * 144];
  const int tid = threadIdx.x, lane = tid & 63, wave = __builtin_amdgcn_readfirstlane(tid >> 6);
  const int b = blockIdx.x;
  const int xcd = b & 7, idx = b >> 3;
  const int mt_ = (xcd >> 1) * 8 + (idx & 7);
  const int nt_ = (xcd & 1) * 4 + (idx >> 3);
  const int t0 = mt_ * 64, f0 = nt_ * 112;
  const u16* z2n = (const u16*)(ws + WS_Z2N);
  const u16* Y = (const u16*)(ws + WS_Y);
  u16* oh = (u16*)(ws + WS_OH);
  const int g = lane >> 4, r16 = lane & 15;
  const int h = b & 15;
  u32x4 zv[2];
  float bb[2];
  STAMP(2, 0);
  const int kq = wave >> 1, wn = (wave & 1) ^ (wave >> 2);
  const u16* bp = z2n + ((size_t)(nt_ * 64 + kq) * 7 + 4 * wn) * 512 + lane * 8;
  const int arow = r16 * 256 + (((4 * kq + g) ^ r16) << 4);
  const u16* src0;
  const u16* src1;
  {
    const int row = 4 * wave + (lane >> 4);
    src0 = wsp + (size_t)(t0 + row) * S_ + (((lane & 15) ^ (row & 15)) << 3);
    src1 = src0 + (size_t)32 * S_;
  }
  const unsigned ldsb = __builtin_amdgcn_readfirstlane((unsigned)(size_t)(LDS_AS char*)sm) + wave * 1024;
#define K3_AISSUE(T)                                                                            \
  do {                                                                                          \
    glds16_asm(src0 + (T)*128, ldsb + ((T)&7) * K3_STAGE);                                      \
    glds16_asm(src1 + (T)*128, ldsb + ((T)&7) * K3_STAGE + 8192);                               \
  } while (0)
  f32x4 acc[4][4];
  bf16x8 aA[4], aB[4], b0[4], b1[4], b2[4], b3[4];
#define K3_BLOAD(BF, T, NT)                                                                     \
  do { _Pragma("unroll") for (int n = 0; n < NT; ++n) BF[n] = *(const bf16x8*)(bp + ((T)*28 + n) * 512); } while (0)
#define K3_AREAD(AF, T)                                                                         \
  do { _Pragma("unroll") for (int m = 0; m < 4; ++m) AF[m] = *(const bf16x8*)(sm + ((T)&7) * K3_STAGE + arow + m * 4096); } while (0)
#define K3_MMA(AF, BF, NT)                                                                      \
  do {                                                                                          \
    _Pragma("unroll") for (int n = 0; n < NT; ++n)                                              \
      _Pragma("unroll") for (int m = 0; m < 4; ++m) acc[n][m] = mfma16(BF[n], AF[m], acc[n][m]); \
  } while (0)
#define K3_STEP(T, AC, AN, BC, BL, NT)                                                          \
  do {                                                                                          \
    asm volatile("s_waitcnt vmcnt(12)" ::: "memory");                                           \
    BAR();                                                                                      \
    STAMP(2, 5 + (T));                                                                          \
    if ((T) + 8 < 16) K3_AISSUE((T) + 8);                                                       \
    if ((T) + 1 < 16) K3_AREAD(AN, (T) + 1);                                                    \
    if ((T) + 3 < 16) K3_BLOAD(BL, (T) + 3, NT);                                                \
    K3_MMA(AC, BC, NT);                                                                         \
    SGB(0x100, 4);                                                                              \
    _Pragma("unroll") for (int i_ = 0; i_ < NT; ++i_) { SGB(0x008, 2); SGB(0x020, 1); }         \
  } while (0)
#define K3_LOOP(NT) \
  do { \
    STAMP(2, 4); \
    K3_AREAD(aA, 0); \
    K3_STEP(0, aA, aB, b0, b3, NT); \
    K3_STEP(1, aB, aA, b1, b0, NT); \
    K3_STEP(2, aA, aB, b2, b1, NT); \
    K3_STEP(3, aB, aA, b3, b2, NT); \
    K3_STEP(4, aA, aB, b0, b3, NT); \
    K3_STEP(5, aB, aA, b1, b0, NT); \
    K3_STEP(6, aA, aB, b2, b1, NT); \
    K3_STEP(7, aB, aA, b3, b2, NT); \
    K3_STEP(8, aA, aB, b0, b3, NT); \
    K3_STEP(9, aB, aA, b1, b0, NT); \
    K3_STEP(10, aA, aB, b2, b1, NT); \
    K3_STEP(11, aB, aA, b3, b2, NT); \
    K3_STEP(12, aA, aB, b0, b3, NT); \
    _Pragma("unroll") for (int i = 0; i < 2; ++i) {                                             \
      int id = tid + 512 * i;                                                                   \
      if (id >= 896) id = tid;                                                                  \
      const int t = id / 14, c = id % 14;                                                       \
      zv[i] = *(const u32x4*)(Y + (size_t)(t0 + t) * YLD + 1344 + f0 + c * 8);                  \
      bb[i] = bf2f(bsp[t0 + t]);                                                                \
    }                                                                                           \
    K3_STEP(13, aB, aA, b1, b0, NT); \
    {                                                                                           \
      const int te = (tid < 448) ? tid : 0;                                                     \
      const f32x4* kvp = (const f32x4*)(ws + WS_KVP) + (size_t)h * 448 + te;                    \
      _Pragma("unroll") for (int p = 0; p < 8; ++p) part[p] = kvp[(size_t)p * 16 * 448];        \
      const u32x4* pq = (const u32x4*)(ws + WS_PHQ) + ((size_t)(h * 128 + (b >> 4) * 8 + wave) * 2) * 64 + lane; \
      c1 = pq[0];                                                                               \
      c2 = pq[64];                                                                              \
    }                                                                                           \
    K3_STEP(14, aA, aB, b2, b1, NT); \
    K3_STEP(15, aB, aA, b3, b2, NT); \
  } while (0)
  K3_AISSUE(0);
  K3_AISSUE(1);
  if (wn == 0) { K3_BLOAD(b0, 0, 4); } else { K3_BLOAD(b0, 0, 3); }
  K3_AISSUE(2);
  if (wn == 0) { K3_BLOAD(b1, 1, 4); } else { K3_BLOAD(b1, 1, 3); }
  K3_AISSUE(3);
  if (wn == 0) { K3_BLOAD(b2, 2, 4); } else { K3_BLOAD(b2, 2, 3); }
  K3_AISSUE(4); K3_AISSUE(5); K3_AISSUE(6); K3_AISSUE(7);
  asm volatile("s_waitcnt vmcnt(20)" ::: "memory");
  STAMP(2, 1);
  BAR();
  f32x4 part[8];
  u32x4 c1, c2;
  SCHED();
#pragma unroll
  for (int n = 0; n < 4; ++n)
#pragma unroll
    for (int m = 0; m < 4; ++m) acc[n][m] = f32x4{0.f, 0.f, 0.f, 0.f};
  if (wn == 0) K3_LOOP(4);
  else K3_LOOP(3);
  {
    char* KT = sm + K3_AUX;
    if (tid < 448) {
      const f32x4 s = ((part[0] + part[1]) + (part[2] + part[3])) + ((part[4] + part[5]) + (part[6] + part[7]));
      const int fp = tid >> 3, d0 = 4 * (tid & 7);
      const int mf = (fp >= 28) ? 1 : 0, f = fp - 28 * mf;
      const int pos = mf * 32 + ((f & 15) >> 2) * 8 + (f & 3) + 4 * (f >> 4);
      const unsigned a = pk2(s[0], s[1]), c = pk2(s[2], s[3]);
      char* d = KT + d0 * 144 + pos * 2;
      *(u16*)(d) = (u16)a; *(u16*)(d + 144) = (u16)(a >> 16);
      *(u16*)(d + 288) = (u16)c; *(u16*)(d + 432) = (u16)(c >> 16);
    } else {
      const int i = tid - 448, d = i >> 1, mf = i & 1;
      *(u32x2*)(KT + d * 144 + (mf * 32 + 28) * 2) = u32x2{0u, 0u};
    }
  }
  BAR();
  STAMP(2, 21);
  {
    const char* KT = sm + K3_AUX;
    bf16x8 af[2][2];
#pragma unroll
    for (int dt = 0; dt < 2; ++dt)
#pragma unroll
      for (int mf = 0; mf < 2; ++mf) af[dt][mf] = *(const bf16x8*)(KT + (16 * dt + r16) * 144 + (32 * mf + 8 * g) * 2);
    f32x4 o0 = {0.f, 0.f, 0.f, 0.f}, o1 = {0.f, 0.f, 0.f, 0.f};
    o0 = mfma16(af[0][0], __builtin_bit_cast(bf16x8, c1), o0);
    o1 = mfma16(af[1][0], __builtin_bit_cast(bf16x8, c1), o1);
    o0 = mfma16(af[0][1], __builtin_bit_cast(bf16x8, c2), o0);
    o1 = mfma16(af[1][1], __builtin_bit_cast(bf16x8, c2), o1);
    float den = __shfl(o1[0], 48 + r16, 64);
    den = rbf(rbf(den) + 1e-6f);
    const float inv = 1.f / den;
    const int mtile = (b >> 4) * 8 + wave;
    {
      const int k = h * 28 + 4 * g;
      u16* dst = oh + ((size_t)(mtile * 42 + (k >> 5)) * 64 + ((k >> 3) & 3) * 16 + r16) * 8 + (k & 7);
      st8_wt(dst, u32x2{pk2(rbf(o0[0]) * inv, rbf(o0[1]) * inv), pk2(rbf(o0[2]) * inv, rbf(o0[3]) * inv)});
    }
    if (g != 3) {
      const int k = h * 28 + 16 + 4 * g;
      u16* dst = oh + ((size_t)(mtile * 42 + (k >> 5)) * 64 + ((k >> 3) & 3) * 16 + r16) * 8 + (k & 7);
      st8_wt(dst, u32x2{pk2(rbf(o1[0]) * inv, rbf(o1[1]) * inv), pk2(rbf(o1[2]) * inv, rbf(o1[3]) * inv)});
    }
  }
  {
    char* rd = sm + kq * (64 * K3_RS) + r16 * K3_RS + (64 * wn + 4 * g) * 4;
#pragma unroll
    for (int n = 0; n < 4; ++n)
      if (n < 3 || wn == 0) {
#pragma unroll
        for (int m = 0; m < 4; ++m) *(f32x4*)(rd + 16 * m * K3_RS + 64 * n) = acc[n][m];
      }
  }
  BAR();
#pragma unroll
  for (int i = 0; i < 2; ++i) {
    const int id = tid + 512 * i;
    if (id < 896) {
      const int t = id / 14, c = id % 14;
      const char* p = sm + t * K3_RS + c * 32;
      f32x4 v0 = (*(const f32x4*)(p) + *(const f32x4*)(p + 64 * K3_RS)) +
                 (*(const f32x4*)(p + 128 * K3_RS) + *(const f32x4*)(p + 192 * K3_RS));
      f32x4 v1 = (*(const f32x4*)(p + 16) + *(const f32x4*)(p + 64 * K3_RS + 16)) +
                 (*(const f32x4*)(p + 128 * K3_RS + 16) + *(const f32x4*)(p + 192 * K3_RS + 16));
      u32x4 z;
#pragma unroll
      for (int j = 0; j < 4; ++j) z[j] = gelu_pk(zv[i][j]);
      u32x4 o;
      o[0] = pk2(lo_f(z[0]) * rbf(rbf(v0[0]) + bb[i]), hi_f(z[0]) * rbf(rbf(v0[1]) + bb[i]));
      o[1] = pk2(lo_f(z[1]) * rbf(rbf(v0[2]) + bb[i]), hi_f(z[1]) * rbf(rbf(v0[3]) + bb[i]));
      o[2] = pk2(lo_f(z[2]) * rbf(rbf(v1[0]) + bb[i]), hi_f(z[2]) * rbf(rbf(v1[1]) + bb[i]));
      o[3] = pk2(lo_f(z[3]) * rbf(rbf(v1[2]) + bb[i]), hi_f(z[3]) * rbf(rbf(v1[3]) + bb[i]));
      const int k = 448 + f0 + c * 8, tt = t0 + t;
      st16_wt(oh + ((size_t)((tt >> 4) * 42 + (k >> 5)) * 64 + ((k >> 3) & 3) * 16 + (tt & 15)) * 8, o);
    }
  }
  STAMP(2, 31);
}

__global__ __launch_bounds__(384) void k4_kernel(const u16* __restrict__ x, const char* __restrict__ ws,
                                                  u16* __restrict__ out) {
  __shared__ __attribute__((aligned(16))) char sm[6 * 32 * K3_RS];
  const int tid = threadIdx.x, lane = tid & 63, wave = __builtin_amdgcn_readfirstlane(tid >> 6);
  const int b = blockIdx.x;
  const int xcd = b & 7, idx = b >> 3;
  const int mt_ = xcd * 8 + (idx & 7), nt_ = idx >> 3;
  const int t0 = mt_ * 32, n0 = nt_ * 112;
  const int g = lane >> 4, r16 = lane & 15;
  STAMP(3, 0);
  const u16* ap = (const u16*)(ws + WS_OH) + ((size_t)(mt_ * 2 * 42 + wave * 7) * 64 + lane) * 8;
  const u16* bp = (const u16*)(ws + WS_WT) + ((size_t)(nt_ * 7 * 42 + wave * 7) * 64 + lane) * 8;
  bf16x8 a0[2], b0[7], a1[2], b1[7], a2[2], b2[7], a3[2], b3[7];
#define K4_LOAD(AF, BF, KS)                                                                     \
  do {                                                                                          \
    _Pragma("unroll") for (int m = 0; m < 2; ++m) AF[m] = *(const bf16x8*)(ap + (m * 42 + (KS)) * 512); \
    _Pragma("unroll") for (int n = 0; n < 7; ++n) BF[n] = *(const bf16x8*)(bp + (n * 42 + (KS)) * 512); \
  } while (0)
  K4_LOAD(a0, b0, 0);
  K4_LOAD(a1, b1, 1);
  K4_LOAD(a2, b2, 2);
  K4_LOAD(a3, b3, 3);
  u32x4 xv[2];
#pragma unroll
  for (int i = 0; i < 2; ++i) {
    int id = tid + 384 * i;
    if (id >= 448) id = tid;
    xv[i] = *(const u32x4*)(x + (size_t)(t0 + id / 14) * D_ + n0 + (id % 14) * 8);
  }
  SCHED();
  f32x4 acc[7][2];
#pragma unroll
  for (int n = 0; n < 7; ++n) { acc[n][0] = f32x4{0.f, 0.f, 0.f, 0.f}; acc[n][1] = f32x4{0.f, 0.f, 0.f, 0.f}; }
#define K4_MMA(AF, BF)                                                                          \
  do {                                                                                          \
    _Pragma("unroll") for (int n = 0; n < 7; ++n) {                                             \
      acc[n][0] = mfma16(BF[n], AF[0], acc[n][0]);                                              \
      acc[n][1] = mfma16(BF[n], AF[1], acc[n][1]);                                              \
    }                                                                                           \
    SCHED();                                                                                    \
  } while (0)
  K4_MMA(a0, b0); K4_LOAD(a0, b0, 4); SCHED();
  K4_MMA(a1, b1); K4_LOAD(a1, b1, 5); SCHED();
  K4_MMA(a2, b2); K4_LOAD(a2, b2, 6); SCHED();
  K4_MMA(a3, b3);
  K4_MMA(a0, b0);
  K4_MMA(a1, b1);
  K4_MMA(a2, b2);
  STAMP(3, 1);
  char* rd = sm + wave * (32 * K3_RS) + r16 * K3_RS + 16 * g;
#pragma unroll
  for (int n = 0; n < 7; ++n)
#pragma unroll
    for (int m = 0; m < 2; ++m) *(f32x4*)(rd + 16 * m * K3_RS + 64 * n) = acc[n][m];
  __syncthreads();
#pragma unroll
  for (int i = 0; i < 2; ++i) {
    const int id = tid + 384 * i;
    if (id < 448) {
      const int et = id / 14, ec = id % 14;
      const char* p = sm + et * K3_RS + ec * 32;
      f32x4 v0 = {0.f, 0.f, 0.f, 0.f}, v1 = {0.f, 0.f, 0.f, 0.f};
#pragma unroll
      for (int w = 0; w < 6; ++w) {
        v0 += *(const f32x4*)(p + w * 32 * K3_RS);
        v1 += *(const f32x4*)(p + w * 32 * K3_RS + 16);
      }
      u32x4 o;
      o[0] = pk2(lo_f(xv[i][0]) + v0[0], hi_f(xv[i][0]) + v0[1]);
      o[1] = pk2(lo_f(xv[i][1]) + v0[2], hi_f(xv[i][1]) + v0[3]);
      o[2] = pk2(lo_f(xv[i][2]) + v1[0], hi_f(xv[i][2]) + v1[1]);
      o[3] = pk2(lo_f(xv[i][3]) + v1[2], hi_f(xv[i][3]) + v1[3]);
      *(u32x4*)(out + (size_t)(t0 + et) * D_ + n0 + ec * 8) = o;
    }
  }
  STAMP(3, 31);
}

extern "C" void kernel_launch(void* const* d_in, const int* in_sizes, int n_in, void* d_out, int out_size,
                              void* d_ws, size_t ws_size, hipStream_t stream) {
  const u16* x = (const u16*)d_in[0];
  const u16* wqkv = (const u16*)d_in[1];
  const u16* wphi = (const u16*)d_in[2];
  const u16* wo = (const u16*)d_in[3];
  const u16* win = (const u16*)d_in[5];
  const u16* wsp = (const u16*)d_in[6];
  const u16* bsp = (const u16*)d_in[7];
  const u16* wout = (const u16*)d_in[8];
  char* ws = (char*)d_ws;
  k1_kernel<<<256, 768, 0, stream>>>(x, wqkv, wo, win, wout, wsp, ws);
  k2_kernel<<<384, 512, 0, stream>>>(wphi, ws);
  k3_kernel<<<256, 512, 0, stream>>>(wsp, bsp, wphi, ws);
  k4_kernel<<<256, 384, 0, stream>>>(x, ws, (u16*)d_out);
}
```

```cpp
#include <hip/hip_runtime.h>
#include <hip/hip_bf16.h>
#include <stdint.h>

typedef __attribute__((ext_vector_type(8))) short bf16x8;
typedef __attribute__((ext_vector_type(4))) short s16x4;
typedef __attribute__((ext_vector_type(4))) float f32x4;
typedef __attribute__((ext_vector_type(2))) float f32x2;
typedef __attribute__((ext_vector_type(4))) unsigned u32x4;
typedef __attribute__((ext_vector_type(2))) unsigned u32x2;
typedef __attribute__((ext_vector_type(2))) __bf16 bf2_t;
typedef unsigned short u16;
#define LDS_AS __attribute__((address_space(3)))

constexpr int S_ = 2048, D_ = 448, NQKV = 1344, NIN = 1792, FH = 896, KO = 1344;

__device__ __forceinline__ unsigned pk2(float a, float b) {
  f32x2 v = {a, b};
  return __builtin_bit_cast(unsigned, __builtin_convertvector(v, bf2_t));
}
__device__ __forceinline__ float lo_f(unsigned u) { return __uint_as_float(u << 16); }
__device__ __forceinline__ float hi_f(unsigned u) { return __uint_as_float(u & 0xffff0000u); }
__device__ __forceinline__ float rbf(float a) { return lo_f(pk2(a, 0.f)); }
__device__ __forceinline__ float bf2f(u16 v) { return __uint_as_float(((unsigned)v) << 16); }
__device__ __forceinline__ float dot2(unsigned a, unsigned b, float c) {
  return __builtin_amdgcn_fdot2_f32_bf16(__builtin_bit_cast(bf2_t, a), __builtin_bit_cast(bf2_t, b), c, false);
}

__device__ __forceinline__ float x16_max(float v) {
  auto r = __builtin_amdgcn_permlane16_swap(__float_as_uint(v), __float_as_uint(v), false, false);
  return fmaxf(__uint_as_float(r[0]), __uint_as_float(r[1]));
}
__device__ __forceinline__ float x32_max(float v) {
  auto r = __builtin_amdgcn_permlane32_swap(__float_as_uint(v), __float_as_uint(v), false, false);
  return fmaxf(__uint_as_float(r[0]), __uint_as_float(r[1]));
}
__device__ __forceinline__ float x16_min(float v) {
  auto r = __builtin_amdgcn_permlane16_swap(__float_as_uint(v), __float_as_uint(v), false, false);
  return fminf(__uint_as_float(r[0]), __uint_as_float(r[1]));
}
__device__ __forceinline__ float x32_min(float v) {
  auto r = __builtin_amdgcn_permlane32_swap(__float_as_uint(v), __float_as_uint(v), false, false);
  return fminf(__uint_as_float(r[0]), __uint_as_float(r[1]));
}
__device__ __forceinline__ float x16_add(float v) {
  auto r = __builtin_amdgcn_permlane16_swap(__float_as_uint(v), __float_as_uint(v), false, false);
  return __uint_as_float(r[0]) + __uint_as_float(r[1]);
}
__device__ __forceinline__ float x32_add(float v) {
  auto r = __builtin_amdgcn_permlane32_swap(__float_as_uint(v), __float_as_uint(v), false, false);
  return __uint_as_float(r[0]) + __uint_as_float(r[1]);
}
__device__ __forceinline__ float red8_add(float v) {
  v += __uint_as_float(__builtin_amdgcn_mov_dpp(__float_as_uint(v), 0xB1, 0xf, 0xf, true));
  v += __uint_as_float(__builtin_amdgcn_mov_dpp(__float_as_uint(v), 0x4E, 0xf, 0xf, true));
  v += __uint_as_float(__builtin_amdgcn_mov_dpp(__float_as_uint(v), 0x141, 0xf, 0xf, true));
  return v;
}
__device__ __forceinline__ f32x4 mfma16(bf16x8 a, bf16x8 b, f32x4 c) {
  return __builtin_amdgcn_mfma_f32_16x16x32_bf16(a, b, c, 0, 0, 0);
}
__device__ __forceinline__ unsigned gelu_pk(unsigned p) {
  const f32x2 u = {lo_f(p), hi_f(p)};
  const f32x2 c1 = {-2.3022081986f, -2.3022081986f};
  const f32x2 c2 = {-0.1029432397f, -0.1029432397f};
  f32x2 t = u * u;
  f32x2 a = u * (t * c2 + c1);
  f32x2 d = {1.f + __builtin_amdgcn_exp2f(a[0]), 1.f + __builtin_amdgcn_exp2f(a[1])};
  f32x2 r = {__builtin_amdgcn_rcpf(d[0]), __builtin_amdgcn_rcpf(d[1])};
  f32x2 o = u * r;
  return pk2(o[0], o[1]);
}
#define SCHED() __builtin_amdgcn_sched_barrier(0)
#define BAR() do { SCHED(); asm volatile("" ::: "memory"); __builtin_amdgcn_s_waitcnt(0xC07F); __builtin_amdgcn_s_barrier(); asm volatile("" ::: "memory"); SCHED(); } while (0)
__device__ __forceinline__ void glds16(const void* g, char* l) {
  __builtin_amdgcn_global_load_lds((const unsigned*)g, (LDS_AS unsigned*)l, 16, 0, 0);
}
__device__ __forceinline__ bf16x8 tr_frag(const char* p0, const char* p1) {
  s16x4 a = __builtin_amdgcn_ds_read_tr16_b64_v4i16((LDS_AS s16x4*)p0);
  s16x4 c = __builtin_amdgcn_ds_read_tr16_b64_v4i16((LDS_AS s16x4*)p1);
  return __builtin_shufflevector(a, c, 0, 1, 2, 3, 4, 5, 6, 7);
}

constexpr int YLD = 3136;
constexpr size_t WS_Y = 0;
constexpr size_t WS_Z2N = WS_Y + (size_t)S_ * YLD * 2 + 256;
constexpr size_t WS_OH = WS_Z2N + (size_t)S_ * 896 * 2;
constexpr size_t WS_WT = WS_OH + (size_t)S_ * KO * 2;
constexpr size_t WS_KVP = WS_WT + (size_t)D_ * KO * 2;
constexpr size_t WS_PHQ = WS_KVP + (size_t)8 * 16 * 56 * 32 * 4;
constexpr size_t WS_CNT = WS_PHQ + (size_t)16 * 128 * 2048;
constexpr size_t WS_END = WS_CNT + 48 * 128;

#define STAMP(K, I) do { } while (0)
__device__ __forceinline__ void st16_wt(void* p, u32x4 v) {
  asm volatile("global_store_dwordx4 %0, %1, off sc0 sc1\n\ts_nop 1" ::"v"(p), "v"(v) : "memory");
}
__device__ __forceinline__ void st8_wt(void* p, u32x2 v) {
  asm volatile("global_store_dwordx2 %0, %1, off sc0 sc1\n\ts_nop 1" ::"v"(p), "v"(v) : "memory");
}
#define SGB(mask, n) __builtin_amdgcn_sched_group_barrier(mask, n, 0)

__device__ __forceinline__ void phi_map(bf16x8 qf, bf16x8 wp0, bf16x8 wp1, int g, unsigned (&pp)[2][2],
                                        unsigned (&pm)[2][2]) {
  f32x4 z = {0.f, 0.f, 0.f, 0.f};
  f32x4 p0 = mfma16(wp0, qf, z);
  f32x4 p1 = mfma16(wp1, qf, z);
  float v[8];
  {
    unsigned a = pk2(p0[0], p0[1]), b = pk2(p0[2], p0[3]), c = pk2(p1[0], p1[1]), d = pk2(p1[2], p1[3]);
    v[0] = lo_f(a); v[1] = hi_f(a); v[2] = lo_f(b); v[3] = hi_f(b);
    v[4] = lo_f(c); v[5] = hi_f(c); v[6] = lo_f(d); v[7] = hi_f(d);
  }
  const bool valid1 = (g != 3);
  if (!valid1) { v[4] = v[0]; v[5] = v[0]; v[6] = v[0]; v[7] = v[0]; }
  float mx = fmaxf(fmaxf(fmaxf(v[0], v[1]), fmaxf(v[2], v[3])), fmaxf(fmaxf(v[4], v[5]), fmaxf(v[6], v[7])));
  float mn = fminf(fminf(fminf(v[0], v[1]), fminf(v[2], v[3])), fminf(fminf(v[4], v[5]), fminf(v[6], v[7])));
  mx = x32_max(x16_max(mx));
  mn = x32_min(x16_min(mn));
  const float L2E = 1.4426950408889634f;
  const float mxl = mx * L2E, mnl = mn * L2E;
  float ep[8], em[8];
  float sp = 0.f, sm = 0.f;
#pragma unroll
  for (int j = 0; j < 8; ++j) {
    ep[j] = __builtin_amdgcn_exp2f(__builtin_fmaf(v[j], L2E, -mxl));
    em[j] = __builtin_amdgcn_exp2f(__builtin_fmaf(v[j], -L2E, mnl));
    if (j >= 4) { ep[j] = valid1 ? ep[j] : 0.f; em[j] = valid1 ? em[j] : 0.f; }
    sp += ep[j];
    sm += em[j];
  }
  sp = x32_add(x16_add(sp));
  sm = x32_add(x16_add(sm));
  const float rp = __builtin_amdgcn_rcpf(sp), rm = __builtin_amdgcn_rcpf(sm);
  pp[0][0] = pk2(ep[0] * rp, ep[1] * rp); pp[0][1] = pk2(ep[2] * rp, ep[3] * rp);
  pp[1][0] = pk2(ep[4] * rp, ep[5] * rp); pp[1][1] = pk2(ep[6] * rp, ep[7] * rp);
  pm[0][0] = pk2(em[0] * rm, em[1] * rm); pm[0][1] = pk2(em[2] * rm, em[3] * rm);
  pm[1][0] = pk2(em[4] * rm, em[5] * rm); pm[1][1] = pk2(em[6] * rm, em[7] * rm);
}

constexpr int K1_XST = 128 * 128;
constexpr int K1_STAGE = K1_XST + 64 * 448;
constexpr int K1_LDS = 3 * K1_STAGE;

__global__ __launch_bounds__(768) void k1_kernel(const u16* __restrict__ x, const u16* __restrict__ wqkv,
                                                  const u16* __restrict__ wo, const u16* __restrict__ win,
                                                  const u16* __restrict__ wout, const u16* __restrict__ wsp,
                                                  char* __restrict__ ws) {
  __shared__ __attribute__((aligned(16))) char sm[K1_LDS];
  const int tid = threadIdx.x, lane = tid & 63, wave = __builtin_amdgcn_readfirstlane(tid >> 6);
  const int b = blockIdx.x;
  STAMP(0, 0);
  if (b >= 224) {
    const int kb = b - 224;
    const int k0 = kb * 64;
    if (kb < 21) {
    u16* T = (u16*)sm;
    constexpr int TS = 456;
    u32x4 v[5];
#pragma unroll
    for (int j = 0; j < 5; ++j) {
      int i = tid + 768 * j;
      if (i >= 64 * 56) i = tid;
      const int r = i / 56, c = i % 56;
      const int k = k0 + r;
      const u16* src = (k < 448) ? (wo + (size_t)k * 448) : (wout + (size_t)(k - 448) * 448);
      v[j] = *(const u32x4*)(src + c * 8);
    }
#pragma unroll
    for (int j = 0; j < 5; ++j) {
      const int i = tid + 768 * j;
      if (i < 64 * 56) *(u32x4*)(T + (i / 56) * TS + (i % 56) * 8) = v[j];
    }
    __syncthreads();
    u16* wT = (u16*)(ws + WS_WT);
    for (int i = tid; i < 448 * 8; i += 768) {
      const int n = i >> 3, c = i & 7;
      u16 t[8];
#pragma unroll
      for (int j = 0; j < 8; ++j) t[j] = T[(c * 8 + j) * TS + n];
      u32x4 o;
#pragma unroll
      for (int j = 0; j < 4; ++j) o[j] = (unsigned)t[2 * j] | ((unsigned)t[2 * j + 1] << 16);
      st16_wt(wT + ((size_t)((n >> 4) * 42 + (k0 >> 5) + (c >> 2)) * 64 + (c & 3) * 16 + (n & 15)) * 8, o);
    }
    }
    if (kb == 31 && tid < 48) *(unsigned*)(ws + WS_CNT + tid * 128) = 0u;
    STAMP(0, 1);
    {
      const char* p = (const char*)wsp + (size_t)kb * 262144 + tid * 16;
      unsigned a = 0;
#pragma unroll
      for (int i = 0; i < 21; ++i) {
        u32x4 v = *(const u32x4*)(p + i * 12288);
        a |= v[0] & v[1] & v[2] & v[3];
      }
      asm volatile("" ::"v"(a));
    }
    STAMP(0, 31);
    return;
  }

  const int xcd = b & 7, idx = b >> 3;
  const int cb = (xcd & 1) * 7 + idx % 7;
  const int rt = (xcd >> 1) * 4 + idx / 7;
  const int row0 = rt * 128;
  const int g = lane >> 4, r16 = lane & 15;
  const int wm = (wave >> 1) & 3, wn = wave & 1;
  constexpr int RST = 464;

  if (wave >= 8) {
    const int lw = wave - 8;
    const u16* W = (cb < 6) ? (wqkv + 224 * cb) : (win + 224 * (cb - 6));
    const int ldw = (cb < 6) ? NQKV : NIN;
    const u16* src[11];
#pragma unroll
    for (int j = 0; j < 11; ++j) {
      const int i = lw + 4 * j;
      if (j < 4) {
        const int row = 8 * i + (lane >> 3);
        src[j] = x + (size_t)(row0 + row) * D_ + (((lane & 7) ^ ((row >> 1) & 7)) << 3);
      } else {
        const int o = (i - 16) * 1024 + lane * 16;
        const int k = o / 448, cp = (o % 448) >> 4;
        const int G = (cp >> 1) ^ ((k >> 3) & 1);
        src[j] = W + (size_t)k * ldw + 8 * (2 * G + (cp & 1));
      }
    }
    const int wstep = 64 * ldw;
    char* dst0 = sm + lw * 1024;
#pragma unroll
    for (int s = 0; s < 2; ++s)
#pragma unroll
      for (int j = 0; j < 11; ++j) glds16(src[j] + s * (j < 4 ? 64 : wstep), dst0 + s * K1_STAGE + j * 4096);
    int nb = 2;
#pragma unroll 1
    for (int t = 0; t < 7; ++t) {
      if (t < 6) asm volatile("s_waitcnt vmcnt(11)" ::: "memory");
      else asm volatile("s_waitcnt vmcnt(0)" ::: "memory");
      __builtin_amdgcn_s_barrier();
      if (t + 2 < 7) {
        char* dst = dst0 + nb * K1_STAGE;
#pragma unroll
        for (int j = 0; j < 11; ++j) glds16(src[j] + (t + 2) * (j < 4 ? 64 : wstep), dst + j * 4096);
        nb = (nb == 2) ? 0 : nb + 1;
      }
    }
    __builtin_amdgcn_s_barrier();
    __builtin_amdgcn_s_barrier();
  } else {
    f32x4 acc[7][2], sacc = {0.f, 0.f, 0.f, 0.f}, qacc = {0.f, 0.f, 0.f, 0.f}, cacc[2];
#pragma unroll
    for (int nt = 0; nt < 7; ++nt) { acc[nt][0] = f32x4{0.f, 0.f, 0.f, 0.f}; acc[nt][1] = f32x4{0.f, 0.f, 0.f, 0.f}; }
    cacc[0] = f32x4{0.f, 0.f, 0.f, 0.f}; cacc[1] = f32x4{0.f, 0.f, 0.f, 0.f};
    const bf16x8 ones = {0x3F80, 0x3F80, 0x3F80, 0x3F80, 0x3F80, 0x3F80, 0x3F80, 0x3F80};
    const int sw = (r16 >> 1) & 7;
    const int xrow = (32 * wm + 16 * wn + r16) * 128, xd = wn ? -2048 : 2048;
    const int xo0 = xrow + ((g ^ sw) << 4), xo1 = xrow + (((4 | g) ^ sw) << 4);
    int woff[7];
#pragma unroll
    for (int i = 0; i < 7; ++i) {
      int nt = i + 2 * wm;
      nt = (nt >= 7) ? nt - 7 : nt;
      const int G = 7 * wn + nt;
      woff[i] = K1_XST + (8 * g + ((lane >> 2) & 3)) * 448 + (lane & 3) * 8 + ((G ^ (g & 1)) << 5);
    }
    bf16x8 xa[2], wa[7], xb[2], wb[7];
#define K1_READ(XF, WF, SB, KS)                                                              \
  do {                                                                                       \
    XF[0] = *(const bf16x8*)((SB) + ((KS) ? xo1 : xo0));                                     \
    XF[1] = *(const bf16x8*)((SB) + ((KS) ? xo1 : xo0) + xd);                                \
    _Pragma("unroll") for (int nt = 0; nt < 7; ++nt) {                                       \
      const char* p_ = (SB) + woff[nt] + (KS)*32 * 448;                                      \
      WF[nt] = tr_frag(p_, p_ + 4 * 448);                                                    \
    }                                                                                        \
  } while (0)
#define K1_MMA(XF, WF)                                                                       \
  do {                                                                                       \
    _Pragma("unroll") for (int nt = 0; nt < 7; ++nt) acc[nt][0] = mfma16(WF[nt], XF[0], acc[nt][0]); \
    sacc = mfma16(ones, XF[0], sacc);                                                        \
    qacc = mfma16(XF[0], XF[0], qacc);                                                       \
    _Pragma("unroll") for (int nt = 0; nt < 7; ++nt) acc[nt][1] = mfma16(WF[nt], XF[1], acc[nt][1]); \
    cacc[0] = mfma16(WF[0], ones, cacc[0]);                                                  \
    cacc[1] = mfma16(WF[1], ones, cacc[1]);                                                  \
  } while (0)
    int rb = 0;
#pragma unroll 1
    for (int t = 0; t < 7; ++t) {
      BAR();
      STAMP(0, 3 + t);
      const char* sb = sm + rb * K1_STAGE;
      rb = (rb == 2) ? 0 : rb + 1;
      K1_READ(xa, wa, sb, 0);
      SCHED();
      K1_READ(xb, wb, sb, 1);
      K1_MMA(xa, wa);
      _Pragma("unroll") for (int i_ = 0; i_ < 8; ++i_) { SGB(0x008, 1); SGB(0x100, 2); }
      SCHED();
      K1_MMA(xb, wb);
    }
    BAR();
    STAMP(0, 17);
    float* CS = (float*)(sm + 128 * RST);
    f32x2* RS_ = (f32x2*)(CS + 224);
    if (r16 == 0) {
#pragma unroll
      for (int i = 0; i < 2; ++i) {
        int nt = i + 2 * wm;
        nt = (nt >= 7) ? nt - 7 : nt;
        *(f32x4*)(CS + 112 * wn + 16 * nt + 4 * g) = cacc[i];
      }
    }
    {
      const float qd = (r16 & 3) == 0 ? qacc[0] : (r16 & 3) == 1 ? qacc[1] : (r16 & 3) == 2 ? qacc[2] : qacc[3];
      if (g == (r16 >> 2)) {
        const float mean = sacc[0] * (1.f / 448.f);
        const float var = fmaxf(qd * (1.f / 448.f) - mean * mean, 0.f);
        const float sd = rbf(sqrtf(rbf(rbf(var) + 1e-5f)));
        const float r = 1.f / sd;
        RS_[32 * wm + 16 * wn + r16] = f32x2{r, r * rbf(mean)};
      }
    }
    BAR();
    f32x2 st[2];
    st[0] = RS_[32 * wm + 16 * wn + r16];
    st[1] = RS_[32 * wm + 16 * (wn ^ 1) + r16];
#pragma unroll
    for (int i = 0; i < 7; ++i) {
      int nt = i + 2 * wm;
      nt = (nt >= 7) ? nt - 7 : nt;
      const f32x4 c4 = *(const f32x4*)(CS + 112 * wn + 16 * nt + 4 * g);
#pragma unroll
      for (int j = 0; j < 2; ++j) {
        char* dst = sm + (32 * wm + 16 * (j ^ wn) + r16) * RST + (112 * wn + 4 * g) * 2 + 32 * nt;
        const f32x4 v = acc[i][j] * st[j][0] - c4 * st[j][1];
        *(u32x2*)(dst) = u32x2{pk2(v[0], v[1]), pk2(v[2], v[3])};
      }
    }
  }
  __syncthreads();
  u16* dstb = (u16*)(ws + WS_Y) + (size_t)row0 * YLD + 224 * cb;
#pragma unroll
  for (int i = 0; i < 5; ++i) {
    const int id = tid + 768 * i;
    if (id < 128 * 28) {
      const int s = id / 28, c = id % 28;
      st16_wt(dstb + (size_t)s * YLD + c * 8, *(const u32x4*)(sm + s * RST + c * 16));
    }
  }
  STAMP(0, 31);
}

__device__ __forceinline__ float red64_add(float v) {
  v = red8_add(v);
  v += __uint_as_float(__builtin_amdgcn_mov_dpp(__float_as_uint(v), 0x140, 0xf, 0xf, true));
  return x32_add(x16_add(v));
}

constexpr int K2_PW = 64 * 80 + 32 * 64;
__global__ __launch_bounds__(512) void k2_kernel(const u16* __restrict__ wphi, char* __restrict__ ws) {
  __shared__ __attribute__((aligned(16))) char sm[8 * 56 * 32 * 4 + 2048];
  const int tid = threadIdx.x, lane = tid & 63, wave = __builtin_amdgcn_readfirstlane(tid >> 6);
  const int b = blockIdx.x;
  const u16* Y = (const u16*)(ws + WS_Y);
  STAMP(1, 0);
  if (b < 128) {
    const int h = b & 15, rg = b >> 4;
    const int g = lane >> 4, r16 = lane & 15;
    const int rowb = rg * 256 + wave * 32;
    u32x2 kv_[2][2];
#pragma unroll
    for (int st = 0; st < 2; ++st) {
      const u16* kp = Y + (size_t)(rowb + st * 16 + r16) * YLD + 448 + h * 28 + 8 * g;
      kv_[st][0] = *(const u32x2*)(kp);
      kv_[st][1] = *(const u32x2*)(kp + 4);
    }
    u32x2 vv[4];
#pragma unroll
    for (int i = 0; i < 4; ++i) {
      const int row = (lane >> 3) + 8 * i, pc = lane & 7;
      vv[i] = *(const u32x2*)(Y + (size_t)(rowb + row) * YLD + 896 + h * 28 + 4 * (pc < 7 ? pc : 0));
      if (pc == 7) vv[i] = u32x2{0x00003F80u, 0u};
    }
    u32x4 wraw = {0u, 0u, 0u, 0u};
    if (tid < 98) wraw = *(const u32x4*)(wphi + (size_t)h * 784 + tid * 8);
    char* WP = sm + 8 * 56 * 32 * 4;
    if (tid < 98) *(u32x4*)(WP + tid * 16) = wraw;
    char* PK = sm + wave * K2_PW;
    char* VS = PK + 64 * 80;
    __syncthreads();
    STAMP(1, 1);
    const char* wp = WP + (8 * g + ((lane >> 2) & 3)) * 56 + (lane & 3) * 8;
    bf16x8 wp0 = tr_frag(wp, wp + 4 * 56);
    bf16x8 wp1 = tr_frag(wp + 32, wp + 4 * 56 + 32);
    if (g == 3) {
#pragma unroll
      for (int j = 4; j < 8; ++j) { wp0[j] = 0; wp1[j] = 0; }
    }
    f32x4 kacc[4][2];
#pragma unroll
    for (int a = 0; a < 4; ++a) { kacc[a][0] = f32x4{0.f, 0.f, 0.f, 0.f}; kacc[a][1] = f32x4{0.f, 0.f, 0.f, 0.f}; }
#pragma unroll
    for (int pr = 0; pr < 1; ++pr) {
#pragma unroll
      for (int i = 0; i < 4; ++i) {
        const int row = (lane >> 3) + 8 * i;
        const int slot = 2 * (row & 15) + (row >> 4);
        *(u32x2*)(VS + slot * 64 + (lane & 7) * 8) = vv[4 * pr + i];
      }
      unsigned pp[2][2][2], pm[2][2][2];
#pragma unroll
      for (int mt = 0; mt < 2; ++mt) {
        u32x4 q4 = {kv_[2 * pr + mt][0][0], kv_[2 * pr + mt][0][1], kv_[2 * pr + mt][1][0], kv_[2 * pr + mt][1][1]};
        if (g == 3) { q4[2] = 0u; q4[3] = 0u; }
        phi_map(__builtin_bit_cast(bf16x8, q4), wp0, wp1, g, pp[mt], pm[mt]);
      }
#pragma unroll
      for (int ft = 0; ft < 2; ++ft) {
        if (ft == 0 || g != 3) {
          char* d0 = PK + (16 * ft + 4 * g) * 80 + r16 * 4;
#pragma unroll
          for (int j = 0; j < 2; ++j) {
            *(unsigned*)(d0 + (2 * j) * 80) = (pp[0][ft][j] & 0xffffu) | (pp[1][ft][j] << 16);
            *(unsigned*)(d0 + (2 * j + 1) * 80) = (pp[0][ft][j] >> 16) | (pp[1][ft][j] & 0xffff0000u);
            *(unsigned*)(d0 + (28 + 2 * j) * 80) = (pm[0][ft][j] & 0xffffu) | (pm[1][ft][j] << 16);
            *(unsigned*)(d0 + (28 + 2 * j + 1) * 80) = (pm[0][ft][j] >> 16) | (pm[1][ft][j] & 0xffff0000u);
          }
        }
      }
      __builtin_amdgcn_s_waitcnt(0xC07F);
      __builtin_amdgcn_wave_barrier();
      bf16x8 bfr[2];
      {
        const char* vp = VS + (8 * g + ((lane >> 2) & 3)) * 64 + (lane & 3) * 8;
        bfr[0] = tr_frag(vp, vp + 4 * 64);
        bfr[1] = tr_frag(vp + 32, vp + 4 * 64 + 32);
      }
#pragma unroll
      for (int a = 0; a < 4; ++a) {
        bf16x8 af = *(const bf16x8*)(PK + (16 * a + r16) * 80 + g * 16);
        kacc[a][0] = mfma16(af, bfr[0], kacc[a][0]);
        kacc[a][1] = mfma16(af, bfr[1], kacc[a][1]);
      }
      __builtin_amdgcn_s_waitcnt(0xC07F);
      __builtin_amdgcn_wave_barrier();
    }
    STAMP(1, 2);
    __syncthreads();
    float* red = (float*)sm + wave * (56 * 32);
#pragma unroll
    for (int a = 0; a < 4; ++a)
#pragma unroll
      for (int c = 0; c < 2; ++c)
#pragma unroll
        for (int j = 0; j < 4; ++j) {
          const int f = 16 * a + 4 * g + j;
          if (f < 56) red[f * 32 + 16 * c + r16] = kacc[a][c][j];
        }
    __syncthreads();
    if (tid < 448) {
      const f32x4* rp = (const f32x4*)sm + tid;
      f32x4 s = (rp[0] + rp[448]) + (rp[2 * 448] + rp[3 * 448]);
      s += (rp[4 * 448] + rp[5 * 448]) + (rp[6 * 448] + rp[7 * 448]);
      st16_wt((f32x4*)(ws + WS_KVP) + (size_t)(rg * 16 + h) * 448 + tid, __builtin_bit_cast(u32x4, s));
    }
  } else if (b >= 256) {
    const int h = b & 15, rg = (b - 256) >> 4;
    const int g = lane >> 4, r16 = lane & 15;
    u32x2 qv[2][2];
#pragma unroll
    for (int st = 0; st < 2; ++st) {
      const u16* qp = Y + (size_t)(rg * 256 + wave * 32 + st * 16 + r16) * YLD + h * 28 + 8 * g;
      qv[st][0] = *(const u32x2*)(qp);
      qv[st][1] = *(const u32x2*)(qp + 4);
    }
    u32x4 wraw = {0u, 0u, 0u, 0u};
    if (tid < 98) wraw = *(const u32x4*)(wphi + (size_t)h * 784 + tid * 8);
    if (tid < 98) *(u32x4*)(sm + tid * 16) = wraw;
    __syncthreads();
    const char* wp = sm + (8 * g + ((lane >> 2) & 3)) * 56 + (lane & 3) * 8;
    bf16x8 wp0 = tr_frag(wp, wp + 4 * 56);
    bf16x8 wp1 = tr_frag(wp + 32, wp + 4 * 56 + 32);
    if (g == 3) {
#pragma unroll
      for (int j = 4; j < 8; ++j) { wp0[j] = 0; wp1[j] = 0; }
    }
#pragma unroll
    for (int st = 0; st < 2; ++st) {
      u32x4 q4 = {qv[st][0][0], qv[st][0][1], qv[st][1][0], qv[st][1][1]};
      if (g == 3) { q4[2] = 0u; q4[3] = 0u; }
      unsigned pp[2][2], pm[2][2];
      phi_map(__builtin_bit_cast(bf16x8, q4), wp0, wp1, g, pp, pm);
      u32x4* dst = (u32x4*)(ws + WS_PHQ) + ((size_t)(h * 128 + rg * 16 + wave * 2 + st) * 2) * 64 + lane;
      st16_wt(dst, u32x4{pp[0][0], pp[0][1], pp[1][0], pp[1][1]});
      st16_wt(dst + 64, u32x4{pm[0][0], pm[0][1], pm[1][0], pm[1][1]});
    }
  } else {
    const int row0 = (b - 128) * 16 + wave * 2;
    u16* z2n = (u16*)(ws + WS_Z2N);
    const int le = (lane < 56) ? lane : 0;
    u32x4 v[2][2];
#pragma unroll
    for (int r = 0; r < 2; ++r)
#pragma unroll
      for (int c = 0; c < 2; ++c) v[r][c] = *(const u32x4*)(Y + (size_t)(row0 + r) * YLD + 2240 + (le + 56 * c) * 8);
#pragma unroll
    for (int r = 0; r < 2; ++r) {
      float s = 0.f;
#pragma unroll
      for (int c = 0; c < 2; ++c)
#pragma unroll
        for (int j = 0; j < 4; ++j) {
          v[r][c][j] = gelu_pk(v[r][c][j]);
          s = dot2(v[r][c][j], 0x3F803F80u, s);
        }
      if (lane >= 56) s = 0.f;
      const float mean = red64_add(s) * (1.f / 896.f);
      const float mb = rbf(mean);
      float q = 0.f;
#pragma unroll
      for (int c = 0; c < 2; ++c)
#pragma unroll
        for (int j = 0; j < 4; ++j) {
          const float a = lo_f(v[r][c][j]) - mean, e = hi_f(v[r][c][j]) - mean;
          q += a * a + e * e;
          v[r][c][j] = pk2(lo_f(v[r][c][j]) - mb, hi_f(v[r][c][j]) - mb);
        }
      if (lane >= 56) q = 0.f;
      const float var = red64_add(q) * (1.f / 896.f);
      const float sd = rbf(sqrtf(rbf(rbf(var) + 1e-5f)));
      const float inv = 1.f / sd;
      if (lane < 56) {
#pragma unroll
        for (int c = 0; c < 2; ++c) {
          u32x4 o;
#pragma unroll
          for (int j = 0; j < 4; ++j) o[j] = pk2(lo_f(v[r][c][j]) * inv, hi_f(v[r][c][j]) * inv);
          *(u32x4*)(sm + (wave * 2 + r) * 1808 + (lane + 56 * c) * 16) = o;
        }
      }
    }
    __syncthreads();
    {
      const int rb0 = (b - 128) * 16;
      const int kb = rb0 >> 5, half = (rb0 >> 4) & 1;
      const int g = lane >> 4;
#pragma unroll
      for (int i = 0; i < 4; ++i) {
        const int u = wave + 8 * i;
        if (u < 28) {
          const int T = 2 * u + (g >> 1), gp = g & 1;
          const char* p = sm + (8 * gp + ((lane >> 2) & 3)) * 1808 + T * 32 + (lane & 3) * 8;
          bf16x8 fr = tr_frag(p, p + 4 * 1808);
          const int pn = T / 7, n = T % 7;
          st16_wt(z2n + ((size_t)((pn * 64 + kb) * 7 + n) * 64 + (2 * half + gp) * 16 + (lane & 15)) * 8, __builtin_bit_cast(u32x4, fr));
        }
      }
    }
  }
  STAMP(1, 31);
}

constexpr int K3_RS = 464;
constexpr int K3_STAGE = 64 * 256;
constexpr int K3_NB = 8;
constexpr int K3_AUX = K3_NB * K3_STAGE;
__device__ __forceinline__ void glds16_asm(const void* gsrc, unsigned lds_dst) {
  unsigned keep;
  asm volatile("s_mov_b32 %0, m0\n\ts_mov_b32 m0, %2\n\ts_nop 0\n\tglobal_load_lds_dwordx4 %1, off\n\ts_mov_b32 m0, %0"
               : "=&s"(keep) : "v"(gsrc), "s"(lds_dst) : "memory");
}
__global__ __launch_bounds__(512) void k3_kernel(const u16* __restrict__ wsp, const u16* __restrict__ bsp,
                                                  const u16* __restrict__ x, char* __restrict__ ws,
                                                  u16* __restrict__ out) {
  __shared__ __attribute__((aligned(16))) char sm[K3_NB * K3_STAGE + 32 * 144];
  const int tid = threadIdx.x, lane = tid & 63, wave = __builtin_amdgcn_readfirstlane(tid >> 6);
  const int b = blockIdx.x;
  const int xcd = b & 7, idx = b >> 3;
  const int mt_ = (xcd >> 1) * 8 + (idx & 7);
  const int nt_ = (xcd & 1) * 4 + (idx >> 3);
  const int t0 = mt_ * 64, f0 = nt_ * 112;
  const u16* z2n = (const u16*)(ws + WS_Z2N);
  const u16* Y = (const u16*)(ws + WS_Y);
  u16* oh = (u16*)(ws + WS_OH);
  const int g = lane >> 4, r16 = lane & 15;
  const int h = b & 15;
  u32x4 zv[2];
  float bb[2];
  STAMP(2, 0);
  const int kq = wave >> 1, wn = (wave & 1) ^ (wave >> 2);
  const u16* bp = z2n + ((size_t)(nt_ * 64 + kq) * 7 + 4 * wn) * 512 + lane * 8;
  const int arow = r16 * 256 + (((4 * kq + g) ^ r16) << 4);
  const u16* src0;
  const u16* src1;
  {
    const int row = 4 * wave + (lane >> 4);
    src0 = wsp + (size_t)(t0 + row) * S_ + (((lane & 15) ^ (row & 15)) << 3);
    src1 = src0 + (size_t)32 * S_;
  }
  const unsigned ldsb = __builtin_amdgcn_readfirstlane((unsigned)(size_t)(LDS_AS char*)sm) + wave * 1024;
#define K3_AISSUE(T)                                                                            \
  do {                                                                                          \
    glds16_asm(src0 + (T)*128, ldsb + ((T)&7) * K3_STAGE);                                      \
    glds16_asm(src1 + (T)*128, ldsb + ((T)&7) * K3_STAGE + 8192);                               \
  } while (0)
  f32x4 acc[4][4];
  bf16x8 aA[4], aB[4], b0[4], b1[4], b2[4], b3[4];
#define K3_BLOAD(BF, T, NT)                                                                     \
  do { _Pragma("unroll") for (int n = 0; n < NT; ++n) BF[n] = *(const bf16x8*)(bp + ((T)*28 + n) * 512); } while (0)
#define K3_AREAD(AF, T)                                                                         \
  do { _Pragma("unroll") for (int m = 0; m < 4; ++m) AF[m] = *(const bf16x8*)(sm + ((T)&7) * K3_STAGE + arow + m * 4096); } while (0)
#define K3_MMA(AF, BF, NT)                                                                      \
  do {                                                                                          \
    _Pragma("unroll") for (int n = 0; n < NT; ++n)                                              \
      _Pragma("unroll") for (int m = 0; m < 4; ++m) acc[n][m] = mfma16(BF[n], AF[m], acc[n][m]); \
  } while (0)
#define K3_STEP(T, AC, AN, BC, BL, NT)                                                          \
  do {                                                                                          \
    asm volatile("s_waitcnt vmcnt(12)" ::: "memory");                                           \
    BAR();                                                                                      \
    STAMP(2, 5 + (T));                                                                          \
    if ((T) + 8 < 16) K3_AISSUE((T) + 8);                                                       \
    if ((T) + 1 < 16) K3_AREAD(AN, (T) + 1);                                                    \
    if ((T) + 3 < 16) K3_BLOAD(BL, (T) + 3, NT);                                                \
    K3_MMA(AC, BC, NT);                                                                         \
    SGB(0x100, 4);                                                                              \
    _Pragma("unroll") for (int i_ = 0; i_ < NT; ++i_) { SGB(0x008, 2); SGB(0x020, 1); }         \
  } while (0)
#define K3_LOOP(NT) \
  do { \
    STAMP(2, 4); \
    K3_AREAD(aA, 0); \
    K3_STEP(0, aA, aB, b0, b3, NT); \
    K3_STEP(1, aB, aA, b1, b0, NT); \
    K3_STEP(2, aA, aB, b2, b1, NT); \
    K3_STEP(3, aB, aA, b3, b2, NT); \
    K3_STEP(4, aA, aB, b0, b3, NT); \
    K3_STEP(5, aB, aA, b1, b0, NT); \
    K3_STEP(6, aA, aB, b2, b1, NT); \
    K3_STEP(7, aB, aA, b3, b2, NT); \
    K3_STEP(8, aA, aB, b0, b3, NT); \
    K3_STEP(9, aB, aA, b1, b0, NT); \
    K3_STEP(10, aA, aB, b2, b1, NT); \
    K3_STEP(11, aB, aA, b3, b2, NT); \
    K3_STEP(12, aA, aB, b0, b3, NT); \
    _Pragma("unroll") for (int i = 0; i < 2; ++i) {                                             \
      int id = tid + 512 * i;                                                                   \
      if (id >= 896) id = tid;                                                                  \
      const int t = id / 14, c = id % 14;                                                       \
      zv[i] = *(const u32x4*)(Y + (size_t)(t0 + t) * YLD + 1344 + f0 + c * 8);                  \
      bb[i] = bf2f(bsp[t0 + t]);                                                                \
    }                                                                                           \
    K3_STEP(13, aB, aA, b1, b0, NT); \
    {                                                                                           \
      const int te = (tid < 448) ? tid : 0;                                                     \
      const f32x4* kvp = (const f32x4*)(ws + WS_KVP) + (size_t)h * 448 + te;                    \
      _Pragma("unroll") for (int p = 0; p < 8; ++p) part[p] = kvp[(size_t)p * 16 * 448];        \
      const u32x4* pq = (const u32x4*)(ws + WS_PHQ) + ((size_t)(h * 128 + (b >> 4) * 8 + wave) * 2) * 64 + lane; \
      c1 = pq[0];                                                                               \
      c2 = pq[64];                                                                              \
    }                                                                                           \
    K3_STEP(14, aA, aB, b2, b1, NT); \
    K3_STEP(15, aB, aA, b3, b2, NT); \
  } while (0)
  K3_AISSUE(0);
  K3_AISSUE(1);
  if (wn == 0) { K3_BLOAD(b0, 0, 4); } else { K3_BLOAD(b0, 0, 3); }
  K3_AISSUE(2);
  if (wn == 0) { K3_BLOAD(b1, 1, 4); } else { K3_BLOAD(b1, 1, 3); }
  K3_AISSUE(3);
  if (wn == 0) { K3_BLOAD(b2, 2, 4); } else { K3_BLOAD(b2, 2, 3); }
  K3_AISSUE(4); K3_AISSUE(5); K3_AISSUE(6); K3_AISSUE(7);
  asm volatile("s_waitcnt vmcnt(20)" ::: "memory");
  STAMP(2, 1);
  BAR();
  f32x4 part[8];
  u32x4 c1, c2;
  SCHED();
#pragma unroll
  for (int n = 0; n < 4; ++n)
#pragma unroll
    for (int m = 0; m < 4; ++m) acc[n][m] = f32x4{0.f, 0.f, 0.f, 0.f};
  if (wn == 0) K3_LOOP(4);
  else K3_LOOP(3);
  {
    char* KT = sm + K3_AUX;
    if (tid < 448) {
      const f32x4 s = ((part[0] + part[1]) + (part[2] + part[3])) + ((part[4] + part[5]) + (part[6] + part[7]));
      const int fp = tid >> 3, d0 = 4 * (tid & 7);
      const int mf = (fp >= 28) ? 1 : 0, f = fp - 28 * mf;
      const int pos = mf * 32 + ((f & 15) >> 2) * 8 + (f & 3) + 4 * (f >> 4);
      const unsigned a = pk2(s[0], s[1]), c = pk2(s[2], s[3]);
      char* d = KT + d0 * 144 + pos * 2;
      *(u16*)(d) = (u16)a; *(u16*)(d + 144) = (u16)(a >> 16);
      *(u16*)(d + 288) = (u16)c; *(u16*)(d + 432) = (u16)(c >> 16);
    } else {
      const int i = tid - 448, d = i >> 1, mf = i & 1;
      *(u32x2*)(KT + d * 144 + (mf * 32 + 28) * 2) = u32x2{0u, 0u};
    }
  }
  BAR();
  STAMP(2, 21);
  {
    const char* KT = sm + K3_AUX;
    bf16x8 af[2][2];
#pragma unroll
    for (int dt = 0; dt < 2; ++dt)
#pragma unroll
      for (int mf = 0; mf < 2; ++mf) af[dt][mf] = *(const bf16x8*)(KT + (16 * dt + r16) * 144 + (32 * mf + 8 * g) * 2);
    f32x4 o0 = {0.f, 0.f, 0.f, 0.f}, o1 = {0.f, 0.f, 0.f, 0.f};
    o0 = mfma16(af[0][0], __builtin_bit_cast(bf16x8, c1), o0);
    o1 = mfma16(af[1][0], __builtin_bit_cast(bf16x8, c1), o1);
    o0 = mfma16(af[0][1], __builtin_bit_cast(bf16x8, c2), o0);
    o1 = mfma16(af[1][1], __builtin_bit_cast(bf16x8, c2), o1);
    float den = __shfl(o1[0], 48 + r16, 64);
    den = rbf(rbf(den) + 1e-6f);
    const float inv = 1.f / den;
    const int mtile = (b >> 4) * 8 + wave;
    {
      const int k = h * 28 + 4 * g;
      u16* dst = oh + ((size_t)(mtile * 42 + (k >> 5)) * 64 + ((k >> 3) & 3) * 16 + r16) * 8 + (k & 7);
      st8_wt(dst, u32x2{pk2(rbf(o0[0]) * inv, rbf(o0[1]) * inv), pk2(rbf(o0[2]) * inv, rbf(o0[3]) * inv)});
    }
    if (g != 3) {
      const int k = h * 28 + 16 + 4 * g;
      u16* dst = oh + ((size_t)(mtile * 42 + (k >> 5)) * 64 + ((k >> 3) & 3) * 16 + r16) * 8 + (k & 7);
      st8_wt(dst, u32x2{pk2(rbf(o1[0]) * inv, rbf(o1[1]) * inv), pk2(rbf(o1[2]) * inv, rbf(o1[3]) * inv)});
    }
  }
  {
    char* rd = sm + kq * (64 * K3_RS) + r16 * K3_RS + (64 * wn + 4 * g) * 4;
#pragma unroll
    for (int n = 0; n < 4; ++n)
      if (n < 3 || wn == 0) {
#pragma unroll
        for (int m = 0; m < 4; ++m) *(f32x4*)(rd + 16 * m * K3_RS + 64 * n) = acc[n][m];
      }
  }
  BAR();
#pragma unroll
  for (int i = 0; i < 2; ++i) {
    const int id = tid + 512 * i;
    if (id < 896) {
      const int t = id / 14, c = id % 14;
      const char* p = sm + t * K3_RS + c * 32;
      f32x4 v0 = (*(const f32x4*)(p) + *(const f32x4*)(p + 64 * K3_RS)) +
                 (*(const f32x4*)(p + 128 * K3_RS) + *(const f32x4*)(p + 192 * K3_RS));
      f32x4 v1 = (*(const f32x4*)(p + 16) + *(const f32x4*)(p + 64 * K3_RS + 16)) +
                 (*(const f32x4*)(p + 128 * K3_RS + 16) + *(const f32x4*)(p + 192 * K3_RS + 16));
      u32x4 z;
#pragma unroll
      for (int j = 0; j < 4; ++j) z[j] = gelu_pk(zv[i][j]);
      u32x4 o;
      o[0] = pk2(lo_f(z[0]) * rbf(rbf(v0[0]) + bb[i]), hi_f(z[0]) * rbf(rbf(v0[1]) + bb[i]));
      o[1] = pk2(lo_f(z[1]) * rbf(rbf(v0[2]) + bb[i]), hi_f(z[1]) * rbf(rbf(v0[3]) + bb[i]));
      o[2] = pk2(lo_f(z[2]) * rbf(rbf(v1[0]) + bb[i]), hi_f(z[2]) * rbf(rbf(v1[1]) + bb[i]));
      o[3] = pk2(lo_f(z[3]) * rbf(rbf(v1[2]) + bb[i]), hi_f(z[3]) * rbf(rbf(v1[3]) + bb[i]));
      const int k = 448 + f0 + c * 8, tt = t0 + t;
      st16_wt(oh + ((size_t)((tt >> 4) * 42 + (k >> 5)) * 64 + ((k >> 3) & 3) * 16 + (tt & 15)) * 8, o);
    }
  }
  asm volatile("s_waitcnt vmcnt(0)" ::: "memory");
  BAR();
  unsigned* cnt = (unsigned*)(ws + WS_CNT);
  if (tid == 0) {
    __hip_atomic_fetch_add(cnt + mt_ * 32, 1u, __ATOMIC_RELAXED, __HIP_MEMORY_SCOPE_AGENT);
    __hip_atomic_fetch_add(cnt + (32 + (b >> 4)) * 32, 1u, __ATOMIC_RELAXED, __HIP_MEMORY_SCOPE_AGENT);
  }
  STAMP(2, 22);
  {
    const int m4 = mt_ * 2 + (nt_ & 1), n4 = nt_ >> 1;
    const int r0 = m4 * 32, n0 = n4 * 112;
    const int wv = (wave < 7) ? wave : 6;
    const u16* ap = (const u16*)(ws + WS_OH) + ((size_t)(m4 * 2 * 42 + wv * 6) * 64 + lane) * 8;
    const u16* wbp = (const u16*)(ws + WS_WT) + ((size_t)(n4 * 7 * 42 + wv * 6) * 64 + lane) * 8;
    bf16x8 w0[7], w1[7], w2[7], w3[7], av[6][2];
#define K4_BL(BF, KS) do { _Pragma("unroll") for (int n = 0; n < 7; ++n) BF[n] = *(const bf16x8*)(wbp + (n * 42 + (KS)) * 512); } while (0)
    K4_BL(w0, 0); K4_BL(w1, 1); K4_BL(w2, 2); K4_BL(w3, 3);
    u32x4 xv = *(const u32x4*)(x + (size_t)(r0 + (tid < 448 ? tid / 14 : 0)) * D_ + n0 + (tid < 448 ? tid % 14 : 0) * 8);
    SCHED();
    if (tid == 0) {
      const unsigned* c0 = cnt + mt_ * 32;
      const unsigned* c1_ = cnt + (32 + (t0 >> 7)) * 32;
      for (int spin = 0; spin < (1 << 22); ++spin) {
        const unsigned h0 = __hip_atomic_load(c0, __ATOMIC_RELAXED, __HIP_MEMORY_SCOPE_AGENT);
        const unsigned h1 = __hip_atomic_load(c1_, __ATOMIC_RELAXED, __HIP_MEMORY_SCOPE_AGENT);
        if (h0 >= 8u && h1 >= 16u) break;
        __builtin_amdgcn_s_sleep(2);
      }
      __builtin_amdgcn_fence(__ATOMIC_ACQUIRE, "agent");
      asm volatile("s_waitcnt vmcnt(0)" ::: "memory");
    }
    BAR();
    STAMP(2, 23);
#pragma unroll
    for (int ks = 0; ks < 6; ++ks)
#pragma unroll
      for (int m = 0; m < 2; ++m) av[ks][m] = *(const bf16x8*)(ap + (m * 42 + ks) * 512);
    SCHED();
    f32x4 oc[7][2];
#pragma unroll
    for (int n = 0; n < 7; ++n) { oc[n][0] = f32x4{0.f, 0.f, 0.f, 0.f}; oc[n][1] = f32x4{0.f, 0.f, 0.f, 0.f}; }
#define K4_MM(BF, KS)                                                                           \
  do {                                                                                          \
    _Pragma("unroll") for (int n = 0; n < 7; ++n) {                                             \
      oc[n][0] = mfma16(BF[n], av[KS][0], oc[n][0]);                                            \
      oc[n][1] = mfma16(BF[n], av[KS][1], oc[n][1]);                                            \
    }                                                                                           \
    SCHED();                                                                                    \
  } while (0)
    K4_MM(w0, 0); K4_BL(w0, 4); SCHED();
    K4_MM(w1, 1); K4_BL(w1, 5); SCHED();
    K4_MM(w2, 2);
    K4_MM(w3, 3);
    K4_MM(w0, 4);
    K4_MM(w1, 5);
    STAMP(2, 24);
    if (wave < 7) {
      char* rd = sm + wave * (32 * K3_RS) + r16 * K3_RS + 16 * g;
#pragma unroll
      for (int n = 0; n < 7; ++n)
#pragma unroll
        for (int m = 0; m < 2; ++m) *(f32x4*)(rd + 16 * m * K3_RS + 64 * n) = oc[n][m];
    }
    BAR();
    if (tid < 448) {
      const int et = tid / 14, ec = tid % 14;
      const char* p = sm + et * K3_RS + ec * 32;
      f32x4 v0 = {0.f, 0.f, 0.f, 0.f}, v1 = {0.f, 0.f, 0.f, 0.f};
#pragma unroll
      for (int w = 0; w < 7; ++w) {
        v0 += *(const f32x4*)(p + w * 32 * K3_RS);
        v1 += *(const f32x4*)(p + w * 32 * K3_RS + 16);
      }
      u32x4 o;
      o[0] = pk2(lo_f(xv[0]) + v0[0], hi_f(xv[0]) + v0[1]);
      o[1] = pk2(lo_f(xv[1]) + v0[2], hi_f(xv[1]) + v0[3]);
      o[2] = pk2(lo_f(xv[2]) + v1[0], hi_f(xv[2]) + v1[1]);
      o[3] = pk2(lo_f(xv[3]) + v1[2], hi_f(xv[3]) + v1[3]);
      *(u32x4*)(out + (size_t)(r0 + et) * D_ + n0 + ec * 8) = o;
    }
  }
  STAMP(2, 31);
}

extern "C" void kernel_launch(void* const* d_in, const int* in_sizes, int n_in, void* d_out, int out_size,
                              void* d_ws, size_t ws_size, hipStream_t stream) {
  const u16* x = (const u16*)d_in[0];
  const u16* wqkv = (const u16*)d_in[1];
  const u16* wphi = (const u16*)d_in[2];
  const u16* wo = (const u16*)d_in[3];
  const u16* win = (const u16*)d_in[5];
  const u16* wsp = (const u16*)d_in[6];
  const u16* bsp = (const u16*)d_in[7];
  const u16* wout = (const u16*)d_in[8];
  char* ws = (char*)d_ws;
  k1_kernel<<<256, 768, 0, stream>>>(x, wqkv, wo, win, wout, wsp, ws);
  k2_kernel<<<384, 512, 0, stream>>>(wphi, ws);
  k3_kernel<<<256, 512, 0, stream>>>(wsp, bsp, x, ws, (u16*)d_out);
}
```

```cpp
#include <hip/hip_runtime.h>
#include <hip/hip_bf16.h>
#include <stdint.h>

typedef __attribute__((ext_vector_type(8))) short bf16x8;
typedef __attribute__((ext_vector_type(4))) short s16x4;
typedef __attribute__((ext_vector_type(4))) float f32x4;
typedef __attribute__((ext_vector_type(2))) float f32x2;
typedef __attribute__((ext_vector_type(4))) unsigned u32x4;
typedef __attribute__((ext_vector_type(2))) unsigned u32x2;
typedef __attribute__((ext_vector_type(2))) __bf16 bf2_t;
typedef unsigned short u16;
#define LDS_AS __attribute__((address_space(3)))

constexpr int S_ = 2048, D_ = 448, NQKV = 1344, NIN = 1792, FH = 896, KO = 1344;

__device__ __forceinline__ unsigned pk2(float a, float b) {
  f32x2 v = {a, b};
  return __builtin_bit_cast(unsigned, __builtin_convertvector(v, bf2_t));
}
__device__ __forceinline__ float lo_f(unsigned u) { return __uint_as_float(u << 16); }
__device__ __forceinline__ float hi_f(unsigned u) { return __uint_as_float(u & 0xffff0000u); }
__device__ __forceinline__ float rbf(float a) { return lo_f(pk2(a, 0.f)); }
__device__ __forceinline__ float bf2f(u16 v) { return __uint_as_float(((unsigned)v) << 16); }
__device__ __forceinline__ float dot2(unsigned a, unsigned b, float c) {
  return __builtin_amdgcn_fdot2_f32_bf16(__builtin_bit_cast(bf2_t, a), __builtin_bit_cast(bf2_t, b), c, false);
}

__device__ __forceinline__ float x16_max(float v) {
  auto r = __builtin_amdgcn_permlane16_swap(__float_as_uint(v), __float_as_uint(v), false, false);
  return fmaxf(__uint_as_float(r[0]), __uint_as_float(r[1]));
}
__device__ __forceinline__ float x32_max(float v) {
  auto r = __builtin_amdgcn_permlane32_swap(__float_as_uint(v), __float_as_uint(v), false, false);
  return fmaxf(__uint_as_float(r[0]), __uint_as_float(r[1]));
}
__device__ __forceinline__ float x16_min(float v) {
  auto r = __builtin_amdgcn_permlane16_swap(__float_as_uint(v), __float_as_uint(v), false, false);
  return fminf(__uint_as_float(r[0]), __uint_as_float(r[1]));
}
__device__ __forceinline__ float x32_min(float v) {
  auto r = __builtin_amdgcn_permlane32_swap(__float_as_uint(v), __float_as_uint(v), false, false);
  return fminf(__uint_as_float(r[0]), __uint_as_float(r[1]));
}
__device__ __forceinline__ float x16_add(float v) {
  auto r = __builtin_amdgcn_permlane16_swap(__float_as_uint(v), __float_as_uint(v), false, false);
  return __uint_as_float(r[0]) + __uint_as_float(r[1]);
}
__device__ __forceinline__ float x32_add(float v) {
  auto r = __builtin_amdgcn_permlane32_swap(__float_as_uint(v), __float_as_uint(v), false, false);
  return __uint_as_float(r[0]) + __uint_as_float(r[1]);
}
__device__ __forceinline__ float red8_add(float v) {
  v += __uint_as_float(__builtin_amdgcn_mov_dpp(__float_as_uint(v), 0xB1, 0xf, 0xf, true));
  v += __uint_as_float(__builtin_amdgcn_mov_dpp(__float_as_uint(v), 0x4E, 0xf, 0xf, true));
  v += __uint_as_float(__builtin_amdgcn_mov_dpp(__float_as_uint(v), 0x141, 0xf, 0xf, true));
  return v;
}
__device__ __forceinline__ f32x4 mfma16(bf16x8 a, bf16x8 b, f32x4 c) {
  return __builtin_amdgcn_mfma_f32_16x16x32_bf16(a, b, c, 0, 0, 0);
}
__device__ __forceinline__ unsigned gelu_pk(unsigned p) {
  const f32x2 u = {lo_f(p), hi_f(p)};
  const f32x2 c1 = {-2.3022081986f, -2.3022081986f};
  const f32x2 c2 = {-0.1029432397f, -0.1029432397f};
  f32x2 t = u * u;
  f32x2 a = u * (t * c2 + c1);
  f32x2 d = {1.f + __builtin_amdgcn_exp2f(a[0]), 1.f + __builtin_amdgcn_exp2f(a[1])};
  f32x2 r = {__builtin_amdgcn_rcpf(d[0]), __builtin_amdgcn_rcpf(d[1])};
  f32x2 o = u * r;
  return pk2(o[0], o[1]);
}
#define SCHED() __builtin_amdgcn_sched_barrier(0)
#define BAR() do { SCHED(); asm volatile("" ::: "memory"); __builtin_amdgcn_s_waitcnt(0xC07F); __builtin_amdgcn_s_barrier(); asm volatile("" ::: "memory"); SCHED(); } while (0)
__device__ __forceinline__ void glds16(const void* g, char* l) {
  __builtin_amdgcn_global_load_lds((const unsigned*)g, (LDS_AS unsigned*)l, 16, 0, 0);
}
__device__ __forceinline__ bf16x8 tr_frag(const char* p0, const char* p1) {
  s16x4 a = __builtin_amdgcn_ds_read_tr16_b64_v4i16((LDS_AS s16x4*)p0);
  s16x4 c = __builtin_amdgcn_ds_read_tr16_b64_v4i16((LDS_AS s16x4*)p1);
  return __builtin_shufflevector(a, c, 0, 1, 2, 3, 4, 5, 6, 7);
}

constexpr int YLD = 3136;
constexpr size_t WS_Y = 0;
constexpr size_t WS_Z2N = WS_Y + (size_t)S_ * YLD * 2 + 256;
constexpr size_t WS_OH = WS_Z2N + (size_t)S_ * 896 * 2;
constexpr size_t WS_WT = WS_OH + (size_t)S_ * KO * 2;
constexpr size_t WS_KVP = WS_WT + (size_t)D_ * KO * 2;
constexpr size_t WS_PHQ = WS_KVP + (size_t)8 * 16 * 56 * 32 * 4;
constexpr size_t WS_END = WS_PHQ + (size_t)16 * 128 * 2048;

#define STAMP(K, I) do { } while (0)
__device__ __forceinline__ void st16_wt(void* p, u32x4 v) {
  asm volatile("global_store_dwordx4 %0, %1, off sc0 sc1\n\ts_nop 1" ::"v"(p), "v"(v) : "memory");
}
__device__ __forceinline__ void st8_wt(void* p, u32x2 v) {
  asm volatile("global_store_dwordx2 %0, %1, off sc0 sc1\n\ts_nop 1" ::"v"(p), "v"(v) : "memory");
}
#define SGB(mask, n) __builtin_amdgcn_sched_group_barrier(mask, n, 0)

__device__ __forceinline__ void phi_map(bf16x8 qf, bf16x8 wp0, bf16x8 wp1, int g, unsigned (&pp)[2][2],
                                        unsigned (&pm)[2][2]) {
  f32x4 z = {0.f, 0.f, 0.f, 0.f};
  f32x4 p0 = mfma16(wp0, qf, z);
  f32x4 p1 = mfma16(wp1, qf, z);
  float v[8];
  {
    unsigned a = pk2(p0[0], p0[1]), b = pk2(p0[2], p0[3]), c = pk2(p1[0], p1[1]), d = pk2(p1[2], p1[3]);
    v[0] = lo_f(a); v[1] = hi_f(a); v[2] = lo_f(b); v[3] = hi_f(b);
    v[4] = lo_f(c); v[5] = hi_f(c); v[6] = lo_f(d); v[7] = hi_f(d);
  }
  const bool valid1 = (g != 3);
  if (!valid1) { v[4] = v[0]; v[5] = v[0]; v[6] = v[0]; v[7] = v[0]; }
  float mx = fmaxf(fmaxf(fmaxf(v[0], v[1]), fmaxf(v[2], v[3])), fmaxf(fmaxf(v[4], v[5]), fmaxf(v[6], v[7])));
  float mn = fminf(fminf(fminf(v[0], v[1]), fminf(v[2], v[3])), fminf(fminf(v[4], v[5]), fminf(v[6], v[7])));
  mx = x32_max(x16_max(mx));
  mn = x32_min(x16_min(mn));
  const float L2E = 1.4426950408889634f;
  const float mxl = mx * L2E, mnl = mn * L2E;
  float ep[8], em[8];
  float sp = 0.f, sm = 0.f;
#pragma unroll
  for (int j = 0; j < 8; ++j) {
    ep[j] = __builtin_amdgcn_exp2f(__builtin_fmaf(v[j], L2E, -mxl));
    em[j] = __builtin_amdgcn_exp2f(__builtin_fmaf(v[j], -L2E, mnl));
    if (j >= 4) { ep[j] = valid1 ? ep[j] : 0.f; em[j] = valid1 ? em[j] : 0.f; }
    sp += ep[j];
    sm += em[j];
  }
  sp = x32_add(x16_add(sp));
  sm = x32_add(x16_add(sm));
  const float rp = __builtin_amdgcn_rcpf(sp), rm = __builtin_amdgcn_rcpf(sm);
  pp[0][0] = pk2(ep[0] * rp, ep[1] * rp); pp[0][1] = pk2(ep[2] * rp, ep[3] * rp);
  pp[1][0] = pk2(ep[4] * rp, ep[5] * rp); pp[1][1] = pk2(ep[6] * rp, ep[7] * rp);
  pm[0][0] = pk2(em[0] * rm, em[1] * rm); pm[0][1] = pk2(em[2] * rm, em[3] * rm);
  pm[1][0] = pk2(em[4] * rm, em[5] * rm); pm[1][1] = pk2(em[6] * rm, em[7] * rm);
}

constexpr int K1_XST = 128 * 128;
constexpr int K1_STAGE = K1_XST + 64 * 448;
constexpr int K1_LDS = 3 * K1_STAGE;

__global__ __launch_bounds__(768) void k1_kernel(const u16* __restrict__ x, const u16* __restrict__ wqkv,
                                                  const u16* __restrict__ wo, const u16* __restrict__ win,
                                                  const u16* __restrict__ wout, const u16* __restrict__ wsp,
                                                  char* __restrict__ ws) {
  __shared__ __attribute__((aligned(16))) char sm[K1_LDS];
  const int tid = threadIdx.x, lane = tid & 63, wave = __builtin_amdgcn_readfirstlane(tid >> 6);
  const int b = blockIdx.x;
  STAMP(0, 0);
  if (b >= 224) {
    const int kb = b - 224;
    const int k0 = kb * 64;
    if (kb < 21) {
    u16* T = (u16*)sm;
    constexpr int TS = 456;
    u32x4 v[5];
#pragma unroll
    for (int j = 0; j < 5; ++j) {
      int i = tid + 768 * j;
      if (i >= 64 * 56) i = tid;
      const int r = i / 56, c = i % 56;
      const int k = k0 + r;
      const u16* src = (k < 448) ? (wo + (size_t)k * 448) : (wout + (size_t)(k - 448) * 448);
      v[j] = *(const u32x4*)(src + c * 8);
    }
#pragma unroll
    for (int j = 0; j < 5; ++j) {
      const int i = tid + 768 * j;
      if (i < 64 * 56) *(u32x4*)(T + (i / 56) * TS + (i % 56) * 8) = v[j];
    }
    __syncthreads();
    u16* wT = (u16*)(ws + WS_WT);
    for (int i = tid; i < 448 * 8; i += 768) {
      const int n = i >> 3, c = i & 7;
      u16 t[8];
#pragma unroll
      for (int j = 0; j < 8; ++j) t[j] = T[(c * 8 + j) * TS + n];
      u32x4 o;
#pragma unroll
      for (int j = 0; j < 4; ++j) o[j] = (unsigned)t[2 * j] | ((unsigned)t[2 * j + 1] << 16);
      st16_wt(wT + ((size_t)((n >> 4) * 42 + (k0 >> 5) + (c >> 2)) * 64 + (c & 3) * 16 + (n & 15)) * 8, o);
    }
    }
    STAMP(0, 1);
    STAMP(0, 31);
    return;
  }

  const int xcd = b & 7, idx = b >> 3;
  const int cb = (xcd & 1) * 7 + idx % 7;
  const int rt = (xcd >> 1) * 4 + idx / 7;
  const int row0 = rt * 128;
  const int g = lane >> 4, r16 = lane & 15;
  const int wm = (wave >> 1) & 3, wn = wave & 1;
  constexpr int RST = 464;

  if (wave >= 8) {
    const int lw = wave - 8;
    const u16* W = (cb < 6) ? (wqkv + 224 * cb) : (win + 224 * (cb - 6));
    const int ldw = (cb < 6) ? NQKV : NIN;
    const u16* src[11];
#pragma unroll
    for (int j = 0; j < 11; ++j) {
      const int i = lw + 4 * j;
      if (j < 4) {
        const int row = 8 * i + (lane >> 3);
        src[j] = x + (size_t)(row0 + row) * D_ + (((lane & 7) ^ ((row >> 1) & 7)) << 3);
      } else {
        const int o = (i - 16) * 1024 + lane * 16;
        const int k = o / 448, cp = (o % 448) >> 4;
        const int G = (cp >> 1) ^ ((k >> 3) & 1);
        src[j] = W + (size_t)k * ldw + 8 * (2 * G + (cp & 1));
      }
    }
    const int wstep = 64 * ldw;
    char* dst0 = sm + lw * 1024;
#pragma unroll
    for (int s = 0; s < 2; ++s)
#pragma unroll
      for (int j = 0; j < 11; ++j) glds16(src[j] + s * (j < 4 ? 64 : wstep), dst0 + s * K1_STAGE + j * 4096);
    int nb = 2;
#pragma unroll 1
    for (int t = 0; t < 7; ++t) {
      if (t < 6) asm volatile("s_waitcnt vmcnt(11)" ::: "memory");
      else asm volatile("s_waitcnt vmcnt(0)" ::: "memory");
      __builtin_amdgcn_s_barrier();
      if (t + 2 < 7) {
        char* dst = dst0 + nb * K1_STAGE;
#pragma unroll
        for (int j = 0; j < 11; ++j) glds16(src[j] + (t + 2) * (j < 4 ? 64 : wstep), dst + j * 4096);
        nb = (nb == 2) ? 0 : nb + 1;
      }
    }
    u32x4 d0, d1, d2;
    {
      const char* p = (const char*)wsp + (size_t)(b * 4 + lw) * 9216 + lane * 16;
#pragma unroll
      for (int i = 0; i < 3; ++i)
        asm volatile("global_load_dwordx4 %0, %3, off offset:0\n\tglobal_load_dwordx4 %1, %3, off offset:1024\n\tglobal_load_dwordx4 %2, %3, off offset:2048"
                     : "=&v"(d0), "=&v"(d1), "=&v"(d2) : "v"(p + i * 3072) : "memory");
    }
    __builtin_amdgcn_s_barrier();
    __builtin_amdgcn_s_barrier();
    asm volatile("s_waitcnt vmcnt(0)" : "+v"(d0), "+v"(d1), "+v"(d2)::"memory");
  } else {
    f32x4 acc[7][2], sacc = {0.f, 0.f, 0.f, 0.f}, qacc = {0.f, 0.f, 0.f, 0.f}, cacc[2];
#pragma unroll
    for (int nt = 0; nt < 7; ++nt) { acc[nt][0] = f32x4{0.f, 0.f, 0.f, 0.f}; acc[nt][1] = f32x4{0.f, 0.f, 0.f, 0.f}; }
    cacc[0] = f32x4{0.f, 0.f, 0.f, 0.f}; cacc[1] = f32x4{0.f, 0.f, 0.f, 0.f};
    const bf16x8 ones = {0x3F80, 0x3F80, 0x3F80, 0x3F80, 0x3F80, 0x3F80, 0x3F80, 0x3F80};
    const int sw = (r16 >> 1) & 7;
    const int xrow = (32 * wm + 16 * wn + r16) * 128, xd = wn ? -2048 : 2048;
    const int xo0 = xrow + ((g ^ sw) << 4), xo1 = xrow + (((4 | g) ^ sw) << 4);
    int woff[7];
#pragma unroll
    for (int i = 0; i < 7; ++i) {
      int nt = i + 2 * wm;
      nt = (nt >= 7) ? nt - 7 : nt;
      const int G = 7 * wn + nt;
      woff[i] = K1_XST + (8 * g + ((lane >> 2) & 3)) * 448 + (lane & 3) * 8 + ((G ^ (g & 1)) << 5);
    }
    bf16x8 xa[2], wa[7], xb[2], wb[7];
#define K1_READ(XF, WF, SB, KS)                                                              \
  do {                                                                                       \
    XF[0] = *(const bf16x8*)((SB) + ((KS) ? xo1 : xo0));                                     \
    XF[1] = *(const bf16x8*)((SB) + ((KS) ? xo1 : xo0) + xd);                                \
    _Pragma("unroll") for (int nt = 0; nt < 7; ++nt) {                                       \
      const char* p_ = (SB) + woff[nt] + (KS)*32 * 448;                                      \
      WF[nt] = tr_frag(p_, p_ + 4 * 448);                                                    \
    }                                                                                        \
  } while (0)
#define K1_MMA(XF, WF)                                                                       \
  do {                                                                                       \
    _Pragma("unroll") for (int nt = 0; nt < 7; ++nt) acc[nt][0] = mfma16(WF[nt], XF[0], acc[nt][0]); \
    sacc = mfma16(ones, XF[0], sacc);                                                        \
    qacc = mfma16(XF[0], XF[0], qacc);                                                       \
    _Pragma("unroll") for (int nt = 0; nt < 7; ++nt) acc[nt][1] = mfma16(WF[nt], XF[1], acc[nt][1]); \
    cacc[0] = mfma16(WF[0], ones, cacc[0]);                                                  \
    cacc[1] = mfma16(WF[1], ones, cacc[1]);                                                  \
  } while (0)
    int rb = 0;
#pragma unroll 1
    for (int t = 0; t < 7; ++t) {
      BAR();
      STAMP(0, 3 + t);
      const char* sb = sm + rb * K1_STAGE;
      rb = (rb == 2) ? 0 : rb + 1;
      K1_READ(xa, wa, sb, 0);
      SCHED();
      K1_READ(xb, wb, sb, 1);
      K1_MMA(xa, wa);
      _Pragma("unroll") for (int i_ = 0; i_ < 8; ++i_) { SGB(0x008, 1); SGB(0x100, 2); }
      SCHED();
      K1_MMA(xb, wb);
    }
    BAR();
    STAMP(0, 17);
    float* CS = (float*)(sm + 128 * RST);
    f32x2* RS_ = (f32x2*)(CS + 224);
    if (r16 == 0) {
#pragma unroll
      for (int i = 0; i < 2; ++i) {
        int nt = i + 2 * wm;
        nt = (nt >= 7) ? nt - 7 : nt;
        *(f32x4*)(CS + 112 * wn + 16 * nt + 4 * g) = cacc[i];
      }
    }
    {
      const float qd = (r16 & 3) == 0 ? qacc[0] : (r16 & 3) == 1 ? qacc[1] : (r16 & 3) == 2 ? qacc[2] : qacc[3];
      if (g == (r16 >> 2)) {
        const float mean = sacc[0] * (1.f / 448.f);
        const float var = fmaxf(qd * (1.f / 448.f) - mean * mean, 0.f);
        const float sd = rbf(sqrtf(rbf(rbf(var) + 1e-5f)));
        const float r = 1.f / sd;
        RS_[32 * wm + 16 * wn + r16] = f32x2{r, r * rbf(mean)};
      }
    }
    BAR();
    f32x2 st[2];
    st[0] = RS_[32 * wm + 16 * wn + r16];
    st[1] = RS_[32 * wm + 16 * (wn ^ 1) + r16];
#pragma unroll
    for (int i = 0; i < 7; ++i) {
      int nt = i + 2 * wm;
      nt = (nt >= 7) ? nt - 7 : nt;
      const f32x4 c4 = *(const f32x4*)(CS + 112 * wn + 16 * nt + 4 * g);
#pragma unroll
      for (int j = 0; j < 2; ++j) {
        char* dst = sm + (32 * wm + 16 * (j ^ wn) + r16) * RST + (112 * wn + 4 * g) * 2 + 32 * nt;
        const f32x4 v = acc[i][j] * st[j][0] - c4 * st[j][1];
        *(u32x2*)(dst) = u32x2{pk2(v[0], v[1]), pk2(v[2], v[3])};
      }
    }
  }
  __syncthreads();
  u16* dstb = (u16*)(ws + WS_Y) + (size_t)row0 * YLD + 224 * cb;
#pragma unroll
  for (int i = 0; i < 5; ++i) {
    const int id = tid + 768 * i;
    if (id < 128 * 28) {
      const int s = id / 28, c = id % 28;
      st16_wt(dstb + (size_t)s * YLD + c * 8, *(const u32x4*)(sm + s * RST + c * 16));
    }
  }
  STAMP(0, 31);
}

__device__ __forceinline__ float red64_add(float v) {
  v = red8_add(v);
  v += __uint_as_float(__builtin_amdgcn_mov_dpp(__float_as_uint(v), 0x140, 0xf, 0xf, true));
  return x32_add(x16_add(v));
}

constexpr int K2_PW = 64 * 80 + 32 * 64;
__global__ __launch_bounds__(512) void k2_kernel(const u16* __restrict__ wphi, char* __restrict__ ws) {
  __shared__ __attribute__((aligned(16))) char sm[8 * 56 * 32 * 4 + 2048];
  const int tid = threadIdx.x, lane = tid & 63, wave = __builtin_amdgcn_readfirstlane(tid >> 6);
  const int b = blockIdx.x;
  const u16* Y = (const u16*)(ws + WS_Y);
  STAMP(1, 0);
  if (b < 128) {
    const int h = b & 15, rg = b >> 4;
    const int g = lane >> 4, r16 = lane & 15;
    const int rowb = rg * 256 + wave * 32;
    u32x2 kv_[2][2];
#pragma unroll
    for (int st = 0; st < 2; ++st) {
      const u16* kp = Y + (size_t)(rowb + st * 16 + r16) * YLD + 448 + h * 28 + 8 * g;
      kv_[st][0] = *(const u32x2*)(kp);
      kv_[st][1] = *(const u32x2*)(kp + 4);
    }
    u32x2 vv[4];
#pragma unroll
    for (int i = 0; i < 4; ++i) {
      const int row = (lane >> 3) + 8 * i, pc = lane & 7;
      vv[i] = *(const u32x2*)(Y + (size_t)(rowb + row) * YLD + 896 + h * 28 + 4 * (pc < 7 ? pc : 0));
      if (pc == 7) vv[i] = u32x2{0x00003F80u, 0u};
    }
    u32x4 wraw = {0u, 0u, 0u, 0u};
    if (tid < 98) wraw = *(const u32x4*)(wphi + (size_t)h * 784 + tid * 8);
    char* WP = sm + 8 * 56 * 32 * 4;
    if (tid < 98) *(u32x4*)(WP + tid * 16) = wraw;
    char* PK = sm + wave * K2_PW;
    char* VS = PK + 64 * 80;
    __syncthreads();
    STAMP(1, 1);
    const char* wp = WP + (8 * g + ((lane >> 2) & 3)) * 56 + (lane & 3) * 8;
    bf16x8 wp0 = tr_frag(wp, wp + 4 * 56);
    bf16x8 wp1 = tr_frag(wp + 32, wp + 4 * 56 + 32);
    if (g == 3) {
#pragma unroll
      for (int j = 4; j < 8; ++j) { wp0[j] = 0; wp1[j] = 0; }
    }
    f32x4 kacc[4][2];
#pragma unroll
    for (int a = 0; a < 4; ++a) { kacc[a][0] = f32x4{0.f, 0.f, 0.f, 0.f}; kacc[a][1] = f32x4{0.f, 0.f, 0.f, 0.f}; }
#pragma unroll
    for (int pr = 0; pr < 1; ++pr) {
#pragma unroll
      for (int i = 0; i < 4; ++i) {
        const int row = (lane >> 3) + 8 * i;
        const int slot = 2 * (row & 15) + (row >> 4);
        *(u32x2*)(VS + slot * 64 + (lane & 7) * 8) = vv[4 * pr + i];
      }
      unsigned pp[2][2][2], pm[2][2][2];
#pragma unroll
      for (int mt = 0; mt < 2; ++mt) {
        u32x4 q4 = {kv_[2 * pr + mt][0][0], kv_[2 * pr + mt][0][1], kv_[2 * pr + mt][1][0], kv_[2 * pr + mt][1][1]};
        if (g == 3) { q4[2] = 0u; q4[3] = 0u; }
        phi_map(__builtin_bit_cast(bf16x8, q4), wp0, wp1, g, pp[mt], pm[mt]);
      }
#pragma unroll
      for (int ft = 0; ft < 2; ++ft) {
        if (ft == 0 || g != 3) {
          char* d0 = PK + (16 * ft + 4 * g) * 80 + r16 * 4;
#pragma unroll
          for (int j = 0; j < 2; ++j) {
            *(unsigned*)(d0 + (2 * j) * 80) = (pp[0][ft][j] & 0xffffu) | (pp[1][ft][j] << 16);
            *(unsigned*)(d0 + (2 * j + 1) * 80) = (pp[0][ft][j] >> 16) | (pp[1][ft][j] & 0xffff0000u);
            *(unsigned*)(d0 + (28 + 2 * j) * 80) = (pm[0][ft][j] & 0xffffu) | (pm[1][ft][j] << 16);
            *(unsigned*)(d0 + (28 + 2 * j + 1) * 80) = (pm[0][ft][j] >> 16) | (pm[1][ft][j] & 0xffff0000u);
          }
        }
      }
      __builtin_amdgcn_s_waitcnt(0xC07F);
      __builtin_amdgcn_wave_barrier();
      bf16x8 bfr[2];
      {
        const char* vp = VS + (8 * g + ((lane >> 2) & 3)) * 64 + (lane & 3) * 8;
        bfr[0] = tr_frag(vp, vp + 4 * 64);
        bfr[1] = tr_frag(vp + 32, vp + 4 * 64 + 32);
      }
#pragma unroll
      for (int a = 0; a < 4; ++a) {
        bf16x8 af = *(const bf16x8*)(PK + (16 * a + r16) * 80 + g * 16);
        kacc[a][0] = mfma16(af, bfr[0], kacc[a][0]);
        kacc[a][1] = mfma16(af, bfr[1], kacc[a][1]);
      }
      __builtin_amdgcn_s_waitcnt(0xC07F);
      __builtin_amdgcn_wave_barrier();
    }
    STAMP(1, 2);
    __syncthreads();
    float* red = (float*)sm + wave * (56 * 32);
#pragma unroll
    for (int a = 0; a < 4; ++a)
#pragma unroll
      for (int c = 0; c < 2; ++c)
#pragma unroll
        for (int j = 0; j < 4; ++j) {
          const int f = 16 * a + 4 * g + j;
          if (f < 56) red[f * 32 + 16 * c + r16] = kacc[a][c][j];
        }
    __syncthreads();
    if (tid < 448) {
      const f32x4* rp = (const f32x4*)sm + tid;
      f32x4 s = (rp[0] + rp[448]) + (rp[2 * 448] + rp[3 * 448]);
      s += (rp[4 * 448] + rp[5 * 448]) + (rp[6 * 448] + rp[7 * 448]);
      st16_wt((f32x4*)(ws + WS_KVP) + (size_t)(rg * 16 + h) * 448 + tid, __builtin_bit_cast(u32x4, s));
    }
  } else if (b >= 256) {
    const int h = b & 15, rg = (b - 256) >> 4;
    const int g = lane >> 4, r16 = lane & 15;
    u32x2 qv[2][2];
#pragma unroll
    for (int st = 0; st < 2; ++st) {
      const u16* qp = Y + (size_t)(rg * 256 + wave * 32 + st * 16 + r16) * YLD + h * 28 + 8 * g;
      qv[st][0] = *(const u32x2*)(qp);
      qv[st][1] = *(const u32x2*)(qp + 4);
    }
    u32x4 wraw = {0u, 0u, 0u, 0u};
    if (tid < 98) wraw = *(const u32x4*)(wphi + (size_t)h * 784 + tid * 8);
    if (tid < 98) *(u32x4*)(sm + tid * 16) = wraw;
    __syncthreads();
    const char* wp = sm + (8 * g + ((lane >> 2) & 3)) * 56 + (lane & 3) * 8;
    bf16x8 wp0 = tr_frag(wp, wp + 4 * 56);
    bf16x8 wp1 = tr_frag(wp + 32, wp + 4 * 56 + 32);
    if (g == 3) {
#pragma unroll
      for (int j = 4; j < 8; ++j) { wp0[j] = 0; wp1[j] = 0; }
    }
#pragma unroll
    for (int st = 0; st < 2; ++st) {
      u32x4 q4 = {qv[st][0][0], qv[st][0][1], qv[st][1][0], qv[st][1][1]};
      if (g == 3) { q4[2] = 0u; q4[3] = 0u; }
      unsigned pp[2][2], pm[2][2];
      phi_map(__builtin_bit_cast(bf16x8, q4), wp0, wp1, g, pp, pm);
      u32x4* dst = (u32x4*)(ws + WS_PHQ) + ((size_t)(h * 128 + rg * 16 + wave * 2 + st) * 2) * 64 + lane;
      st16_wt(dst, u32x4{pp[0][0], pp[0][1], pp[1][0], pp[1][1]});
      st16_wt(dst + 64, u32x4{pm[0][0], pm[0][1], pm[1][0], pm[1][1]});
    }
  } else {
    const int row0 = (b - 128) * 16 + wave * 2;
    u16* z2n = (u16*)(ws + WS_Z2N);
    const int le = (lane < 56) ? lane : 0;
    u32x4 v[2][2];
#pragma unroll
    for (int r = 0; r < 2; ++r)
#pragma unroll
      for (int c = 0; c < 2; ++c) v[r][c] = *(const u32x4*)(Y + (size_t)(row0 + r) * YLD + 2240 + (le + 56 * c) * 8);
#pragma unroll
    for (int r = 0; r < 2; ++r) {
      float s = 0.f;
#pragma unroll
      for (int c = 0; c < 2; ++c)
#pragma unroll
        for (int j = 0; j < 4; ++j) {
          v[r][c][j] = gelu_pk(v[r][c][j]);
          s = dot2(v[r][c][j], 0x3F803F80u, s);
        }
      if (lane >= 56) s = 0.f;
      const float mean = red64_add(s) * (1.f / 896.f);
      const float mb = rbf(mean);
      float q = 0.f;
#pragma unroll
      for (int c = 0; c < 2; ++c)
#pragma unroll
        for (int j = 0; j < 4; ++j) {
          const float a = lo_f(v[r][c][j]) - mean, e = hi_f(v[r][c][j]) - mean;
          q += a * a + e * e;
          v[r][c][j] = pk2(lo_f(v[r][c][j]) - mb, hi_f(v[r][c][j]) - mb);
        }
      if (lane >= 56) q = 0.f;
      const float var = red64_add(q) * (1.f / 896.f);
      const float sd = rbf(sqrtf(rbf(rbf(var) + 1e-5f)));
      const float inv = 1.f / sd;
      if (lane < 56) {
#pragma unroll
        for (int c = 0; c < 2; ++c) {
          u32x4 o;
#pragma unroll
          for (int j = 0; j < 4; ++j) o[j] = pk2(lo_f(v[r][c][j]) * inv, hi_f(v[r][c][j]) * inv);
          *(u32x4*)(sm + (wave * 2 + r) * 1808 + (lane + 56 * c) * 16) = o;
        }
      }
    }
    __syncthreads();
    {
      const int rb0 = (b - 128) * 16;
      const int kb = rb0 >> 5, half = (rb0 >> 4) & 1;
      const int g = lane >> 4;
#pragma unroll
      for (int i = 0; i < 4; ++i) {
        const int u = wave + 8 * i;
        if (u < 28) {
          const int T = 2 * u + (g >> 1), gp = g & 1;
          const char* p = sm + (8 * gp + ((lane >> 2) & 3)) * 1808 + T * 32 + (lane & 3) * 8;
          bf16x8 fr = tr_frag(p, p + 4 * 1808);
          const int pn = T / 7, n = T % 7;
          st16_wt(z2n + ((size_t)((pn * 64 + kb) * 7 + n) * 64 + (2 * half + gp) * 16 + (lane & 15)) * 8, __builtin_bit_cast(u32x4, fr));
        }
      }
    }
  }
  STAMP(1, 31);
}

constexpr int K3_RS = 464;
constexpr int K3_STAGE = 64 * 256;
constexpr int K3_NB = 8;
constexpr int K3_AUX = K3_NB * K3_STAGE;
__device__ __forceinline__ void glds16_asm(const void* gsrc, unsigned lds_dst) {
  unsigned keep;
  asm volatile("s_mov_b32 %0, m0\n\ts_mov_b32 m0, %2\n\ts_nop 0\n\tglobal_load_lds_dwordx4 %1, off\n\ts_mov_b32 m0, %0"
               : "=&s"(keep) : "v"(gsrc), "s"(lds_dst) : "memory");
}
__global__ __launch_bounds__(512) void k3_kernel(const u16* __restrict__ wsp, const u16* __restrict__ bsp,
                                                  const u16* __restrict__ wphi, char* __restrict__ ws) {
  __shared__ __attribute__((aligned(16))) char sm[K3_NB * K3_STAGE + 32 * 144];
  const int tid = threadIdx.x, lane = tid & 63, wave = __builtin_amdgcn_readfirstlane(tid >> 6);
  const int b = blockIdx.x;
  const int xcd = b & 7, idx = b >> 3;
  const int mt_ = (xcd >> 1) * 8 + (idx & 7);
  const int nt_ = (xcd & 1) * 4 + (idx >> 3);
  const int t0 = mt_ * 64, f0 = nt_ * 112;
  const u16* z2n = (const u16*)(ws + WS_Z2N);
  const u16* Y = (const u16*)(ws + WS_Y);
  u16* oh = (u16*)(ws + WS_OH);
  const int g = lane >> 4, r16 = lane & 15;
  const int h = b & 15;
  u32x4 zv[2];
  float bb[2];
  STAMP(2, 0);
  const int kq = wave >> 1, wn = (wave & 1) ^ (wave >> 2);
  const u16* bp = z2n + ((size_t)(nt_ * 64 + kq) * 7 + 4 * wn) * 512 + lane * 8;
  const int arow = r16 * 256 + (((4 * kq + g) ^ r16) << 4);
  const u16* src0;
  const u16* src1;
  {
    const int row = 4 * wave + (lane >> 4);
    src0 = wsp + (size_t)(t0 + row) * S_ + (((lane & 15) ^ (row & 15)) << 3);
    src1 = src0 + (size_t)32 * S_;
  }
  const unsigned ldsb = __builtin_amdgcn_readfirstlane((unsigned)(size_t)(LDS_AS char*)sm) + wave * 1024;
#define K3_AISSUE(T)                                                                            \
  do {                                                                                          \
    glds16_asm(src0 + (T)*128, ldsb + ((T)&7) * K3_STAGE);                                      \
    glds16_asm(src1 + (T)*128, ldsb + ((T)&7) * K3_STAGE + 8192);                               \
  } while (0)
  f32x4 acc[4][4];
  bf16x8 aA[4], aB[4], b0[4], b1[4], b2[4], b3[4];
#define K3_BLOAD(BF, T, NT)                                                                     \
  do { _Pragma("unroll") for (int n = 0; n < NT; ++n) BF[n] = *(const bf16x8*)(bp + ((T)*28 + n) * 512); } while (0)
#define K3_AREAD(AF, T)                                                                         \
  do { _Pragma("unroll") for (int m = 0; m < 4; ++m) AF[m] = *(const bf16x8*)(sm + ((T)&7) * K3_STAGE + arow + m * 4096); } while (0)
#define K3_MMA(AF, BF, NT)                                                                      \
  do {                                                                                          \
    _Pragma("unroll") for (int n = 0; n < NT; ++n)                                              \
      _Pragma("unroll") for (int m = 0; m < 4; ++m) acc[n][m] = mfma16(BF[n], AF[m], acc[n][m]); \
  } while (0)
#define K3_STEP(T, AC, AN, BC, BL, NT)                                                          \
  do {                                                                                          \
    asm volatile("s_waitcnt vmcnt(12)" ::: "memory");                                           \
    BAR();                                                                                      \
    STAMP(2, 5 + (T));                                                                          \
    if ((T) + 8 < 16) K3_AISSUE((T) + 8);                                                       \
    if ((T) + 1 < 16) K3_AREAD(AN, (T) + 1);                                                    \
    if ((T) + 3 < 16) K3_BLOAD(BL, (T) + 3, NT);                                                \
    K3_MMA(AC, BC, NT);                                                                         \
    SGB(0x100, 4);                                                                              \
    _Pragma("unroll") for (int i_ = 0; i_ < NT; ++i_) { SGB(0x008, 2); SGB(0x020, 1); }         \
  } while (0)
#define K3_LOOP(NT) \
  do { \
    STAMP(2, 4); \
    K3_AREAD(aA, 0); \
    K3_STEP(0, aA, aB, b0, b3, NT); \
    K3_STEP(1, aB, aA, b1, b0, NT); \
    K3_STEP(2, aA, aB, b2, b1, NT); \
    K3_STEP(3, aB, aA, b3, b2, NT); \
    K3_STEP(4, aA, aB, b0, b3, NT); \
    K3_STEP(5, aB, aA, b1, b0, NT); \
    K3_STEP(6, aA, aB, b2, b1, NT); \
    K3_STEP(7, aB, aA, b3, b2, NT); \
    K3_STEP(8, aA, aB, b0, b3, NT); \
    K3_STEP(9, aB, aA, b1, b0, NT); \
    K3_STEP(10, aA, aB, b2, b1, NT); \
    K3_STEP(11, aB, aA, b3, b2, NT); \
    K3_STEP(12, aA, aB, b0, b3, NT); \
    _Pragma("unroll") for (int i = 0; i < 2; ++i) {                                             \
      int id = tid + 512 * i;                                                                   \
      if (id >= 896) id = tid;                                                                  \
      const int t = id / 14, c = id % 14;                                                       \
      zv[i] = *(const u32x4*)(Y + (size_t)(t0 + t) * YLD + 1344 + f0 + c * 8);                  \
      bb[i] = bf2f(bsp[t0 + t]);                                                                \
    }                                                                                           \
    K3_STEP(13, aB, aA, b1, b0, NT); \
    {                                                                                           \
      const int te = (tid < 448) ? tid : 0;                                                     \
      const f32x4* kvp = (const f32x4*)(ws + WS_KVP) + (size_t)h * 448 + te;                    \
      _Pragma("unroll") for (int p = 0; p < 8; ++p) part[p] = kvp[(size_t)p * 16 * 448];        \
      const u32x4* pq = (const u32x4*)(ws + WS_PHQ) + ((size_t)(h * 128 + (b >> 4) * 8 + wave) * 2) * 64 + lane; \
      c1 = pq[0];                                                                               \
      c2 = pq[64];                                                                              \
    }                                                                                           \
    K3_STEP(14, aA, aB, b2, b1, NT); \
    K3_STEP(15, aB, aA, b3, b2, NT); \
  } while (0)
  K3_AISSUE(0);
  K3_AISSUE(1);
  if (wn == 0) { K3_BLOAD(b0, 0, 4); } else { K3_BLOAD(b0, 0, 3); }
  K3_AISSUE(2);
  if (wn == 0) { K3_BLOAD(b1, 1, 4); } else { K3_BLOAD(b1, 1, 3); }
  K3_AISSUE(3);
  if (wn == 0) { K3_BLOAD(b2, 2, 4); } else { K3_BLOAD(b2, 2, 3); }
  K3_AISSUE(4); K3_AISSUE(5); K3_AISSUE(6); K3_AISSUE(7);
  asm volatile("s_waitcnt vmcnt(20)" ::: "memory");
  STAMP(2, 1);
  BAR();
  f32x4 part[8];
  u32x4 c1, c2;
  SCHED();
#pragma unroll
  for (int n = 0; n < 4; ++n)
#pragma unroll
    for (int m = 0; m < 4; ++m) acc[n][m] = f32x4{0.f, 0.f, 0.f, 0.f};
  if (wn == 0) K3_LOOP(4);
  else K3_LOOP(3);
  {
    char* KT = sm + K3_AUX;
    if (tid < 448) {
      const f32x4 s = ((part[0] + part[1]) + (part[2] + part[3])) + ((part[4] + part[5]) + (part[6] + part[7]));
      const int fp = tid >> 3, d0 = 4 * (tid & 7);
      const int mf = (fp >= 28) ? 1 : 0, f = fp - 28 * mf;
      const int pos = mf * 32 + ((f & 15) >> 2) * 8 + (f & 3) + 4 * (f >> 4);
      const unsigned a = pk2(s[0], s[1]), c = pk2(s[2], s[3]);
      char* d = KT + d0 * 144 + pos * 2;
      *(u16*)(d) = (u16)a; *(u16*)(d + 144) = (u16)(a >> 16);
      *(u16*)(d + 288) = (u16)c; *(u16*)(d + 432) = (u16)(c >> 16);
    } else {
      const int i = tid - 448, d = i >> 1, mf = i & 1;
      *(u32x2*)(KT + d * 144 + (mf * 32 + 28) * 2) = u32x2{0u, 0u};
    }
  }
  BAR();
  STAMP(2, 21);
  {
    const char* KT = sm + K3_AUX;
    bf16x8 af[2][2];
#pragma unroll
    for (int dt = 0; dt < 2; ++dt)
#pragma unroll
      for (int mf = 0; mf < 2; ++mf) af[dt][mf] = *(const bf16x8*)(KT + (16 * dt + r16) * 144 + (32 * mf + 8 * g) * 2);
    f32x4 o0 = {0.f, 0.f, 0.f, 0.f}, o1 = {0.f, 0.f, 0.f, 0.f};
    o0 = mfma16(af[0][0], __builtin_bit_cast(bf16x8, c1), o0);
    o1 = mfma16(af[1][0], __builtin_bit_cast(bf16x8, c1), o1);
    o0 = mfma16(af[0][1], __builtin_bit_cast(bf16x8, c2), o0);
    o1 = mfma16(af[1][1], __builtin_bit_cast(bf16x8, c2), o1);
    float den = __shfl(o1[0], 48 + r16, 64);
    den = rbf(rbf(den) + 1e-6f);
    const float inv = 1.f / den;
    const int mtile = (b >> 4) * 8 + wave;
    {
      const int k = h * 28 + 4 * g;
      u16* dst = oh + ((size_t)(mtile * 42 + (k >> 5)) * 64 + ((k >> 3) & 3) * 16 + r16) * 8 + (k & 7);
      st8_wt(dst, u32x2{pk2(rbf(o0[0]) * inv, rbf(o0[1]) * inv), pk2(rbf(o0[2]) * inv, rbf(o0[3]) * inv)});
    }
    if (g != 3) {
      const int k = h * 28 + 16 + 4 * g;
      u16* dst = oh + ((size_t)(mtile * 42 + (k >> 5)) * 64 + ((k >> 3) & 3) * 16 + r16) * 8 + (k & 7);
      st8_wt(dst, u32x2{pk2(rbf(o1[0]) * inv, rbf(o1[1]) * inv), pk2(rbf(o1[2]) * inv, rbf(o1[3]) * inv)});
    }
  }
  {
    char* rd = sm + kq * (64 * K3_RS) + r16 * K3_RS + (64 * wn + 4 * g) * 4;
#pragma unroll
    for (int n = 0; n < 4; ++n)
      if (n < 3 || wn == 0) {
#pragma unroll
        for (int m = 0; m < 4; ++m) *(f32x4*)(rd + 16 * m * K3_RS + 64 * n) = acc[n][m];
      }
  }
  BAR();
#pragma unroll
  for (int i = 0; i < 2; ++i) {
    const int id = tid + 512 * i;
    if (id < 896) {
      const int t = id / 14, c = id % 14;
      const char* p = sm + t * K3_RS + c * 32;
      f32x4 v0 = (*(const f32x4*)(p) + *(const f32x4*)(p + 64 * K3_RS)) +
                 (*(const f32x4*)(p + 128 * K3_RS) + *(const f32x4*)(p + 192 * K3_RS));
      f32x4 v1 = (*(const f32x4*)(p + 16) + *(const f32x4*)(p + 64 * K3_RS + 16)) +
                 (*(const f32x4*)(p + 128 * K3_RS + 16) + *(const f32x4*)(p + 192 * K3_RS + 16));
      u32x4 z;
#pragma unroll
      for (int j = 0; j < 4; ++j) z[j] = gelu_pk(zv[i][j]);
      u32x4 o;
      o[0] = pk2(lo_f(z[0]) * rbf(rbf(v0[0]) + bb[i]), hi_f(z[0]) * rbf(rbf(v0[1]) + bb[i]));
      o[1] = pk2(lo_f(z[1]) * rbf(rbf(v0[2]) + bb[i]), hi_f(z[1]) * rbf(rbf(v0[3]) + bb[i]));
      o[2] = pk2(lo_f(z[2]) * rbf(rbf(v1[0]) + bb[i]), hi_f(z[2]) * rbf(rbf(v1[1]) + bb[i]));
      o[3] = pk2(lo_f(z[3]) * rbf(rbf(v1[2]) + bb[i]), hi_f(z[3]) * rbf(rbf(v1[3]) + bb[i]));
      const int k = 448 + f0 + c * 8, tt = t0 + t;
      st16_wt(oh + ((size_t)((tt >> 4) * 42 + (k >> 5)) * 64 + ((k >> 3) & 3) * 16 + (tt & 15)) * 8, o);
    }
  }
  STAMP(2, 31);
}

__global__ __launch_bounds__(384) void k4_kernel(const u16* __restrict__ x, const char* __restrict__ ws,
                                                  u16* __restrict__ out) {
  __shared__ __attribute__((aligned(16))) char sm[6 * 32 * K3_RS];
  const int tid = threadIdx.x, lane = tid & 63, wave = __builtin_amdgcn_readfirstlane(tid >> 6);
  const int b = blockIdx.x;
  const int xcd = b & 7, idx = b >> 3;
  const int mt_ = xcd * 8 + (idx & 7), nt_ = idx >> 3;
  const int t0 = mt_ * 32, n0 = nt_ * 112;
  const int g = lane >> 4, r16 = lane & 15;
  STAMP(3, 0);
  const u16* ap = (const u16*)(ws + WS_OH) + ((size_t)(mt_ * 2 * 42 + wave * 7) * 64 + lane) * 8;
  const u16* bp = (const u16*)(ws + WS_WT) + ((size_t)(nt_ * 7 * 42 + wave * 7) * 64 + lane) * 8;
  bf16x8 a0[2], b0[7], a1[2], b1[7], a2[2], b2[7], a3[2], b3[7];
#define K4_LOAD(AF, BF, KS)                                                                     \
  do {                                                                                          \
    _Pragma("unroll") for (int m = 0; m < 2; ++m) AF[m] = *(const bf16x8*)(ap + (m * 42 + (KS)) * 512); \
    _Pragma("unroll") for (int n = 0; n < 7; ++n) BF[n] = *(const bf16x8*)(bp + (n * 42 + (KS)) * 512); \
  } while (0)
  K4_LOAD(a0, b0, 0);
  K4_LOAD(a1, b1, 1);
  K4_LOAD(a2, b2, 2);
  K4_LOAD(a3, b3, 3);
  u32x4 xv[2];
#pragma unroll
  for (int i = 0; i < 2; ++i) {
    int id = tid + 384 * i;
    if (id >= 448) id = tid;
    xv[i] = *(const u32x4*)(x + (size_t)(t0 + id / 14) * D_ + n0 + (id % 14) * 8);
  }
  SCHED();
  f32x4 acc[7][2];
#pragma unroll
  for (int n = 0; n < 7; ++n) { acc[n][0] = f32x4{0.f, 0.f, 0.f, 0.f}; acc[n][1] = f32x4{0.f, 0.f, 0.f, 0.f}; }
#define K4_MMA(AF, BF)                                                                          \
  do {                                                                                          \
    _Pragma("unroll") for (int n = 0; n < 7; ++n) {                                             \
      acc[n][0] = mfma16(BF[n], AF[0], acc[n][0]);                                              \
      acc[n][1] = mfma16(BF[n], AF[1], acc[n][1]);                                              \
    }                                                                                           \
    SCHED();                                                                                    \
  } while (0)
  K4_MMA(a0, b0); K4_LOAD(a0, b0, 4); SCHED();
  K4_MMA(a1, b1); K4_LOAD(a1, b1, 5); SCHED();
  K4_MMA(a2, b2); K4_LOAD(a2, b2, 6); SCHED();
  K4_MMA(a3, b3);
  K4_MMA(a0, b0);
  K4_MMA(a1, b1);
  K4_MMA(a2, b2);
  STAMP(3, 1);
  char* rd = sm + wave * (32 * K3_RS) + r16 * K3_RS + 16 * g;
#pragma unroll
  for (int n = 0; n < 7; ++n)
#pragma unroll
    for (int m = 0; m < 2; ++m) *(f32x4*)(rd + 16 * m * K3_RS + 64 * n) = acc[n][m];
  __syncthreads();
#pragma unroll
  for (int i = 0; i < 2; ++i) {
    const int id = tid + 384 * i;
    if (id < 448) {
      const int et = id / 14, ec = id % 14;
      const char* p = sm + et * K3_RS + ec * 32;
      f32x4 v0 = {0.f, 0.f, 0.f, 0.f}, v1 = {0.f, 0.f, 0.f, 0.f};
#pragma unroll
      for (int w = 0; w < 6; ++w) {
        v0 += *(const f32x4*)(p + w * 32 * K3_RS);
        v1 += *(const f32x4*)(p + w * 32 * K3_RS + 16);
      }
      u32x4 o;
      o[0] = pk2(lo_f(xv[i][0]) + v0[0], hi_f(xv[i][0]) + v0[1]);
      o[1] = pk2(lo_f(xv[i][1]) + v0[2], hi_f(xv[i][1]) + v0[3]);
      o[2] = pk2(lo_f(xv[i][2]) + v1[0], hi_f(xv[i][2]) + v1[1]);
      o[3] = pk2(lo_f(xv[i][3]) + v1[2], hi_f(xv[i][3]) + v1[3]);
      *(u32x4*)(out + (size_t)(t0 + et) * D_ + n0 + ec * 8) = o;
    }
  }
  STAMP(3, 31);
}

extern "C" void kernel_launch(void* const* d_in, const int* in_sizes, int n_in, void* d_out, int out_size,
                              void* d_ws, size_t ws_size, hipStream_t stream) {
  const u16* x = (const u16*)d_in[0];
  const u16* wqkv = (const u16*)d_in[1];
  const u16* wphi = (const u16*)d_in[2];
  const u16* wo = (const u16*)d_in[3];
  const u16* win = (const u16*)d_in[5];
  const u16* wsp = (const u16*)d_in[6];
  const u16* bsp = (const u16*)d_in[7];
  const u16* wout = (const u16*)d_in[8];
  char* ws = (char*)d_ws;
  k1_kernel<<<245, 768, 0, stream>>>(x, wqkv, wo, win, wout, wsp, ws);
  k2_kernel<<<384, 512, 0, stream>>>(wphi, ws);
  k3_kernel<<<256, 512, 0, stream>>>(wsp, bsp, wphi, ws);
  k4_kernel<<<256, 384, 0, stream>>>(x, ws, (u16*)d_out);
}
```

```cpp
#include <hip/hip_runtime.h>
#include <hip/hip_bf16.h>
#include <stdint.h>

typedef __attribute__((ext_vector_type(8))) short bf16x8;
typedef __attribute__((ext_vector_type(4))) short s16x4;
typedef __attribute__((ext_vector_type(4))) float f32x4;
typedef __attribute__((ext_vector_type(2))) float f32x2;
typedef __attribute__((ext_vector_type(4))) unsigned u32x4;
typedef __attribute__((ext_vector_type(2))) unsigned u32x2;
typedef __attribute__((ext_vector_type(2))) __bf16 bf2_t;
typedef unsigned short u16;
#define LDS_AS __attribute__((address_space(3)))

constexpr int S_ = 2048, D_ = 448, NQKV = 1344, NIN = 1792, FH = 896, KO = 1344;

__device__ __forceinline__ unsigned pk2(float a, float b) {
  f32x2 v = {a, b};
  return __builtin_bit_cast(unsigned, __builtin_convertvector(v, bf2_t));
}
__device__ __forceinline__ float lo_f(unsigned u) { return __uint_as_float(u << 16); }
__device__ __forceinline__ float hi_f(unsigned u) { return __uint_as_float(u & 0xffff0000u); }
__device__ __forceinline__ float rbf(float a) { return lo_f(pk2(a, 0.f)); }
__device__ __forceinline__ float bf2f(u16 v) { return __uint_as_float(((unsigned)v) << 16); }
__device__ __forceinline__ float dot2(unsigned a, unsigned b, float c) {
  return __builtin_amdgcn_fdot2_f32_bf16(__builtin_bit_cast(bf2_t, a), __builtin_bit_cast(bf2_t, b), c, false);
}

__device__ __forceinline__ float x16_max(float v) {
  auto r = __builtin_amdgcn_permlane16_swap(__float_as_uint(v), __float_as_uint(v), false, false);
  return fmaxf(__uint_as_float(r[0]), __uint_as_float(r[1]));
}
__device__ __forceinline__ float x32_max(float v) {
  auto r = __builtin_amdgcn_permlane32_swap(__float_as_uint(v), __float_as_uint(v), false, false);
  return fmaxf(__uint_as_float(r[0]), __uint_as_float(r[1]));
}
__device__ __forceinline__ float x16_min(float v) {
  auto r = __builtin_amdgcn_permlane16_swap(__float_as_uint(v), __float_as_uint(v), false, false);
  return fminf(__uint_as_float(r[0]), __uint_as_float(r[1]));
}
__device__ __forceinline__ float x32_min(float v) {
  auto r = __builtin_amdgcn_permlane32_swap(__float_as_uint(v), __float_as_uint(v), false, false);
  return fminf(__uint_as_float(r[0]), __uint_as_float(r[1]));
}
__device__ __forceinline__ float x16_add(float v) {
  auto r = __builtin_amdgcn_permlane16_swap(__float_as_uint(v), __float_as_uint(v), false, false);
  return __uint_as_float(r[0]) + __uint_as_float(r[1]);
}
__device__ __forceinline__ float x32_add(float v) {
  auto r = __builtin_amdgcn_permlane32_swap(__float_as_uint(v), __float_as_uint(v), false, false);
  return __uint_as_float(r[0]) + __uint_as_float(r[1]);
}
__device__ __forceinline__ float red8_add(float v) {
  v += __uint_as_float(__builtin_amdgcn_mov_dpp(__float_as_uint(v), 0xB1, 0xf, 0xf, true));
  v += __uint_as_float(__builtin_amdgcn_mov_dpp(__float_as_uint(v), 0x4E, 0xf, 0xf, true));
  v += __uint_as_float(__builtin_amdgcn_mov_dpp(__float_as_uint(v), 0x141, 0xf, 0xf, true));
  return v;
}
__device__ __forceinline__ f32x4 mfma16(bf16x8 a, bf16x8 b, f32x4 c) {
  return __builtin_amdgcn_mfma_f32_16x16x32_bf16(a, b, c, 0, 0, 0);
}
__device__ __forceinline__ unsigned gelu_pk(unsigned p) {
  const f32x2 u = {lo_f(p), hi_f(p)};
  const f32x2 c1 = {-2.3022081986f, -2.3022081986f};
  const f32x2 c2 = {-0.1029432397f, -0.1029432397f};
  f32x2 t = u * u;
  f32x2 a = u * (t * c2 + c1);
  f32x2 d = {1.f + __builtin_amdgcn_exp2f(a[0]), 1.f + __builtin_amdgcn_exp2f(a[1])};
  f32x2 r = {__builtin_amdgcn_rcpf(d[0]), __builtin_amdgcn_rcpf(d[1])};
  f32x2 o = u * r;
  return pk2(o[0], o[1]);
}
#define SCHED() __builtin_amdgcn_sched_barrier(0)
#define BAR() do { SCHED(); asm volatile("" ::: "memory"); __builtin_amdgcn_s_waitcnt(0xC07F); __builtin_amdgcn_s_barrier(); asm volatile("" ::: "memory"); SCHED(); } while (0)
__device__ __forceinline__ void glds16(const void* g, char* l) {
  __builtin_amdgcn_global_load_lds((const unsigned*)g, (LDS_AS unsigned*)l, 16, 0, 0);
}
__device__ __forceinline__ bf16x8 tr_frag(const char* p0, const char* p1) {
  s16x4 a = __builtin_amdgcn_ds_read_tr16_b64_v4i16((LDS_AS s16x4*)p0);
  s16x4 c = __builtin_amdgcn_ds_read_tr16_b64_v4i16((LDS_AS s16x4*)p1);
  return __builtin_shufflevector(a, c, 0, 1, 2, 3, 4, 5, 6, 7);
}

constexpr int YLD = 3136;
constexpr size_t WS_Y = 0;
constexpr size_t WS_Z2N = WS_Y + (size_t)S_ * YLD * 2 + 256;
constexpr size_t WS_OH = WS_Z2N + (size_t)S_ * 896 * 2;
constexpr size_t WS_WT = WS_OH + (size_t)S_ * KO * 2;
constexpr size_t WS_KVP = WS_WT + (size_t)D_ * KO * 2;
constexpr size_t WS_PHQ = WS_KVP + (size_t)8 * 16 * 56 * 32 * 4;
constexpr size_t WS_END = WS_PHQ + (size_t)16 * 128 * 2048;

#define STAMP(K, I) do { } while (0)
__device__ __forceinline__ void st16_wt(void* p, u32x4 v) {
  asm volatile("global_store_dwordx4 %0, %1, off sc0 sc1\n\ts_nop 1" ::"v"(p), "v"(v) : "memory");
}
__device__ __forceinline__ void st8_wt(void* p, u32x2 v) {
  asm volatile("global_store_dwordx2 %0, %1, off sc0 sc1\n\ts_nop 1" ::"v"(p), "v"(v) : "memory");
}
#define SGB(mask, n) __builtin_amdgcn_sched_group_barrier(mask, n, 0)

__device__ __forceinline__ void phi_map(bf16x8 qf, bf16x8 wp0, bf16x8 wp1, int g, unsigned (&pp)[2][2],
                                        unsigned (&pm)[2][2]) {
  f32x4 z = {0.f, 0.f, 0.f, 0.f};
  f32x4 p0 = mfma16(wp0, qf, z);
  f32x4 p1 = mfma16(wp1, qf, z);
  float v[8];
  {
    unsigned a = pk2(p0[0], p0[1]), b = pk2(p0[2], p0[3]), c = pk2(p1[0], p1[1]), d = pk2(p1[2], p1[3]);
    v[0] = lo_f(a); v[1] = hi_f(a); v[2] = lo_f(b); v[3] = hi_f(b);
    v[4] = lo_f(c); v[5] = hi_f(c); v[6] = lo_f(d); v[7] = hi_f(d);
  }
  const bool valid1 = (g != 3);
  if (!valid1) { v[4] = v[0]; v[5] = v[0]; v[6] = v[0]; v[7] = v[0]; }
  float mx = fmaxf(fmaxf(fmaxf(v[0], v[1]), fmaxf(v[2], v[3])), fmaxf(fmaxf(v[4], v[5]), fmaxf(v[6], v[7])));
  float mn = fminf(fminf(fminf(v[0], v[1]), fminf(v[2], v[3])), fminf(fminf(v[4], v[5]), fminf(v[6], v[7])));
  mx = x32_max(x16_max(mx));
  mn = x32_min(x16_min(mn));
  const float L2E = 1.4426950408889634f;
  const float mxl = mx * L2E, mnl = mn * L2E;
  float ep[8], em[8];
  float sp = 0.f, sm = 0.f;
#pragma unroll
  for (int j = 0; j < 8; ++j) {
    ep[j] = __builtin_amdgcn_exp2f(__builtin_fmaf(v[j], L2E, -mxl));
    em[j] = __builtin_amdgcn_exp2f(__builtin_fmaf(v[j], -L2E, mnl));
    if (j >= 4) { ep[j] = valid1 ? ep[j] : 0.f; em[j] = valid1 ? em[j] : 0.f; }
    sp += ep[j];
    sm += em[j];
  }
  sp = x32_add(x16_add(sp));
  sm = x32_add(x16_add(sm));
  const float rp = __builtin_amdgcn_rcpf(sp), rm = __builtin_amdgcn_rcpf(sm);
  pp[0][0] = pk2(ep[0] * rp, ep[1] * rp); pp[0][1] = pk2(ep[2] * rp, ep[3] * rp);
  pp[1][0] = pk2(ep[4] * rp, ep[5] * rp); pp[1][1] = pk2(ep[6] * rp, ep[7] * rp);
  pm[0][0] = pk2(em[0] * rm, em[1] * rm); pm[0][1] = pk2(em[2] * rm, em[3] * rm);
  pm[1][0] = pk2(em[4] * rm, em[5] * rm); pm[1][1] = pk2(em[6] * rm, em[7] * rm);
}

constexpr int K1_XST = 128 * 128;
constexpr int K1_STAGE = K1_XST + 64 * 448;
constexpr int K1_LDS = 3 * K1_STAGE;

__global__ __launch_bounds__(768) void k1_kernel(const u16* __restrict__ x, const u16* __restrict__ wqkv,
                                                  const u16* __restrict__ wo, const u16* __restrict__ win,
                                                  const u16* __restrict__ wout, const u16* __restrict__ wsp,
                                                  char* __restrict__ ws) {
  __shared__ __attribute__((aligned(16))) char sm[K1_LDS];
  const int tid = threadIdx.x, lane = tid & 63, wave = __builtin_amdgcn_readfirstlane(tid >> 6);
  const int b = blockIdx.x;
  STAMP(0, 0);
  if (b >= 224) {
    const int kb = b - 224;
    const int k0 = kb * 64;
    if (kb < 21) {
    u16* T = (u16*)sm;
    constexpr int TS = 456;
    u32x4 v[5];
#pragma unroll
    for (int j = 0; j < 5; ++j) {
      int i = tid + 768 * j;
      if (i >= 64 * 56) i = tid;
      const int r = i / 56, c = i % 56;
      const int k = k0 + r;
      const u16* src = (k < 448) ? (wo + (size_t)k * 448) : (wout + (size_t)(k - 448) * 448);
      v[j] = *(const u32x4*)(src + c * 8);
    }
#pragma unroll
    for (int j = 0; j < 5; ++j) {
      const int i = tid + 768 * j;
      if (i < 64 * 56) *(u32x4*)(T + (i / 56) * TS + (i % 56) * 8) = v[j];
    }
    __syncthreads();
    u16* wT = (u16*)(ws + WS_WT);
    for (int i = tid; i < 448 * 8; i += 768) {
      const int n = i >> 3, c = i & 7;
      u16 t[8];
#pragma unroll
      for (int j = 0; j < 8; ++j) t[j] = T[(c * 8 + j) * TS + n];
      u32x4 o;
#pragma unroll
      for (int j = 0; j < 4; ++j) o[j] = (unsigned)t[2 * j] | ((unsigned)t[2 * j + 1] << 16);
      st16_wt(wT + ((size_t)((n >> 4) * 42 + (k0 >> 5) + (c >> 2)) * 64 + (c & 3) * 16 + (n & 15)) * 8, o);
    }
    }
    STAMP(0, 1);
    STAMP(0, 31);
    return;
  }

  const int xcd = b & 7, idx = b >> 3;
  const int cb = (xcd & 1) * 7 + idx % 7;
  const int rt = (xcd >> 1) * 4 + idx / 7;
  const int row0 = rt * 128;
  const int g = lane >> 4, r16 = lane & 15;
  const int wm = (wave >> 1) & 3, wn = wave & 1;
  constexpr int RST = 464;

  if (wave >= 8) {
    const int lw = wave - 8;
    const u16* W = (cb < 6) ? (wqkv + 224 * cb) : (win + 224 * (cb - 6));
    const int ldw = (cb < 6) ? NQKV : NIN;
    const u16* src[11];
#pragma unroll
    for (int j = 0; j < 11; ++j) {
      const int i = lw + 4 * j;
      if (j < 4) {
        const int row = 8 * i + (lane >> 3);
        src[j] = x + (size_t)(row0 + row) * D_ + (((lane & 7) ^ ((row >> 1) & 7)) << 3);
      } else {
        const int o = (i - 16) * 1024 + lane * 16;
        const int k = o / 448, cp = (o % 448) >> 4;
        const int G = (cp >> 1) ^ ((k >> 3) & 1);
        src[j] = W + (size_t)k * ldw + 8 * (2 * G + (cp & 1));
      }
    }
    const int wstep = 64 * ldw;
    char* dst0 = sm + lw * 1024;
#pragma unroll
    for (int s = 0; s < 2; ++s)
#pragma unroll
      for (int j = 0; j < 11; ++j) glds16(src[j] + s * (j < 4 ? 64 : wstep), dst0 + s * K1_STAGE + j * 4096);
    int nb = 2;
#pragma unroll 1
    for (int t = 0; t < 7; ++t) {
      if (t < 6) asm volatile("s_waitcnt vmcnt(11)" ::: "memory");
      else asm volatile("s_waitcnt vmcnt(0)" ::: "memory");
      __builtin_amdgcn_s_barrier();
      if (t + 2 < 7) {
        char* dst = dst0 + nb * K1_STAGE;
#pragma unroll
        for (int j = 0; j < 11; ++j) glds16(src[j] + (t + 2) * (j < 4 ? 64 : wstep), dst + j * 4096);
        nb = (nb == 2) ? 0 : nb + 1;
      }
    }
    __builtin_amdgcn_s_barrier();
    __builtin_amdgcn_s_barrier();
  } else {
    f32x4 acc[7][2], sacc = {0.f, 0.f, 0.f, 0.f}, qacc = {0.f, 0.f, 0.f, 0.f}, cacc[2];
#pragma unroll
    for (int nt = 0; nt < 7; ++nt) { acc[nt][0] = f32x4{0.f, 0.f, 0.f, 0.f}; acc[nt][1] = f32x4{0.f, 0.f, 0.f, 0.f}; }
    cacc[0] = f32x4{0.f, 0.f, 0.f, 0.f}; cacc[1] = f32x4{0.f, 0.f, 0.f, 0.f};
    const bf16x8 ones = {0x3F80, 0x3F80, 0x3F80, 0x3F80, 0x3F80, 0x3F80, 0x3F80, 0x3F80};
    const int sw = (r16 >> 1) & 7;
    const int xrow = (32 * wm + 16 * wn + r16) * 128, xd = wn ? -2048 : 2048;
    const int xo0 = xrow + ((g ^ sw) << 4), xo1 = xrow + (((4 | g) ^ sw) << 4);
    int woff[7];
#pragma unroll
    for (int i = 0; i < 7; ++i) {
      int nt = i + 2 * wm;
      nt = (nt >= 7) ? nt - 7 : nt;
      const int G = 7 * wn + nt;
      woff[i] = K1_XST + (8 * g + ((lane >> 2) & 3)) * 448 + (lane & 3) * 8 + ((G ^ (g & 1)) << 5);
    }
    bf16x8 xa[2], wa[7], xb[2], wb[7];
#define K1_READ(XF, WF, SB, KS)                                                              \
  do {                                                                                       \
    XF[0] = *(const bf16x8*)((SB) + ((KS) ? xo1 : xo0));                                     \
    XF[1] = *(const bf16x8*)((SB) + ((KS) ? xo1 : xo0) + xd);                                \
    _Pragma("unroll") for (int nt = 0; nt < 7; ++nt) {                                       \
      const char* p_ = (SB) + woff[nt] + (KS)*32 * 448;                                      \
      WF[nt] = tr_frag(p_, p_ + 4 * 448);                                                    \
    }                                                                                        \
  } while (0)
#define K1_MMA(XF, WF)                                                                       \
  do {                                                                                       \
    _Pragma("unroll") for (int nt = 0; nt < 7; ++nt) acc[nt][0] = mfma16(WF[nt], XF[0], acc[nt][0]); \
    sacc = mfma16(ones, XF[0], sacc);                                                        \
    qacc = mfma16(XF[0], XF[0], qacc);                                                       \
    _Pragma("unroll") for (int nt = 0; nt < 7; ++nt) acc[nt][1] = mfma16(WF[nt], XF[1], acc[nt][1]); \
    cacc[0] = mfma16(WF[0], ones, cacc[0]);                                                  \
    cacc[1] = mfma16(WF[1], ones, cacc[1]);                                                  \
  } while (0)
    int rb = 0;
#pragma unroll 1
    for (int t = 0; t < 7; ++t) {
      BAR();
      STAMP(0, 3 + t);
      const char* sb = sm + rb * K1_STAGE;
      rb = (rb == 2) ? 0 : rb + 1;
      K1_READ(xa, wa, sb, 0);
      SCHED();
      K1_READ(xb, wb, sb, 1);
      K1_MMA(xa, wa);
      _Pragma("unroll") for (int i_ = 0; i_ < 8; ++i_) { SGB(0x008, 1); SGB(0x100, 2); }
      SCHED();
      K1_MMA(xb, wb);
    }
    BAR();
    STAMP(0, 17);
    float* CS = (float*)(sm + 128 * RST);
    f32x2* RS_ = (f32x2*)(CS + 224);
    if (r16 == 0) {
#pragma unroll
      for (int i = 0; i < 2; ++i) {
        int nt = i + 2 * wm;
        nt = (nt >= 7) ? nt - 7 : nt;
        *(f32x4*)(CS + 112 * wn + 16 * nt + 4 * g) = cacc[i];
      }
    }
    {
      const float qd = (r16 & 3) == 0 ? qacc[0] : (r16 & 3) == 1 ? qacc[1] : (r16 & 3) == 2 ? qacc[2] : qacc[3];
      if (g == (r16 >> 2)) {
        const float mean = sacc[0] * (1.f / 448.f);
        const float var = fmaxf(qd * (1.f / 448.f) - mean * mean, 0.f);
        const float sd = rbf(sqrtf(rbf(rbf(var) + 1e-5f)));
        const float r = 1.f / sd;
        RS_[32 * wm + 16 * wn + r16] = f32x2{r, r * rbf(mean)};
      }
    }
    BAR();
    f32x2 st[2];
    st[0] = RS_[32 * wm + 16 * wn + r16];
    st[1] = RS_[32 * wm + 16 * (wn ^ 1) + r16];
#pragma unroll
    for (int i = 0; i < 7; ++i) {
      int nt = i + 2 * wm;
      nt = (nt >= 7) ? nt - 7 : nt;
      const f32x4 c4 = *(const f32x4*)(CS + 112 * wn + 16 * nt + 4 * g);
#pragma unroll
      for (int j = 0; j < 2; ++j) {
        char* dst = sm + (32 * wm + 16 * (j ^ wn) + r16) * RST + (112 * wn + 4 * g) * 2 + 32 * nt;
        const f32x4 v = acc[i][j] * st[j][0] - c4 * st[j][1];
        *(u32x2*)(dst) = u32x2{pk2(v[0], v[1]), pk2(v[2], v[3])};
      }
    }
  }
  __syncthreads();
  u16* dstb = (u16*)(ws + WS_Y) + (size_t)row0 * YLD + 224 * cb;
#pragma unroll
  for (int i = 0; i < 5; ++i) {
    const int id = tid + 768 * i;
    if (id < 128 * 28) {
      const int s = id / 28, c = id % 28;
      st16_wt(dstb + (size_t)s * YLD + c * 8, *(const u32x4*)(sm + s * RST + c * 16));
    }
  }
  STAMP(0, 31);
}

__device__ __forceinline__ float red64_add(float v) {
  v = red8_add(v);
  v += __uint_as_float(__builtin_amdgcn_mov_dpp(__float_as_uint(v), 0x140, 0xf, 0xf, true));
  return x32_add(x16_add(v));
}

constexpr int K2_PW = 64 * 80 + 32 * 64;
__global__ __launch_bounds__(512) void k2_kernel(const u16* __restrict__ wphi, const u16* __restrict__ wsp,
                                                  char* __restrict__ ws) {
  __shared__ __attribute__((aligned(16))) char sm[8 * 56 * 32 * 4 + 2048];
  const int tid = threadIdx.x, lane = tid & 63, wave = __builtin_amdgcn_readfirstlane(tid >> 6);
  const int b = blockIdx.x;
  const u16* Y = (const u16*)(ws + WS_Y);
  STAMP(1, 0);
  if (b < 128) {
    const int h = b & 15, rg = b >> 4;
    const int g = lane >> 4, r16 = lane & 15;
    const int rowb = rg * 256 + wave * 32;
    u32x2 kv_[2][2];
#pragma unroll
    for (int st = 0; st < 2; ++st) {
      const u16* kp = Y + (size_t)(rowb + st * 16 + r16) * YLD + 448 + h * 28 + 8 * g;
      kv_[st][0] = *(const u32x2*)(kp);
      kv_[st][1] = *(const u32x2*)(kp + 4);
    }
    u32x2 vv[4];
#pragma unroll
    for (int i = 0; i < 4; ++i) {
      const int row = (lane >> 3) + 8 * i, pc = lane & 7;
      vv[i] = *(const u32x2*)(Y + (size_t)(rowb + row) * YLD + 896 + h * 28 + 4 * (pc < 7 ? pc : 0));
      if (pc == 7) vv[i] = u32x2{0x00003F80u, 0u};
    }
    u32x4 wraw = {0u, 0u, 0u, 0u};
    if (tid < 98) wraw = *(const u32x4*)(wphi + (size_t)h * 784 + tid * 8);
    char* WP = sm + 8 * 56 * 32 * 4;
    if (tid < 98) *(u32x4*)(WP + tid * 16) = wraw;
    char* PK = sm + wave * K2_PW;
    char* VS = PK + 64 * 80;
    __syncthreads();
    STAMP(1, 1);
    const char* wp = WP + (8 * g + ((lane >> 2) & 3)) * 56 + (lane & 3) * 8;
    bf16x8 wp0 = tr_frag(wp, wp + 4 * 56);
    bf16x8 wp1 = tr_frag(wp + 32, wp + 4 * 56 + 32);
    if (g == 3) {
#pragma unroll
      for (int j = 4; j < 8; ++j) { wp0[j] = 0; wp1[j] = 0; }
    }
    f32x4 kacc[4][2];
#pragma unroll
    for (int a = 0; a < 4; ++a) { kacc[a][0] = f32x4{0.f, 0.f, 0.f, 0.f}; kacc[a][1] = f32x4{0.f, 0.f, 0.f, 0.f}; }
#pragma unroll
    for (int pr = 0; pr < 1; ++pr) {
#pragma unroll
      for (int i = 0; i < 4; ++i) {
        const int row = (lane >> 3) + 8 * i;
        const int slot = 2 * (row & 15) + (row >> 4);
        *(u32x2*)(VS + slot * 64 + (lane & 7) * 8) = vv[4 * pr + i];
      }
      unsigned pp[2][2][2], pm[2][2][2];
#pragma unroll
      for (int mt = 0; mt < 2; ++mt) {
        u32x4 q4 = {kv_[2 * pr + mt][0][0], kv_[2 * pr + mt][0][1], kv_[2 * pr + mt][1][0], kv_[2 * pr + mt][1][1]};
        if (g == 3) { q4[2] = 0u; q4[3] = 0u; }
        phi_map(__builtin_bit_cast(bf16x8, q4), wp0, wp1, g, pp[mt], pm[mt]);
      }
#pragma unroll
      for (int ft = 0; ft < 2; ++ft) {
        if (ft == 0 || g != 3) {
          char* d0 = PK + (16 * ft + 4 * g) * 80 + r16 * 4;
#pragma unroll
          for (int j = 0; j < 2; ++j) {
            *(unsigned*)(d0 + (2 * j) * 80) = (pp[0][ft][j] & 0xffffu) | (pp[1][ft][j] << 16);
            *(unsigned*)(d0 + (2 * j + 1) * 80) = (pp[0][ft][j] >> 16) | (pp[1][ft][j] & 0xffff0000u);
            *(unsigned*)(d0 + (28 + 2 * j) * 80) = (pm[0][ft][j] & 0xffffu) | (pm[1][ft][j] << 16);
            *(unsigned*)(d0 + (28 + 2 * j + 1) * 80) = (pm[0][ft][j] >> 16) | (pm[1][ft][j] & 0xffff0000u);
          }
        }
      }
      __builtin_amdgcn_s_waitcnt(0xC07F);
      __builtin_amdgcn_wave_barrier();
      bf16x8 bfr[2];
      {
        const char* vp = VS + (8 * g + ((lane >> 2) & 3)) * 64 + (lane & 3) * 8;
        bfr[0] = tr_frag(vp, vp + 4 * 64);
        bfr[1] = tr_frag(vp + 32, vp + 4 * 64 + 32);
      }
#pragma unroll
      for (int a = 0; a < 4; ++a) {
        bf16x8 af = *(const bf16x8*)(PK + (16 * a + r16) * 80 + g * 16);
        kacc[a][0] = mfma16(af, bfr[0], kacc[a][0]);
        kacc[a][1] = mfma16(af, bfr[1], kacc[a][1]);
      }
      __builtin_amdgcn_s_waitcnt(0xC07F);
      __builtin_amdgcn_wave_barrier();
    }
    STAMP(1, 2);
    __syncthreads();
    float* red = (float*)sm + wave * (56 * 32);
#pragma unroll
    for (int a = 0; a < 4; ++a)
#pragma unroll
      for (int c = 0; c < 2; ++c)
#pragma unroll
        for (int j = 0; j < 4; ++j) {
          const int f = 16 * a + 4 * g + j;
          if (f < 56) red[f * 32 + 16 * c + r16] = kacc[a][c][j];
        }
    __syncthreads();
    if (tid < 448) {
      const f32x4* rp = (const f32x4*)sm + tid;
      f32x4 s = (rp[0] + rp[448]) + (rp[2 * 448] + rp[3 * 448]);
      s += (rp[4 * 448] + rp[5 * 448]) + (rp[6 * 448] + rp[7 * 448]);
      st16_wt((f32x4*)(ws + WS_KVP) + (size_t)(rg * 16 + h) * 448 + tid, __builtin_bit_cast(u32x4, s));
    }
  } else if (b >= 256) {
    const int h = b & 15, rg = (b - 256) >> 4;
    const int g = lane >> 4, r16 = lane & 15;
    u32x2 qv[2][2];
#pragma unroll
    for (int st = 0; st < 2; ++st) {
      const u16* qp = Y + (size_t)(rg * 256 + wave * 32 + st * 16 + r16) * YLD + h * 28 + 8 * g;
      qv[st][0] = *(const u32x2*)(qp);
      qv[st][1] = *(const u32x2*)(qp + 4);
    }
    u32x4 wraw = {0u, 0u, 0u, 0u};
    if (tid < 98) wraw = *(const u32x4*)(wphi + (size_t)h * 784 + tid * 8);
    if (tid < 98) *(u32x4*)(sm + tid * 16) = wraw;
    __syncthreads();
    const char* wp = sm + (8 * g + ((lane >> 2) & 3)) * 56 + (lane & 3) * 8;
    bf16x8 wp0 = tr_frag(wp, wp + 4 * 56);
    bf16x8 wp1 = tr_frag(wp + 32, wp + 4 * 56 + 32);
    if (g == 3) {
#pragma unroll
      for (int j = 4; j < 8; ++j) { wp0[j] = 0; wp1[j] = 0; }
    }
#pragma unroll
    for (int st = 0; st < 2; ++st) {
      u32x4 q4 = {qv[st][0][0], qv[st][0][1], qv[st][1][0], qv[st][1][1]};
      if (g == 3) { q4[2] = 0u; q4[3] = 0u; }
      unsigned pp[2][2], pm[2][2];
      phi_map(__builtin_bit_cast(bf16x8, q4), wp0, wp1, g, pp, pm);
      u32x4* dst = (u32x4*)(ws + WS_PHQ) + ((size_t)(h * 128 + rg * 16 + wave * 2 + st) * 2) * 64 + lane;
      st16_wt(dst, u32x4{pp[0][0], pp[0][1], pp[1][0], pp[1][1]});
      st16_wt(dst + 64, u32x4{pm[0][0], pm[0][1], pm[1][0], pm[1][1]});
    }
  } else {
    const int row0 = (b - 128) * 16 + wave * 2;
    u16* z2n = (u16*)(ws + WS_Z2N);
    const int le = (lane < 56) ? lane : 0;
    u32x4 v[2][2];
#pragma unroll
    for (int r = 0; r < 2; ++r)
#pragma unroll
      for (int c = 0; c < 2; ++c) v[r][c] = *(const u32x4*)(Y + (size_t)(row0 + r) * YLD + 2240 + (le + 56 * c) * 8);
#pragma unroll
    for (int r = 0; r < 2; ++r) {
      float s = 0.f;
#pragma unroll
      for (int c = 0; c < 2; ++c)
#pragma unroll
        for (int j = 0; j < 4; ++j) {
          v[r][c][j] = gelu_pk(v[r][c][j]);
          s = dot2(v[r][c][j], 0x3F803F80u, s);
        }
      if (lane >= 56) s = 0.f;
      const float mean = red64_add(s) * (1.f / 896.f);
      const float mb = rbf(mean);
      float q = 0.f;
#pragma unroll
      for (int c = 0; c < 2; ++c)
#pragma unroll
        for (int j = 0; j < 4; ++j) {
          const float a = lo_f(v[r][c][j]) - mean, e = hi_f(v[r][c][j]) - mean;
          q += a * a + e * e;
          v[r][c][j] = pk2(lo_f(v[r][c][j]) - mb, hi_f(v[r][c][j]) - mb);
        }
      if (lane >= 56) q = 0.f;
      const float var = red64_add(q) * (1.f / 896.f);
      const float sd = rbf(sqrtf(rbf(rbf(var) + 1e-5f)));
      const float inv = 1.f / sd;
      if (lane < 56) {
#pragma unroll
        for (int c = 0; c < 2; ++c) {
          u32x4 o;
#pragma unroll
          for (int j = 0; j < 4; ++j) o[j] = pk2(lo_f(v[r][c][j]) * inv, hi_f(v[r][c][j]) * inv);
          *(u32x4*)(sm + (wave * 2 + r) * 1808 + (lane + 56 * c) * 16) = o;
        }
      }
    }
    __syncthreads();
    {
      const int rb0 = (b - 128) * 16;
      const int kb = rb0 >> 5, half = (rb0 >> 4) & 1;
      const int g = lane >> 4;
#pragma unroll
      for (int i = 0; i < 4; ++i) {
        const int u = wave + 8 * i;
        if (u < 28) {
          const int T = 2 * u + (g >> 1), gp = g & 1;
          const char* p = sm + (8 * gp + ((lane >> 2) & 3)) * 1808 + T * 32 + (lane & 3) * 8;
          bf16x8 fr = tr_frag(p, p + 4 * 1808);
          const int pn = T / 7, n = T % 7;
          st16_wt(z2n + ((size_t)((pn * 64 + kb) * 7 + n) * 64 + (2 * half + gp) * 16 + (lane & 15)) * 8, __builtin_bit_cast(u32x4, fr));
        }
      }
    }
    {
      const int bb = b - 128;
      const char* p = (const char*)wsp + (size_t)((bb & 7) >> 1) * (2u << 20) + (size_t)((bb >> 3) * 8 + wave) * 16384 + lane * 16;
      unsigned a = 0;
#pragma unroll
      for (int i = 0; i < 16; ++i) {
        u32x4 v = *(const u32x4*)(p + i * 1024);
        a |= v[0] & v[1] & v[2] & v[3];
      }
      asm volatile("" ::"v"(a));
    }
  }
  STAMP(1, 31);
}

constexpr int K3_RS = 464;
constexpr int K3_STAGE = 64 * 256;
constexpr int K3_NB = 8;
constexpr int K3_AUX = K3_NB * K3_STAGE;
__device__ __forceinline__ void glds16_asm(const void* gsrc, unsigned lds_dst) {
  unsigned keep;
  asm volatile("s_mov_b32 %0, m0\n\ts_mov_b32 m0, %2\n\ts_nop 0\n\tglobal_load_lds_dwordx4 %1, off\n\ts_mov_b32 m0, %0"
               : "=&s"(keep) : "v"(gsrc), "s"(lds_dst) : "memory");
}
__global__ __launch_bounds__(512) void k3_kernel(const u16* __restrict__ wsp, const u16* __restrict__ bsp,
                                                  const u16* __restrict__ wphi, char* __restrict__ ws) {
  __shared__ __attribute__((aligned(16))) char sm[K3_NB * K3_STAGE + 32 * 144];
  const int tid = threadIdx.x, lane = tid & 63, wave = __builtin_amdgcn_readfirstlane(tid >> 6);
  const int b = blockIdx.x;
  const int xcd = b & 7, idx = b >> 3;
  const int mt_ = (xcd >> 1) * 8 + (idx & 7);
  const int nt_ = (xcd & 1) * 4 + (idx >> 3);
  const int t0 = mt_ * 64, f0 = nt_ * 112;
  const u16* z2n = (const u16*)(ws + WS_Z2N);
  const u16* Y = (const u16*)(ws + WS_Y);
  u16* oh = (u16*)(ws + WS_OH);
  const int g = lane >> 4, r16 = lane & 15;
  const int h = b & 15;
  u32x4 zv[2];
  float bb[2];
  STAMP(2, 0);
  const int kq = wave >> 1, wn = (wave & 1) ^ (wave >> 2);
  const u16* bp = z2n + ((size_t)(nt_ * 64 + kq) * 7 + 4 * wn) * 512 + lane * 8;
  const int arow = r16 * 256 + (((4 * kq + g) ^ r16) << 4);
  const u16* src0;
  const u16* src1;
  {
    const int row = 4 * wave + (lane >> 4);
    src0 = wsp + (size_t)(t0 + row) * S_ + (((lane & 15) ^ (row & 15)) << 3);
    src1 = src0 + (size_t)32 * S_;
  }
  const unsigned ldsb = __builtin_amdgcn_readfirstlane((unsigned)(size_t)(LDS_AS char*)sm) + wave * 1024;
#define K3_AISSUE(T)                                                                            \
  do {                                                                                          \
    glds16_asm(src0 + (T)*128, ldsb + ((T)&7) * K3_STAGE);                                      \
    glds16_asm(src1 + (T)*128, ldsb + ((T)&7) * K3_STAGE + 8192);                               \
  } while (0)
  f32x4 acc[4][4];
  bf16x8 aA[4], aB[4], b0[4], b1[4], b2[4], b3[4];
#define K3_BLOAD(BF, T, NT)                                                                     \
  do { _Pragma("unroll") for (int n = 0; n < NT; ++n) BF[n] = *(const bf16x8*)(bp + ((T)*28 + n) * 512); } while (0)
#define K3_AREAD(AF, T)                                                                         \
  do { _Pragma("unroll") for (int m = 0; m < 4; ++m) AF[m] = *(const bf16x8*)(sm + ((T)&7) * K3_STAGE + arow + m * 4096); } while (0)
#define K3_MMA(AF, BF, NT)                                                                      \
  do {                                                                                          \
    _Pragma("unroll") for (int n = 0; n < NT; ++n)                                              \
      _Pragma("unroll") for (int m = 0; m < 4; ++m) acc[n][m] = mfma16(BF[n], AF[m], acc[n][m]); \
  } while (0)
#define K3_STEP(T, AC, AN, BC, BL, NT)                                                          \
  do {                                                                                          \
    asm volatile("s_waitcnt vmcnt(12)" ::: "memory");                                           \
    BAR();                                                                                      \
    STAMP(2, 5 + (T));                                                                          \
    if ((T) + 8 < 16) K3_AISSUE((T) + 8);                                                       \
    if ((T) + 1 < 16) K3_AREAD(AN, (T) + 1);                                                    \
    if ((T) + 3 < 16) K3_BLOAD(BL, (T) + 3, NT);                                                \
    K3_MMA(AC, BC, NT);                                                                         \
    SGB(0x100, 4);                                                                              \
    _Pragma("unroll") for (int i_ = 0; i_ < NT; ++i_) { SGB(0x008, 2); SGB(0x020, 1); }         \
  } while (0)
#define K3_LOOP(NT) \
  do { \
    STAMP(2, 4); \
    K3_AREAD(aA, 0); \
    K3_STEP(0, aA, aB, b0, b3, NT); \
    K3_STEP(1, aB, aA, b1, b0, NT); \
    K3_STEP(2, aA, aB, b2, b1, NT); \
    K3_STEP(3, aB, aA, b3, b2, NT); \
    K3_STEP(4, aA, aB, b0, b3, NT); \
    K3_STEP(5, aB, aA, b1, b0, NT); \
    K3_STEP(6, aA, aB, b2, b1, NT); \
    K3_STEP(7, aB, aA, b3, b2, NT); \
    K3_STEP(8, aA, aB, b0, b3, NT); \
    K3_STEP(9, aB, aA, b1, b0, NT); \
    K3_STEP(10, aA, aB, b2, b1, NT); \
    K3_STEP(11, aB, aA, b3, b2, NT); \
    K3_STEP(12, aA, aB, b0, b3, NT); \
    _Pragma("unroll") for (int i = 0; i < 2; ++i) {                                             \
      int id = tid + 512 * i;                                                                   \
      if (id >= 896) id = tid;                                                                  \
      const int t = id / 14, c = id % 14;                                                       \
      zv[i] = *(const u32x4*)(Y + (size_t)(t0 + t) * YLD + 1344 + f0 + c * 8);                  \
      bb[i] = bf2f(bsp[t0 + t]);                                                                \
    }                                                                                           \
    K3_STEP(13, aB, aA, b1, b0, NT); \
    {                                                                                           \
      const int te = (tid < 448) ? tid : 0;                                                     \
      const f32x4* kvp = (const f32x4*)(ws + WS_KVP) + (size_t)h * 448 + te;                    \
      _Pragma("unroll") for (int p = 0; p < 8; ++p) part[p] = kvp[(size_t)p * 16 * 448];        \
      const u32x4* pq = (const u32x4*)(ws + WS_PHQ) + ((size_t)(h * 128 + (b >> 4) * 8 + wave) * 2) * 64 + lane; \
      c1 = pq[0];                                                                               \
      c2 = pq[64];                                                                              \
    }                                                                                           \
    K3_STEP(14, aA, aB, b2, b1, NT); \
    K3_STEP(15, aB, aA, b3, b2, NT); \
  } while (0)
  K3_AISSUE(0);
  K3_AISSUE(1);
  if (wn == 0) { K3_BLOAD(b0, 0, 4); } else { K3_BLOAD(b0, 0, 3); }
  K3_AISSUE(2);
  if (wn == 0) { K3_BLOAD(b1, 1, 4); } else { K3_BLOAD(b1, 1, 3); }
  K3_AISSUE(3);
  if (wn == 0) { K3_BLOAD(b2, 2, 4); } else { K3_BLOAD(b2, 2, 3); }
  K3_AISSUE(4); K3_AISSUE(5); K3_AISSUE(6); K3_AISSUE(7);
  asm volatile("s_waitcnt vmcnt(20)" ::: "memory");
  STAMP(2, 1);
  BAR();
  f32x4 part[8];
  u32x4 c1, c2;
  SCHED();
#pragma unroll
  for (int n = 0; n < 4; ++n)
#pragma unroll
    for (int m = 0; m < 4; ++m) acc[n][m] = f32x4{0.f, 0.f, 0.f, 0.f};
  if (wn == 0) K3_LOOP(4);
  else K3_LOOP(3);
  {
    char* KT = sm + K3_AUX;
    if (tid < 448) {
      const f32x4 s = ((part[0] + part[1]) + (part[2] + part[3])) + ((part[4] + part[5]) + (part[6] + part[7]));
      const int fp = tid >> 3, d0 = 4 * (tid & 7);
      const int mf = (fp >= 28) ? 1 : 0, f = fp - 28 * mf;
      const int pos = mf * 32 + ((f & 15) >> 2) * 8 + (f & 3) + 4 * (f >> 4);
      const unsigned a = pk2(s[0], s[1]), c = pk2(s[2], s[3]);
      char* d = KT + d0 * 144 + pos * 2;
      *(u16*)(d) = (u16)a; *(u16*)(d + 144) = (u16)(a >> 16);
      *(u16*)(d + 288) = (u16)c; *(u16*)(d + 432) = (u16)(c >> 16);
    } else {
      const int i = tid - 448, d = i >> 1, mf = i & 1;
      *(u32x2*)(KT + d * 144 + (mf * 32 + 28) * 2) = u32x2{0u, 0u};
    }
  }
  BAR();
  STAMP(2, 21);
  {
    const char* KT = sm + K3_AUX;
    bf16x8 af[2][2];
#pragma unroll
    for (int dt = 0; dt < 2; ++dt)
#pragma unroll
      for (int mf = 0; mf < 2; ++mf) af[dt][mf] = *(const bf16x8*)(KT + (16 * dt + r16) * 144 + (32 * mf + 8 * g) * 2);
    f32x4 o0 = {0.f, 0.f, 0.f, 0.f}, o1 = {0.f, 0.f, 0.f, 0.f};
    o0 = mfma16(af[0][0], __builtin_bit_cast(bf16x8, c1), o0);
    o1 = mfma16(af[1][0], __builtin_bit_cast(bf16x8, c1), o1);
    o0 = mfma16(af[0][1], __builtin_bit_cast(bf16x8, c2), o0);
    o1 = mfma16(af[1][1], __builtin_bit_cast(bf16x8, c2), o1);
    float den = __shfl(o1[0], 48 + r16, 64);
    den = rbf(rbf(den) + 1e-6f);
    const float inv = 1.f / den;
    const int mtile = (b >> 4) * 8 + wave;
    {
      const int k = h * 28 + 4 * g;
      u16* dst = oh + ((size_t)(mtile * 42 + (k >> 5)) * 64 + ((k >> 3) & 3) * 16 + r16) * 8 + (k & 7);
      st8_wt(dst, u32x2{pk2(rbf(o0[0]) * inv, rbf(o0[1]) * inv), pk2(rbf(o0[2]) * inv, rbf(o0[3]) * inv)});
    }
    if (g != 3) {
      const int k = h * 28 + 16 + 4 * g;
      u16* dst = oh + ((size_t)(mtile * 42 + (k >> 5)) * 64 + ((k >> 3) & 3) * 16 + r16) * 8 + (k & 7);
      st8_wt(dst, u32x2{pk2(rbf(o1[0]) * inv, rbf(o1[1]) * inv), pk2(rbf(o1[2]) * inv, rbf(o1[3]) * inv)});
    }
  }
  {
    char* rd = sm + kq * (64 * K3_RS) + r16 * K3_RS + (64 * wn + 4 * g) * 4;
#pragma unroll
    for (int n = 0; n < 4; ++n)
      if (n < 3 || wn == 0) {
#pragma unroll
        for (int m = 0; m < 4; ++m) *(f32x4*)(rd + 16 * m * K3_RS + 64 * n) = acc[n][m];
      }
  }
  BAR();
#pragma unroll
  for (int i = 0; i < 2; ++i) {
    const int id = tid + 512 * i;
    if (id < 896) {
      const int t = id / 14, c = id % 14;
      const char* p = sm + t * K3_RS + c * 32;
      f32x4 v0 = (*(const f32x4*)(p) + *(const f32x4*)(p + 64 * K3_RS)) +
                 (*(const f32x4*)(p + 128 * K3_RS) + *(const f32x4*)(p + 192 * K3_RS));
      f32x4 v1 = (*(const f32x4*)(p + 16) + *(const f32x4*)(p + 64 * K3_RS + 16)) +
                 (*(const f32x4*)(p + 128 * K3_RS + 16) + *(const f32x4*)(p + 192 * K3_RS + 16));
      u32x4 z;
#pragma unroll
      for (int j = 0; j < 4; ++j) z[j] = gelu_pk(zv[i][j]);
      u32x4 o;
      o[0] = pk2(lo_f(z[0]) * rbf(rbf(v0[0]) + bb[i]), hi_f(z[0]) * rbf(rbf(v0[1]) + bb[i]));
      o[1] = pk2(lo_f(z[1]) * rbf(rbf(v0[2]) + bb[i]), hi_f(z[1]) * rbf(rbf(v0[3]) + bb[i]));
      o[2] = pk2(lo_f(z[2]) * rbf(rbf(v1[0]) + bb[i]), hi_f(z[2]) * rbf(rbf(v1[1]) + bb[i]));
      o[3] = pk2(lo_f(z[3]) * rbf(rbf(v1[2]) + bb[i]), hi_f(z[3]) * rbf(rbf(v1[3]) + bb[i]));
      const int k = 448 + f0 + c * 8, tt = t0 + t;
      st16_wt(oh + ((size_t)((tt >> 4) * 42 + (k >> 5)) * 64 + ((k >> 3) & 3) * 16 + (tt & 15)) * 8, o);
    }
  }
  STAMP(2, 31);
}

__global__ __launch_bounds__(384) void k4_kernel(const u16* __restrict__ x, const char* __restrict__ ws,
                                                  u16* __restrict__ out) {
  __shared__ __attribute__((aligned(16))) char sm[6 * 32 * K3_RS];
  const int tid = threadIdx.x, lane = tid & 63, wave = __builtin_amdgcn_readfirstlane(tid >> 6);
  const int b = blockIdx.x;
  const int xcd = b & 7, idx = b >> 3;
  const int mt_ = xcd * 8 + (idx & 7), nt_ = idx >> 3;
  const int t0 = mt_ * 32, n0 = nt_ * 112;
  const int g = lane >> 4, r16 = lane & 15;
  STAMP(3, 0);
  const u16* ap = (const u16*)(ws + WS_OH) + ((size_t)(mt_ * 2 * 42 + wave * 7) * 64 + lane) * 8;
  const u16* bp = (const u16*)(ws + WS_WT) + ((size_t)(nt_ * 7 * 42 + wave * 7) * 64 + lane) * 8;
  bf16x8 a0[2], b0[7], a1[2], b1[7], a2[2], b2[7], a3[2], b3[7];
#define K4_LOAD(AF, BF, KS)                                                                     \
  do {                                                                                          \
    _Pragma("unroll") for (int m = 0; m < 2; ++m) AF[m] = *(const bf16x8*)(ap + (m * 42 + (KS)) * 512); \
    _Pragma("unroll") for (int n = 0; n < 7; ++n) BF[n] = *(const bf16x8*)(bp + (n * 42 + (KS)) * 512); \
  } while (0)
  K4_LOAD(a0, b0, 0);
  K4_LOAD(a1, b1, 1);
  K4_LOAD(a2, b2, 2);
  K4_LOAD(a3, b3, 3);
  u32x4 xv[2];
#pragma unroll
  for (int i = 0; i < 2; ++i) {
    int id = tid + 384 * i;
    if (id >= 448) id = tid;
    xv[i] = *(const u32x4*)(x + (size_t)(t0 + id / 14) * D_ + n0 + (id % 14) * 8);
  }
  SCHED();
  f32x4 acc[7][2];
#pragma unroll
  for (int n = 0; n < 7; ++n) { acc[n][0] = f32x4{0.f, 0.f, 0.f, 0.f}; acc[n][1] = f32x4{0.f, 0.f, 0.f, 0.f}; }
#define K4_MMA(AF, BF)                                                                          \
  do {                                                                                          \
    _Pragma("unroll") for (int n = 0; n < 7; ++n) {                                             \
      acc[n][0] = mfma16(BF[n], AF[0], acc[n][0]);                                              \
      acc[n][1] = mfma16(BF[n], AF[1], acc[n][1]);                                              \
    }                                                                                           \
    SCHED();                                                                                    \
  } while (0)
  K4_MMA(a0, b0); K4_LOAD(a0, b0, 4); SCHED();
  K4_MMA(a1, b1); K4_LOAD(a1, b1, 5); SCHED();
  K4_MMA(a2, b2); K4_LOAD(a2, b2, 6); SCHED();
  K4_MMA(a3, b3);
  K4_MMA(a0, b0);
  K4_MMA(a1, b1);
  K4_MMA(a2, b2);
  STAMP(3, 1);
  char* rd = sm + wave * (32 * K3_RS) + r16 * K3_RS + 16 * g;
#pragma unroll
  for (int n = 0; n < 7; ++n)
#pragma unroll
    for (int m = 0; m < 2; ++m) *(f32x4*)(rd + 16 * m * K3_RS + 64 * n) = acc[n][m];
  __syncthreads();
#pragma unroll
  for (int i = 0; i < 2; ++i) {
    const int id = tid + 384 * i;
    if (id < 448) {
      const int et = id / 14, ec = id % 14;
      const char* p = sm + et * K3_RS + ec * 32;
      f32x4 v0 = {0.f, 0.f, 0.f, 0.f}, v1 = {0.f, 0.f, 0.f, 0.f};
#pragma unroll
      for (int w = 0; w < 6; ++w) {
        v0 += *(const f32x4*)(p + w * 32 * K3_RS);
        v1 += *(const f32x4*)(p + w * 32 * K3_RS + 16);
      }
      u32x4 o;
      o[0] = pk2(lo_f(xv[i][0]) + v0[0], hi_f(xv[i][0]) + v0[1]);
      o[1] = pk2(lo_f(xv[i][1]) + v0[2], hi_f(xv[i][1]) + v0[3]);
      o[2] = pk2(lo_f(xv[i][2]) + v1[0], hi_f(xv[i][2]) + v1[1]);
      o[3] = pk2(lo_f(xv[i][3]) + v1[2], hi_f(xv[i][3]) + v1[3]);
      *(u32x4*)(out + (size_t)(t0 + et) * D_ + n0 + ec * 8) = o;
    }
  }
  STAMP(3, 31);
}

extern "C" void kernel_launch(void* const* d_in, const int* in_sizes, int n_in, void* d_out, int out_size,
                              void* d_ws, size_t ws_size, hipStream_t stream) {
  const u16* x = (const u16*)d_in[0];
  const u16* wqkv = (const u16*)d_in[1];
  const u16* wphi = (const u16*)d_in[2];
  const u16* wo = (const u16*)d_in[3];
  const u16* win = (const u16*)d_in[5];
  const u16* wsp = (const u16*)d_in[6];
  const u16* bsp = (const u16*)d_in[7];
  const u16* wout = (const u16*)d_in[8];
  char* ws = (char*)d_ws;
  k1_kernel<<<245, 768, 0, stream>>>(x, wqkv, wo, win, wout, wsp, ws);
  k2_kernel<<<384, 512, 0, stream>>>(wphi, wsp, ws);
  k3_kernel<<<256, 512, 0, stream>>>(wsp, bsp, wphi, ws);
  k4_kernel<<<256, 384, 0, stream>>>(x, ws, (u16*)d_out);
}
```

```cpp
#include <hip/hip_runtime.h>
#include <hip/hip_bf16.h>
#include <stdint.h>

typedef __attribute__((ext_vector_type(8))) short bf16x8;
typedef __attribute__((ext_vector_type(4))) short s16x4;
typedef __attribute__((ext_vector_type(4))) float f32x4;
typedef __attribute__((ext_vector_type(2))) float f32x2;
typedef __attribute__((ext_vector_type(4))) unsigned u32x4;
typedef __attribute__((ext_vector_type(2))) unsigned u32x2;
typedef __attribute__((ext_vector_type(2))) __bf16 bf2_t;
typedef unsigned short u16;
#define LDS_AS __attribute__((address_space(3)))

constexpr int S_ = 2048, D_ = 448, NQKV = 1344, NIN = 1792, FH = 896, KO = 1344;

__device__ __forceinline__ unsigned pk2(float a, float b) {
  f32x2 v = {a, b};
  return __builtin_bit_cast(unsigned, __builtin_convertvector(v, bf2_t));
}
__device__ __forceinline__ float lo_f(unsigned u) { return __uint_as_float(u << 16); }
__device__ __forceinline__ float hi_f(unsigned u) { return __uint_as_float(u & 0xffff0000u); }
__device__ __forceinline__ float rbf(float a) { return lo_f(pk2(a, 0.f)); }
__device__ __forceinline__ float bf2f(u16 v) { return __uint_as_float(((unsigned)v) << 16); }
__device__ __forceinline__ float dot2(unsigned a, unsigned b, float c) {
  return __builtin_amdgcn_fdot2_f32_bf16(__builtin_bit_cast(bf2_t, a), __builtin_bit_cast(bf2_t, b), c, false);
}

__device__ __forceinline__ float x16_max(float v) {
  auto r = __builtin_amdgcn_permlane16_swap(__float_as_uint(v), __float_as_uint(v), false, false);
  return fmaxf(__uint_as_float(r[0]), __uint_as_float(r[1]));
}
__device__ __forceinline__ float x32_max(float v) {
  auto r = __builtin_amdgcn_permlane32_swap(__float_as_uint(v), __float_as_uint(v), false, false);
  return fmaxf(__uint_as_float(r[0]), __uint_as_float(r[1]));
}
__device__ __forceinline__ float x16_min(float v) {
  auto r = __builtin_amdgcn_permlane16_swap(__float_as_uint(v), __float_as_uint(v), false, false);
  return fminf(__uint_as_float(r[0]), __uint_as_float(r[1]));
}
__device__ __forceinline__ float x32_min(float v) {
  auto r = __builtin_amdgcn_permlane32_swap(__float_as_uint(v), __float_as_uint(v), false, false);
  return fminf(__uint_as_float(r[0]), __uint_as_float(r[1]));
}
__device__ __forceinline__ float x16_add(float v) {
  auto r = __builtin_amdgcn_permlane16_swap(__float_as_uint(v), __float_as_uint(v), false, false);
  return __uint_as_float(r[0]) + __uint_as_float(r[1]);
}
__device__ __forceinline__ float x32_add(float v) {
  auto r = __builtin_amdgcn_permlane32_swap(__float_as_uint(v), __float_as_uint(v), false, false);
  return __uint_as_float(r[0]) + __uint_as_float(r[1]);
}
__device__ __forceinline__ float red8_add(float v) {
  v += __uint_as_float(__builtin_amdgcn_mov_dpp(__float_as_uint(v), 0xB1, 0xf, 0xf, true));
  v += __uint_as_float(__builtin_amdgcn_mov_dpp(__float_as_uint(v), 0x4E, 0xf, 0xf, true));
  v += __uint_as_float(__builtin_amdgcn_mov_dpp(__float_as_uint(v), 0x141, 0xf, 0xf, true));
  return v;
}
__device__ __forceinline__ f32x4 mfma16(bf16x8 a, bf16x8 b, f32x4 c) {
  return __builtin_amdgcn_mfma_f32_16x16x32_bf16(a, b, c, 0, 0, 0);
}
__device__ __forceinline__ unsigned gelu_pk(unsigned p) {
  const f32x2 u = {lo_f(p), hi_f(p)};
  const f32x2 c1 = {-2.3022081986f, -2.3022081986f};
  const f32x2 c2 = {-0.1029432397f, -0.1029432397f};
  f32x2 t = u * u;
  f32x2 a = u * (t * c2 + c1);
  f32x2 d = {1.f + __builtin_amdgcn_exp2f(a[0]), 1.f + __builtin_amdgcn_exp2f(a[1])};
  f32x2 r = {__builtin_amdgcn_rcpf(d[0]), __builtin_amdgcn_rcpf(d[1])};
  f32x2 o = u * r;
  return pk2(o[0], o[1]);
}
#define SCHED() __builtin_amdgcn_sched_barrier(0)
#define BAR() do { SCHED(); asm volatile("" ::: "memory"); __builtin_amdgcn_s_waitcnt(0xC07F); __builtin_amdgcn_s_barrier(); asm volatile("" ::: "memory"); SCHED(); } while (0)
__device__ __forceinline__ void glds16(const void* g, char* l) {
  __builtin_amdgcn_global_load_lds((const unsigned*)g, (LDS_AS unsigned*)l, 16, 0, 0);
}
__device__ __forceinline__ bf16x8 tr_frag(const char* p0, const char* p1) {
  s16x4 a = __builtin_amdgcn_ds_read_tr16_b64_v4i16((LDS_AS s16x4*)p0);
  s16x4 c = __builtin_amdgcn_ds_read_tr16_b64_v4i16((LDS_AS s16x4*)p1);
  return __builtin_shufflevector(a, c, 0, 1, 2, 3, 4, 5, 6, 7);
}

constexpr int YLD = 3136;
constexpr size_t WS_Y = 0;
constexpr size_t WS_Z2N = WS_Y + (size_t)S_ * YLD * 2 + 256;
constexpr size_t WS_OH = WS_Z2N + (size_t)S_ * 896 * 2;
constexpr size_t WS_WT = WS_OH + (size_t)S_ * KO * 2;
constexpr size_t WS_KVP = WS_WT + (size_t)D_ * KO * 2;
constexpr size_t WS_PHQ = WS_KVP + (size_t)8 * 16 * 56 * 32 * 4;
constexpr size_t WS_END = WS_PHQ + (size_t)16 * 128 * 2048;

__device__ __forceinline__ void st16_wt(void* p, u32x4 v) {
  asm volatile("global_store_dwordx4 %0, %1, off sc0 sc1\n\ts_nop 1" ::"v"(p), "v"(v) : "memory");
}
__device__ __forceinline__ void st8_wt(void* p, u32x2 v) {
  asm volatile("global_store_dwordx2 %0, %1, off sc0 sc1\n\ts_nop 1" ::"v"(p), "v"(v) : "memory");
}
#define SGB(mask, n) __builtin_amdgcn_sched_group_barrier(mask, n, 0)

__device__ __forceinline__ void phi_map(bf16x8 qf, bf16x8 wp0, bf16x8 wp1, int g, unsigned (&pp)[2][2],
                                        unsigned (&pm)[2][2]) {
  f32x4 z = {0.f, 0.f, 0.f, 0.f};
  f32x4 p0 = mfma16(wp0, qf, z);
  f32x4 p1 = mfma16(wp1, qf, z);
  float v[8];
  {
    unsigned a = pk2(p0[0], p0[1]), b = pk2(p0[2], p0[3]), c = pk2(p1[0], p1[1]), d = pk2(p1[2], p1[3]);
    v[0] = lo_f(a); v[1] = hi_f(a); v[2] = lo_f(b); v[3] = hi_f(b);
    v[4] = lo_f(c); v[5] = hi_f(c); v[6] = lo_f(d); v[7] = hi_f(d);
  }
  const bool valid1 = (g != 3);
  if (!valid1) { v[4] = v[0]; v[5] = v[0]; v[6] = v[0]; v[7] = v[0]; }
  float mx = fmaxf(fmaxf(fmaxf(v[0], v[1]), fmaxf(v[2], v[3])), fmaxf(fmaxf(v[4], v[5]), fmaxf(v[6], v[7])));
  float mn = fminf(fminf(fminf(v[0], v[1]), fminf(v[2], v[3])), fminf(fminf(v[4], v[5]), fminf(v[6], v[7])));
  mx = x32_max(x16_max(mx));
  mn = x32_min(x16_min(mn));
  const float L2E = 1.4426950408889634f;
  const float mxl = mx * L2E, mnl = mn * L2E;
  float ep[8], em[8];
  float sp = 0.f, sm = 0.f;
#pragma unroll
  for (int j = 0; j < 8; ++j) {
    ep[j] = __builtin_amdgcn_exp2f(__builtin_fmaf(v[j], L2E, -mxl));
    em[j] = __builtin_amdgcn_exp2f(__builtin_fmaf(v[j], -L2E, mnl));
    if (j >= 4) { ep[j] = valid1 ? ep[j] : 0.f; em[j] = valid1 ? em[j] : 0.f; }
    sp += ep[j];
    sm += em[j];
  }
  sp = x32_add(x16_add(sp));
  sm = x32_add(x16_add(sm));
  const float rp = __builtin_amdgcn_rcpf(sp), rm = __builtin_amdgcn_rcpf(sm);
  pp[0][0] = pk2(ep[0] * rp, ep[1] * rp); pp[0][1] = pk2(ep[2] * rp, ep[3] * rp);
  pp[1][0] = pk2(ep[4] * rp, ep[5] * rp); pp[1][1] = pk2(ep[6] * rp, ep[7] * rp);
  pm[0][0] = pk2(em[0] * rm, em[1] * rm); pm[0][1] = pk2(em[2] * rm, em[3] * rm);
  pm[1][0] = pk2(em[4] * rm, em[5] * rm); pm[1][1] = pk2(em[6] * rm, em[7] * rm);
}

constexpr int K1_XST = 128 * 128;
constexpr int K1_STAGE = K1_XST + 64 * 448;
constexpr int K1_LDS = 3 * K1_STAGE;

__global__ __launch_bounds__(768) void k1_kernel(const u16* __restrict__ x, const u16* __restrict__ wqkv,
                                                  const u16* __restrict__ wo, const u16* __restrict__ win,
                                                  const u16* __restrict__ wout, const u16* __restrict__ wsp,
                                                  char* __restrict__ ws) {
  __shared__ __attribute__((aligned(16))) char sm[K1_LDS];
  const int tid = threadIdx.x, lane = tid & 63, wave = __builtin_amdgcn_readfirstlane(tid >> 6);
  const int b = blockIdx.x;
  if (b >= 224) {
    const int kb = b - 224;
    const int k0 = kb * 64;
    if (kb < 21) {
    u16* T = (u16*)sm;
    constexpr int TS = 456;
    u32x4 v[5];
#pragma unroll
    for (int j = 0; j < 5; ++j) {
      int i = tid + 768 * j;
      if (i >= 64 * 56) i = tid;
      const int r = i / 56, c = i % 56;
      const int k = k0 + r;
      const u16* src = (k < 448) ? (wo + (size_t)k * 448) : (wout + (size_t)(k - 448) * 448);
      v[j] = *(const u32x4*)(src + c * 8);
    }
#pragma unroll
    for (int j = 0; j < 5; ++j) {
      const int i = tid + 768 * j;
      if (i < 64 * 56) *(u32x4*)(T + (i / 56) * TS + (i % 56) * 8) = v[j];
    }
    __syncthreads();
    u16* wT = (u16*)(ws + WS_WT);
    for (int i = tid; i < 448 * 8; i += 768) {
      const int n = i >> 3, c = i & 7;
      u16 t[8];
#pragma unroll
      for (int j = 0; j < 8; ++j) t[j] = T[(c * 8 + j) * TS + n];
      u32x4 o;
#pragma unroll
      for (int j = 0; j < 4; ++j) o[j] = (unsigned)t[2 * j] | ((unsigned)t[2 * j + 1] << 16);
      st16_wt(wT + ((size_t)((n >> 4) * 42 + (k0 >> 5) + (c >> 2)) * 64 + (c & 3) * 16 + (n & 15)) * 8, o);
    }
    }
    return;
  }

  const int xcd = b & 7, idx = b >> 3;
  const int cb = (xcd & 1) * 7 + idx % 7;
  const int rt = (xcd >> 1) * 4 + idx / 7;
  const int row0 = rt * 128;
  const int g = lane >> 4, r16 = lane & 15;
  const int wm = (wave >> 1) & 3, wn = wave & 1;
  constexpr int RST = 464;

  if (wave >= 8) {
    const int lw = wave - 8;
    const u16* W = (cb < 6) ? (wqkv + 224 * cb) : (win + 224 * (cb - 6));
    const int ldw = (cb < 6) ? NQKV : NIN;
    const u16* src[11];
#pragma unroll
    for (int j = 0; j < 11; ++j) {
      const int i = lw + 4 * j;
      if (j < 4) {
        const int row = 8 * i + (lane >> 3);
        src[j] = x + (size_t)(row0 + row) * D_ + (((lane & 7) ^ ((row >> 1) & 7)) << 3);
      } else {
        const int o = (i - 16) * 1024 + lane * 16;
        const int k = o / 448, cp = (o % 448) >> 4;
        const int G = (cp >> 1) ^ ((k >> 3) & 1);
        src[j] = W + (size_t)k * ldw + 8 * (2 * G + (cp & 1));
      }
    }
    const int wstep = 64 * ldw;
    char* dst0 = sm + lw * 1024;
#pragma unroll
    for (int s = 0; s < 2; ++s)
#pragma unroll
      for (int j = 0; j < 11; ++j) glds16(src[j] + s * (j < 4 ? 64 : wstep), dst0 + s * K1_STAGE + j * 4096);
    int nb = 2;
#pragma unroll 1
    for (int t = 0; t < 7; ++t) {
      if (t < 6) asm volatile("s_waitcnt vmcnt(11)" ::: "memory");
      else asm volatile("s_waitcnt vmcnt(0)" ::: "memory");
      __builtin_amdgcn_s_barrier();
      if (t + 2 < 7) {
        char* dst = dst0 + nb * K1_STAGE;
#pragma unroll
        for (int j = 0; j < 11; ++j) glds16(src[j] + (t + 2) * (j < 4 ? 64 : wstep), dst + j * 4096);
        nb = (nb == 2) ? 0 : nb + 1;
      }
    }
    __builtin_amdgcn_s_barrier();
    __builtin_amdgcn_s_barrier();
  } else {
    f32x4 acc[7][2], sacc = {0.f, 0.f, 0.f, 0.f}, qacc = {0.f, 0.f, 0.f, 0.f}, cacc[2];
#pragma unroll
    for (int nt = 0; nt < 7; ++nt) { acc[nt][0] = f32x4{0.f, 0.f, 0.f, 0.f}; acc[nt][1] = f32x4{0.f, 0.f, 0.f, 0.f}; }
    cacc[0] = f32x4{0.f, 0.f, 0.f, 0.f}; cacc[1] = f32x4{0.f, 0.f, 0.f, 0.f};
    const bf16x8 ones = {0x3F80, 0x3F80, 0x3F80, 0x3F80, 0x3F80, 0x3F80, 0x3F80, 0x3F80};
    const int sw = (r16 >> 1) & 7;
    const int xrow = (32 * wm + 16 * wn + r16) * 128, xd = wn ? -2048 : 2048;
    const int xo0 = xrow + ((g ^ sw) << 4), xo1 = xrow + (((4 | g) ^ sw) << 4);
    int woff[7];
#pragma unroll
    for (int i = 0; i < 7; ++i) {
      int nt = i + 2 * wm;
      nt = (nt >= 7) ? nt - 7 : nt;
      const int G = 7 * wn + nt;
      woff[i] = K1_XST + (8 * g + ((lane >> 2) & 3)) * 448 + (lane & 3) * 8 + ((G ^ (g & 1)) << 5);
    }
    bf16x8 xa[2], wa[7], xb[2], wb[7];
#define K1_READ(XF, WF, SB, KS)                                                              \
  do {                                                                                       \
    XF[0] = *(const bf16x8*)((SB) + ((KS) ? xo1 : xo0));                                     \
    XF[1] = *(const bf16x8*)((SB) + ((KS) ? xo1 : xo0) + xd);                                \
    _Pragma("unroll") for (int nt = 0; nt < 7; ++nt) {                                       \
      const char* p_ = (SB) + woff[nt] + (KS)*32 * 448;                                      \
      WF[nt] = tr_frag(p_, p_ + 4 * 448);                                                    \
    }                                                                                        \
  } while (0)
#define K1_MMA(XF, WF)                                                                       \
  do {                                                                                       \
    _Pragma("unroll") for (int nt = 0; nt < 7; ++nt) acc[nt][0] = mfma16(WF[nt], XF[0], acc[nt][0]); \
    sacc = mfma16(ones, XF[0], sacc);                                                        \
    qacc = mfma16(XF[0], XF[0], qacc);                                                       \
    _Pragma("unroll") for (int nt = 0; nt < 7; ++nt) acc[nt][1] = mfma16(WF[nt], XF[1], acc[nt][1]); \
    cacc[0] = mfma16(WF[0], ones, cacc[0]);                                                  \
    cacc[1] = mfma16(WF[1], ones, cacc[1]);                                                  \
  } while (0)
    int rb = 0;
#pragma unroll 1
    for (int t = 0; t < 7; ++t) {
      BAR();
      const char* sb = sm + rb * K1_STAGE;
      rb = (rb == 2) ? 0 : rb + 1;
      K1_READ(xa, wa, sb, 0);
      SCHED();
      K1_READ(xb, wb, sb, 1);
      K1_MMA(xa, wa);
      _Pragma("unroll") for (int i_ = 0; i_ < 8; ++i_) { SGB(0x008, 1); SGB(0x100, 2); }
      SCHED();
      K1_MMA(xb, wb);
    }
    BAR();
    float* CS = (float*)(sm + 128 * RST);
    f32x2* RS_ = (f32x2*)(CS + 224);
    if (r16 == 0) {
#pragma unroll
      for (int i = 0; i < 2; ++i) {
        int nt = i + 2 * wm;
        nt = (nt >= 7) ? nt - 7 : nt;
        *(f32x4*)(CS + 112 * wn + 16 * nt + 4 * g) = cacc[i];
      }
    }
    {
      const float qd = (r16 & 3) == 0 ? qacc[0] : (r16 & 3) == 1 ? qacc[1] : (r16 & 3) == 2 ? qacc[2] : qacc[3];
      if (g == (r16 >> 2)) {
        const float mean = sacc[0] * (1.f / 448.f);
        const float var = fmaxf(qd * (1.f / 448.f) - mean * mean, 0.f);
        const float sd = rbf(sqrtf(rbf(rbf(var) + 1e-5f)));
        const float r = 1.f / sd;
        RS_[32 * wm + 16 * wn + r16] = f32x2{r, r * rbf(mean)};
      }
    }
    BAR();
    f32x2 st[2];
    st[0] = RS_[32 * wm + 16 * wn + r16];
    st[1] = RS_[32 * wm + 16 * (wn ^ 1) + r16];
#pragma unroll
    for (int i = 0; i < 7; ++i) {
      int nt = i + 2 * wm;
      nt = (nt >= 7) ? nt - 7 : nt;
      const f32x4 c4 = *(const f32x4*)(CS + 112 * wn + 16 * nt + 4 * g);
#pragma unroll
      for (int j = 0; j < 2; ++j) {
        char* dst = sm + (32 * wm + 16 * (j ^ wn) + r16) * RST + (112 * wn + 4 * g) * 2 + 32 * nt;
        const f32x4 v = acc[i][j] * st[j][0] - c4 * st[j][1];
        *(u32x2*)(dst) = u32x2{pk2(v[0], v[1]), pk2(v[2], v[3])};
      }
    }
  }
  __syncthreads();
  u16* dstb = (u16*)(ws + WS_Y) + (size_t)row0 * YLD + 224 * cb;
#pragma unroll
  for (int i = 0; i < 5; ++i) {
    const int id = tid + 768 * i;
    if (id < 128 * 28) {
      const int s = id / 28, c = id % 28;
      st16_wt(dstb + (size_t)s * YLD + c * 8, *(const u32x4*)(sm + s * RST + c * 16));
    }
  }
}

__device__ __forceinline__ float red64_add(float v) {
  v = red8_add(v);
  v += __uint_as_float(__builtin_amdgcn_mov_dpp(__float_as_uint(v), 0x140, 0xf, 0xf, true));
  return x32_add(x16_add(v));
}

constexpr int K2_PW = 64 * 80 + 32 * 64;
__global__ __launch_bounds__(512) void k2_kernel(const u16* __restrict__ wphi, const u16* __restrict__ wsp,
                                                  char* __restrict__ ws) {
  __shared__ __attribute__((aligned(16))) char sm[8 * 56 * 32 * 4 + 2048];
  const int tid = threadIdx.x, lane = tid & 63, wave = __builtin_amdgcn_readfirstlane(tid >> 6);
  const int b = blockIdx.x;
  const u16* Y = (const u16*)(ws + WS_Y);
  if (b < 128) {
    const int h = b & 15, rg = b >> 4;
    const int g = lane >> 4, r16 = lane & 15;
    const int rowb = rg * 256 + wave * 32;
    u32x2 kv_[2][2];
#pragma unroll
    for (int st = 0; st < 2; ++st) {
      const u16* kp = Y + (size_t)(rowb + st * 16 + r16) * YLD + 448 + h * 28 + 8 * g;
      kv_[st][0] = *(const u32x2*)(kp);
      kv_[st][1] = *(const u32x2*)(kp + 4);
    }
    u32x2 vv[4];
#pragma unroll
    for (int i = 0; i < 4; ++i) {
      const int row = (lane >> 3) + 8 * i, pc = lane & 7;
      vv[i] = *(const u32x2*)(Y + (size_t)(rowb + row) * YLD + 896 + h * 28 + 4 * (pc < 7 ? pc : 0));
      if (pc == 7) vv[i] = u32x2{0x00003F80u, 0u};
    }
    u32x4 wraw = {0u, 0u, 0u, 0u};
    if (tid < 98) wraw = *(const u32x4*)(wphi + (size_t)h * 784 + tid * 8);
    char* WP = sm + 8 * 56 * 32 * 4;
    if (tid < 98) *(u32x4*)(WP + tid * 16) = wraw;
    char* PK = sm + wave * K2_PW;
    char* VS = PK + 64 * 80;
    __syncthreads();
    const char* wp = WP + (8 * g + ((lane >> 2) & 3)) * 56 + (lane & 3) * 8;
    bf16x8 wp0 = tr_frag(wp, wp + 4 * 56);
    bf16x8 wp1 = tr_frag(wp + 32, wp + 4 * 56 + 32);
    if (g == 3) {
#pragma unroll
      for (int j = 4; j < 8; ++j) { wp0[j] = 0; wp1[j] = 0; }
    }
    f32x4 kacc[4][2];
#pragma unroll
    for (int a = 0; a < 4; ++a) { kacc[a][0] = f32x4{0.f, 0.f, 0.f, 0.f}; kacc[a][1] = f32x4{0.f, 0.f, 0.f, 0.f}; }
#pragma unroll
    for (int pr = 0; pr < 1; ++pr) {
#pragma unroll
      for (int i = 0; i < 4; ++i) {
        const int row = (lane >> 3) + 8 * i;
        const int slot = 2 * (row & 15) + (row >> 4);
        *(u32x2*)(VS + slot * 64 + (lane & 7) * 8) = vv[4 * pr + i];
      }
      unsigned pp[2][2][2], pm[2][2][2];
#pragma unroll
      for (int mt = 0; mt < 2; ++mt) {
        u32x4 q4 = {kv_[2 * pr + mt][0][0], kv_[2 * pr + mt][0][1], kv_[2 * pr + mt][1][0], kv_[2 * pr + mt][1][1]};
        if (g == 3) { q4[2] = 0u; q4[3] = 0u; }
        phi_map(__builtin_bit_cast(bf16x8, q4), wp0, wp1, g, pp[mt], pm[mt]);
      }
#pragma unroll
      for (int ft = 0; ft < 2; ++ft) {
        if (ft == 0 || g != 3) {
          char* d0 = PK + (16 * ft + 4 * g) * 80 + r16 * 4;
#pragma unroll
          for (int j = 0; j < 2; ++j) {
            *(unsigned*)(d0 + (2 * j) * 80) = (pp[0][ft][j] & 0xffffu) | (pp[1][ft][j] << 16);
            *(unsigned*)(d0 + (2 * j + 1) * 80) = (pp[0][ft][j] >> 16) | (pp[1][ft][j] & 0xffff0000u);
            *(unsigned*)(d0 + (28 + 2 * j) * 80) = (pm[0][ft][j] & 0xffffu) | (pm[1][ft][j] << 16);
            *(unsigned*)(d0 + (28 + 2 * j + 1) * 80) = (pm[0][ft][j] >> 16) | (pm[1][ft][j] & 0xffff0000u);
          }
        }
      }
      __builtin_amdgcn_s_waitcnt(0xC07F);
      __builtin_amdgcn_wave_barrier();
      bf16x8 bfr[2];
      {
        const char* vp = VS + (8 * g + ((lane >> 2) & 3)) * 64 + (lane & 3) * 8;
        bfr[0] = tr_frag(vp, vp + 4 * 64);
        bfr[1] = tr_frag(vp + 32, vp + 4 * 64 + 32);
      }
#pragma unroll
      for (int a = 0; a < 4; ++a) {
        bf16x8 af = *(const bf16x8*)(PK + (16 * a + r16) * 80 + g * 16);
        kacc[a][0] = mfma16(af, bfr[0], kacc[a][0]);
        kacc[a][1] = mfma16(af, bfr[1], kacc[a][1]);
      }
      __builtin_amdgcn_s_waitcnt(0xC07F);
      __builtin_amdgcn_wave_barrier();
    }
    __syncthreads();
    float* red = (float*)sm + wave * (56 * 32);
#pragma unroll
    for (int a = 0; a < 4; ++a)
#pragma unroll
      for (int c = 0; c < 2; ++c)
#pragma unroll
        for (int j = 0; j < 4; ++j) {
          const int f = 16 * a + 4 * g + j;
          if (f < 56) red[f * 32 + 16 * c + r16] = kacc[a][c][j];
        }
    __syncthreads();
    if (tid < 448) {
      const f32x4* rp = (const f32x4*)sm + tid;
      f32x4 s = (rp[0] + rp[448]) + (rp[2 * 448] + rp[3 * 448]);
      s += (rp[4 * 448] + rp[5 * 448]) + (rp[6 * 448] + rp[7 * 448]);
      st16_wt((f32x4*)(ws + WS_KVP) + (size_t)(rg * 16 + h) * 448 + tid, __builtin_bit_cast(u32x4, s));
    }
  } else if (b >= 256) {
    const int h = b & 15, rg = (b - 256) >> 4;
    const int g = lane >> 4, r16 = lane & 15;
    u32x2 qv[2][2];
#pragma unroll
    for (int st = 0; st < 2; ++st) {
      const u16* qp = Y + (size_t)(rg * 256 + wave * 32 + st * 16 + r16) * YLD + h * 28 + 8 * g;
      qv[st][0] = *(const u32x2*)(qp);
      qv[st][1] = *(const u32x2*)(qp + 4);
    }
    u32x4 wraw = {0u, 0u, 0u, 0u};
    if (tid < 98) wraw = *(const u32x4*)(wphi + (size_t)h * 784 + tid * 8);
    if (tid < 98) *(u32x4*)(sm + tid * 16) = wraw;
    __syncthreads();
    const char* wp = sm + (8 * g + ((lane >> 2) & 3)) * 56 + (lane & 3) * 8;
    bf16x8 wp0 = tr_frag(wp, wp + 4 * 56);
    bf16x8 wp1 = tr_frag(wp + 32, wp + 4 * 56 + 32);
    if (g == 3) {
#pragma unroll
      for (int j = 4; j < 8; ++j) { wp0[j] = 0; wp1[j] = 0; }
    }
#pragma unroll
    for (int st = 0; st < 2; ++st) {
      u32x4 q4 = {qv[st][0][0], qv[st][0][1], qv[st][1][0], qv[st][1][1]};
      if (g == 3) { q4[2] = 0u; q4[3] = 0u; }
      unsigned pp[2][2], pm[2][2];
      phi_map(__builtin_bit_cast(bf16x8, q4), wp0, wp1, g, pp, pm);
      u32x4* dst = (u32x4*)(ws + WS_PHQ) + ((size_t)(h * 128 + rg * 16 + wave * 2 + st) * 2) * 64 + lane;
      st16_wt(dst, u32x4{pp[0][0], pp[0][1], pp[1][0], pp[1][1]});
      st16_wt(dst + 64, u32x4{pm[0][0], pm[0][1], pm[1][0], pm[1][1]});
    }
  } else {
    const int row0 = (b - 128) * 16 + wave * 2;
    u16* z2n = (u16*)(ws + WS_Z2N);
    const int le = (lane < 56) ? lane : 0;
    u32x4 v[2][2];
#pragma unroll
    for (int r = 0; r < 2; ++r)
#pragma unroll
      for (int c = 0; c < 2; ++c) v[r][c] = *(const u32x4*)(Y + (size_t)(row0 + r) * YLD + 2240 + (le + 56 * c) * 8);
#pragma unroll
    for (int r = 0; r < 2; ++r) {
      float s = 0.f;
#pragma unroll
      for (int c = 0; c < 2; ++c)
#pragma unroll
        for (int j = 0; j < 4; ++j) {
          v[r][c][j] = gelu_pk(v[r][c][j]);
          s = dot2(v[r][c][j], 0x3F803F80u, s);
        }
      if (lane >= 56) s = 0.f;
      const float mean = red64_add(s) * (1.f / 896.f);
      const float mb = rbf(mean);
      float q = 0.f;
#pragma unroll
      for (int c = 0; c < 2; ++c)
#pragma unroll
        for (int j = 0; j < 4; ++j) {
          const float a = lo_f(v[r][c][j]) - mean, e = hi_f(v[r][c][j]) - mean;
          q += a * a + e * e;
          v[r][c][j] = pk2(lo_f(v[r][c][j]) - mb, hi_f(v[r][c][j]) - mb);
        }
      if (lane >= 56) q = 0.f;
      const float var = red64_add(q) * (1.f / 896.f);
      const float sd = rbf(sqrtf(rbf(rbf(var) + 1e-5f)));
      const float inv = 1.f / sd;
      if (lane < 56) {
#pragma unroll
        for (int c = 0; c < 2; ++c) {
          u32x4 o;
#pragma unroll
          for (int j = 0; j < 4; ++j) o[j] = pk2(lo_f(v[r][c][j]) * inv, hi_f(v[r][c][j]) * inv);
          *(u32x4*)(sm + (wave * 2 + r) * 1808 + (lane + 56 * c) * 16) = o;
        }
      }
    }
    __syncthreads();
    {
      const int rb0 = (b - 128) * 16;
      const int kb = rb0 >> 5, half = (rb0 >> 4) & 1;
      const int g = lane >> 4;
#pragma unroll
      for (int i = 0; i < 4; ++i) {
        const int u = wave + 8 * i;
        if (u < 28) {
          const int T = 2 * u + (g >> 1), gp = g & 1;
          const char* p = sm + (8 * gp + ((lane >> 2) & 3)) * 1808 + T * 32 + (lane & 3) * 8;
          bf16x8 fr = tr_frag(p, p + 4 * 1808);
          const int pn = T / 7, n = T % 7;
          st16_wt(z2n + ((size_t)((pn * 64 + kb) * 7 + n) * 64 + (2 * half + gp) * 16 + (lane & 15)) * 8, __builtin_bit_cast(u32x4, fr));
        }
      }
    }
    {
      const int bb = b - 128;
      const char* p = (const char*)wsp + (size_t)((bb & 7) >> 1) * (2u << 20) + (size_t)((bb >> 3) * 8 + wave) * 16384 + lane * 16;
      unsigned a = 0;
#pragma unroll
      for (int i = 0; i < 16; ++i) {
        u32x4 v = *(const u32x4*)(p + i * 1024);
        a |= v[0] & v[1] & v[2] & v[3];
      }
      asm volatile("" ::"v"(a));
    }
  }
}

constexpr int K3_RS = 464;
constexpr int K3_STAGE = 64 * 256;
constexpr int K3_NB = 8;
constexpr int K3_AUX = K3_NB * K3_STAGE;
__device__ __forceinline__ void glds16_asm(const void* gsrc, unsigned lds_dst) {
  unsigned keep;
  asm volatile("s_mov_b32 %0, m0\n\ts_mov_b32 m0, %2\n\ts_nop 0\n\tglobal_load_lds_dwordx4 %1, off\n\ts_mov_b32 m0, %0"
               : "=&s"(keep) : "v"(gsrc), "s"(lds_dst) : "memory");
}
__global__ __launch_bounds__(512) void k3_kernel(const u16* __restrict__ wsp, const u16* __restrict__ bsp,
                                                  const u16* __restrict__ wphi, char* __restrict__ ws) {
  __shared__ __attribute__((aligned(16))) char sm[K3_NB * K3_STAGE + 32 * 144];
  const int tid = threadIdx.x, lane = tid & 63, wave = __builtin_amdgcn_readfirstlane(tid >> 6);
  const int b = blockIdx.x;
  const int xcd = b & 7, idx = b >> 3;
  const int mt_ = (xcd >> 1) * 8 + (idx & 7);
  const int nt_ = (xcd & 1) * 4 + (idx >> 3);
  const int t0 = mt_ * 64, f0 = nt_ * 112;
  const u16* z2n = (const u16*)(ws + WS_Z2N);
  const u16* Y = (const u16*)(ws + WS_Y);
  u16* oh = (u16*)(ws + WS_OH);
  const int g = lane >> 4, r16 = lane & 15;
  const int h = b & 15;
  u32x4 zv[2];
  float bb[2];
  const int kq = wave >> 1, wn = (wave & 1) ^ (wave >> 2);
  const u16* bp = z2n + ((size_t)(nt_ * 64 + kq) * 7 + 4 * wn) * 512 + lane * 8;
  const int arow = r16 * 256 + (((4 * kq + g) ^ r16) << 4);
  const u16* src0;
  const u16* src1;
  {
    const int row = 4 * wave + (lane >> 4);
    src0 = wsp + (size_t)(t0 + row) * S_ + (((lane & 15) ^ (row & 15)) << 3);
    src1 = src0 + (size_t)32 * S_;
  }
  const unsigned ldsb = __builtin_amdgcn_readfirstlane((unsigned)(size_t)(LDS_AS char*)sm) + wave * 1024;
#define K3_AISSUE(T)                                                                            \
  do {                                                                                          \
    glds16_asm(src0 + (T)*128, ldsb + ((T)&7) * K3_STAGE);                                      \
    glds16_asm(src1 + (T)*128, ldsb + ((T)&7) * K3_STAGE + 8192);                               \
  } while (0)
  f32x4 acc[4][4];
  bf16x8 aA[4], aB[4], b0[4], b1[4], b2[4], b3[4];
#define K3_BLOAD(BF, T, NT)                                                                     \
  do { _Pragma("unroll") for (int n = 0; n < NT; ++n) BF[n] = *(const bf16x8*)(bp + ((T)*28 + n) * 512); } while (0)
#define K3_AREAD(AF, T)                                                                         \
  do { _Pragma("unroll") for (int m = 0; m < 4; ++m) AF[m] = *(const bf16x8*)(sm + ((T)&7) * K3_STAGE + arow + m * 4096); } while (0)
#define K3_MMA(AF, BF, NT)                                                                      \
  do {                                                                                          \
    _Pragma("unroll") for (int n = 0; n < NT; ++n)                                              \
      _Pragma("unroll") for (int m = 0; m < 4; ++m) acc[n][m] = mfma16(BF[n], AF[m], acc[n][m]); \
  } while (0)
#define K3_STEP(T, AC, AN, BC, BL, NT)                                                          \
  do {                                                                                          \
    asm volatile("s_waitcnt vmcnt(12)" ::: "memory");                                           \
    BAR();                                                                                      \
    if ((T) + 8 < 16) K3_AISSUE((T) + 8);                                                       \
    if ((T) + 1 < 16) K3_AREAD(AN, (T) + 1);                                                    \
    if ((T) + 3 < 16) K3_BLOAD(BL, (T) + 3, NT);                                                \
    K3_MMA(AC, BC, NT);                                                                         \
    SGB(0x100, 4);                                                                              \
    _Pragma("unroll") for (int i_ = 0; i_ < NT; ++i_) { SGB(0x008, 2); SGB(0x020, 1); }         \
  } while (0)
#define K3_LOOP(NT) \
  do { \
    K3_AREAD(aA, 0); \
    K3_STEP(0, aA, aB, b0, b3, NT); \
    K3_STEP(1, aB, aA, b1, b0, NT); \
    K3_STEP(2, aA, aB, b2, b1, NT); \
    K3_STEP(3, aB, aA, b3, b2, NT); \
    K3_STEP(4, aA, aB, b0, b3, NT); \
    K3_STEP(5, aB, aA, b1, b0, NT); \
    K3_STEP(6, aA, aB, b2, b1, NT); \
    K3_STEP(7, aB, aA, b3, b2, NT); \
    K3_STEP(8, aA, aB, b0, b3, NT); \
    K3_STEP(9, aB, aA, b1, b0, NT); \
    K3_STEP(10, aA, aB, b2, b1, NT); \
    K3_STEP(11, aB, aA, b3, b2, NT); \
    K3_STEP(12, aA, aB, b0, b3, NT); \
    _Pragma("unroll") for (int i = 0; i < 2; ++i) {                                             \
      int id = tid + 512 * i;                                                                   \
      if (id >= 896) id = tid;                                                                  \
      const int t = id / 14, c = id % 14;                                                       \
      zv[i] = *(const u32x4*)(Y + (size_t)(t0 + t) * YLD + 1344 + f0 + c * 8);                  \
      bb[i] = bf2f(bsp[t0 + t]);                                                                \
    }                                                                                           \
    K3_STEP(13, aB, aA, b1, b0, NT); \
    {                                                                                           \
      const int te = (tid < 448) ? tid : 0;                                                     \
      const f32x4* kvp = (const f32x4*)(ws + WS_KVP) + (size_t)h * 448 + te;                    \
      _Pragma("unroll") for (int p = 0; p < 8; ++p) part[p] = kvp[(size_t)p * 16 * 448];        \
      const u32x4* pq = (const u32x4*)(ws + WS_PHQ) + ((size_t)(h * 128 + (b >> 4) * 8 + wave) * 2) * 64 + lane; \
      c1 = pq[0];                                                                               \
      c2 = pq[64];                                                                              \
    }                                                                                           \
    K3_STEP(14, aA, aB, b2, b1, NT); \
    K3_STEP(15, aB, aA, b3, b2, NT); \
  } while (0)
  K3_AISSUE(0);
  K3_AISSUE(1);
  if (wn == 0) { K3_BLOAD(b0, 0, 4); } else { K3_BLOAD(b0, 0, 3); }
  K3_AISSUE(2);
  if (wn == 0) { K3_BLOAD(b1, 1, 4); } else { K3_BLOAD(b1, 1, 3); }
  K3_AISSUE(3);
  if (wn == 0) { K3_BLOAD(b2, 2, 4); } else { K3_BLOAD(b2, 2, 3); }
  K3_AISSUE(4); K3_AISSUE(5); K3_AISSUE(6); K3_AISSUE(7);
  asm volatile("s_waitcnt vmcnt(20)" ::: "memory");
  BAR();
  f32x4 part[8];
  u32x4 c1, c2;
  SCHED();
#pragma unroll
  for (int n = 0; n < 4; ++n)
#pragma unroll
    for (int m = 0; m < 4; ++m) acc[n][m] = f32x4{0.f, 0.f, 0.f, 0.f};
  if (wn == 0) K3_LOOP(4);
  else K3_LOOP(3);
  {
    char* KT = sm + K3_AUX;
    if (tid < 448) {
      const f32x4 s = ((part[0] + part[1]) + (part[2] + part[3])) + ((part[4] + part[5]) + (part[6] + part[7]));
      const int fp = tid >> 3, d0 = 4 * (tid & 7);
      const int mf = (fp >= 28) ? 1 : 0, f = fp - 28 * mf;
      const int pos = mf * 32 + ((f & 15) >> 2) * 8 + (f & 3) + 4 * (f >> 4);
      const unsigned a = pk2(s[0], s[1]), c = pk2(s[2], s[3]);
      char* d = KT + d0 * 144 + pos * 2;
      *(u16*)(d) = (u16)a; *(u16*)(d + 144) = (u16)(a >> 16);
      *(u16*)(d + 288) = (u16)c; *(u16*)(d + 432) = (u16)(c >> 16);
    } else {
      const int i = tid - 448, d = i >> 1, mf = i & 1;
      *(u32x2*)(KT + d * 144 + (mf * 32 + 28) * 2) = u32x2{0u, 0u};
    }
  }
  BAR();
  {
    const char* KT = sm + K3_AUX;
    bf16x8 af[2][2];
#pragma unroll
    for (int dt = 0; dt < 2; ++dt)
#pragma unroll
      for (int mf = 0; mf < 2; ++mf) af[dt][mf] = *(const bf16x8*)(KT + (16 * dt + r16) * 144 + (32 * mf + 8 * g) * 2);
    f32x4 o0 = {0.f, 0.f, 0.f, 0.f}, o1 = {0.f, 0.f, 0.f, 0.f};
    o0 = mfma16(af[0][0], __builtin_bit_cast(bf16x8, c1), o0);
    o1 = mfma16(af[1][0], __builtin_bit_cast(bf16x8, c1), o1);
    o0 = mfma16(af[0][1], __builtin_bit_cast(bf16x8, c2), o0);
    o1 = mfma16(af[1][1], __builtin_bit_cast(bf16x8, c2), o1);
    float den = __shfl(o1[0], 48 + r16, 64);
    den = rbf(rbf(den) + 1e-6f);
    const float inv = 1.f / den;
    const int mtile = (b >> 4) * 8 + wave;
    {
      const int k = h * 28 + 4 * g;
      u16* dst = oh + ((size_t)(mtile * 42 + (k >> 5)) * 64 + ((k >> 3) & 3) * 16 + r16) * 8 + (k & 7);
      st8_wt(dst, u32x2{pk2(rbf(o0[0]) * inv, rbf(o0[1]) * inv), pk2(rbf(o0[2]) * inv, rbf(o0[3]) * inv)});
    }
    if (g != 3) {
      const int k = h * 28 + 16 + 4 * g;
      u16* dst = oh + ((size_t)(mtile * 42 + (k >> 5)) * 64 + ((k >> 3) & 3) * 16 + r16) * 8 + (k & 7);
      st8_wt(dst, u32x2{pk2(rbf(o1[0]) * inv, rbf(o1[1]) * inv), pk2(rbf(o1[2]) * inv, rbf(o1[3]) * inv)});
    }
  }
  {
    char* rd = sm + kq * (64 * K3_RS) + r16 * K3_RS + (64 * wn + 4 * g) * 4;
#pragma unroll
    for (int n = 0; n < 4; ++n)
      if (n < 3 || wn == 0) {
#pragma unroll
        for (int m = 0; m < 4; ++m) *(f32x4*)(rd + 16 * m * K3_RS + 64 * n) = acc[n][m];
      }
  }
  BAR();
#pragma unroll
  for (int i = 0; i < 2; ++i) {
    const int id = tid + 512 * i;
    if (id < 896) {
      const int t = id / 14, c = id % 14;
      const char* p = sm + t * K3_RS + c * 32;
      f32x4 v0 = (*(const f32x4*)(p) + *(const f32x4*)(p + 64 * K3_RS)) +
                 (*(const f32x4*)(p + 128 * K3_RS) + *(const f32x4*)(p + 192 * K3_RS));
      f32x4 v1 = (*(const f32x4*)(p + 16) + *(const f32x4*)(p + 64 * K3_RS + 16)) +
                 (*(const f32x4*)(p + 128 * K3_RS + 16) + *(const f32x4*)(p + 192 * K3_RS + 16));
      u32x4 z;
#pragma unroll
      for (int j = 0; j < 4; ++j) z[j] = gelu_pk(zv[i][j]);
      u32x4 o;
      o[0] = pk2(lo_f(z[0]) * rbf(rbf(v0[0]) + bb[i]), hi_f(z[0]) * rbf(rbf(v0[1]) + bb[i]));
      o[1] = pk2(lo_f(z[1]) * rbf(rbf(v0[2]) + bb[i]), hi_f(z[1]) * rbf(rbf(v0[3]) + bb[i]));
      o[2] = pk2(lo_f(z[2]) * rbf(rbf(v1[0]) + bb[i]), hi_f(z[2]) * rbf(rbf(v1[1]) + bb[i]));
      o[3] = pk2(lo_f(z[3]) * rbf(rbf(v1[2]) + bb[i]), hi_f(z[3]) * rbf(rbf(v1[3]) + bb[i]));
      const int k = 448 + f0 + c * 8, tt = t0 + t;
      st16_wt(oh + ((size_t)((tt >> 4) * 42 + (k >> 5)) * 64 + ((k >> 3) & 3) * 16 + (tt & 15)) * 8, o);
    }
  }
}

__global__ __launch_bounds__(384) void k4_kernel(const u16* __restrict__ x, const char* __restrict__ ws,
                                                  u16* __restrict__ out) {
  __shared__ __attribute__((aligned(16))) char sm[6 * 32 * K3_RS];
  const int tid = threadIdx.x, lane = tid & 63, wave = __builtin_amdgcn_readfirstlane(tid >> 6);
  const int b = blockIdx.x;
  const int xcd = b & 7, idx = b >> 3;
  const int mt_ = xcd * 8 + (idx & 7), nt_ = idx >> 3;
  const int t0 = mt_ * 32, n0 = nt_ * 112;
  const int g = lane >> 4, r16 = lane & 15;
  const u16* ap = (const u16*)(ws + WS_OH) + ((size_t)(mt_ * 2 * 42 + wave * 7) * 64 + lane) * 8;
  const u16* bp = (const u16*)(ws + WS_WT) + ((size_t)(nt_ * 7 * 42 + wave * 7) * 64 + lane) * 8;
  bf16x8 a0[2], b0[7], a1[2], b1[7], a2[2], b2[7], a3[2], b3[7];
#define K4_LOAD(AF, BF, KS)                                                                     \
  do {                                                                                          \
    _Pragma("unroll") for (int m = 0; m < 2; ++m) AF[m] = *(const bf16x8*)(ap + (m * 42 + (KS)) * 512); \
    _Pragma("unroll") for (int n = 0; n < 7; ++n) BF[n] = *(const bf16x8*)(bp + (n * 42 + (KS)) * 512); \
  } while (0)
  K4_LOAD(a0, b0, 0);
  K4_LOAD(a1, b1, 1);
  K4_LOAD(a2, b2, 2);
  K4_LOAD(a3, b3, 3);
  u32x4 xv[2];
#pragma unroll
  for (int i = 0; i < 2; ++i) {
    int id = tid + 384 * i;
    if (id >= 448) id = tid;
    xv[i] = *(const u32x4*)(x + (size_t)(t0 + id / 14) * D_ + n0 + (id % 14) * 8);
  }
  SCHED();
  f32x4 acc[7][2];
#pragma unroll
  for (int n = 0; n < 7; ++n) { acc[n][0] = f32x4{0.f, 0.f, 0.f, 0.f}; acc[n][1] = f32x4{0.f, 0.f, 0.f, 0.f}; }
#define K4_MMA(AF, BF)                                                                          \
  do {                                                                                          \
    _Pragma("unroll") for (int n = 0; n < 7; ++n) {                                             \
      acc[n][0] = mfma16(BF[n], AF[0], acc[n][0]);                                              \
      acc[n][1] = mfma16(BF[n], AF[1], acc[n][1]);                                              \
    }                                                                                           \
    SCHED();                                                                                    \
  } while (0)
  K4_MMA(a0, b0); K4_LOAD(a0, b0, 4); SCHED();
  K4_MMA(a1, b1); K4_LOAD(a1, b1, 5); SCHED();
  K4_MMA(a2, b2); K4_LOAD(a2, b2, 6); SCHED();
  K4_MMA(a3, b3);
  K4_MMA(a0, b0);
  K4_MMA(a1, b1);
  K4_MMA(a2, b2);
  char* rd = sm + wave * (32 * K3_RS) + r16 * K3_RS + 16 * g;
#pragma unroll
  for (int n = 0; n < 7; ++n)
#pragma unroll
    for (int m = 0; m < 2; ++m) *(f32x4*)(rd + 16 * m * K3_RS + 64 * n) = acc[n][m];
  __syncthreads();
#pragma unroll
  for (int i = 0; i < 2; ++i) {
    const int id = tid + 384 * i;
    if (id < 448) {
      const int et = id / 14, ec = id % 14;
      const char* p = sm + et * K3_RS + ec * 32;
      f32x4 v0 = {0.f, 0.f, 0.f, 0.f}, v1 = {0.f, 0.f, 0.f, 0.f};
#pragma unroll
      for (int w = 0; w < 6; ++w) {
        v0 += *(const f32x4*)(p + w * 32 * K3_RS);
        v1 += *(const f32x4*)(p + w * 32 * K3_RS + 16);
      }
      u32x4 o;
      o[0] = pk2(lo_f(xv[i][0]) + v0[0], hi_f(xv[i][0]) + v0[1]);
      o[1] = pk2(lo_f(xv[i][1]) + v0[2], hi_f(xv[i][1]) + v0[3]);
      o[2] = pk2(lo_f(xv[i][2]) + v1[0], hi_f(xv[i][2]) + v1[1]);
      o[3] = pk2(lo_f(xv[i][3]) + v1[2], hi_f(xv[i][3]) + v1[3]);
      *(u32x4*)(out + (size_t)(t0 + et) * D_ + n0 + ec * 8) = o;
    }
  }
}

extern "C" void kernel_launch(void* const* d_in, const int* in_sizes, int n_in, void* d_out, int out_size,
                              void* d_ws, size_t ws_size, hipStream_t stream) {
  const u16* x = (const u16*)d_in[0];
  const u16* wqkv = (const u16*)d_in[1];
  const u16* wphi = (const u16*)d_in[2];
  const u16* wo = (const u16*)d_in[3];
  const u16* win = (const u16*)d_in[5];
  const u16* wsp = (const u16*)d_in[6];
  const u16* bsp = (const u16*)d_in[7];
  const u16* wout = (const u16*)d_in[8];
  char* ws = (char*)d_ws;
  k1_kernel<<<245, 768, 0, stream>>>(x, wqkv, wo, win, wout, wsp, ws);
  k2_kernel<<<384, 512, 0, stream>>>(wphi, wsp, ws);
  k3_kernel<<<256, 512, 0, stream>>>(wsp, bsp, wphi, ws);
  k4_kernel<<<256, 384, 0, stream>>>(x, ws, (u16*)d_out);
}
```
